```python
import torch

def _softmax(x, axis=-1):
    return torch.softmax(x, dim=axis)
def _gelu(x):
    return torch.nn.functional.gelu(x, approximate="tanh")
def _silu(x):
    return torch.nn.functional.silu(x)
def _sigmoid(x):
    return torch.sigmoid(x)
def _layernorm(x, axis=-1, eps=1e-5):
    m = x.mean(dim=axis, keepdim=True)
    v = x.var(dim=axis, keepdim=True, unbiased=False)
    return (x - m) / torch.sqrt(v + eps)
def _scatter_add(tgt, idx, src, axis=0):
    return tgt.clone().index_add_(axis, idx, src)
def _segment_sum(src, seg, n):
    out = torch.zeros((n,) + src.shape[1:], dtype=src.dtype)
    return out.index_add_(0, seg, src)
def _pad(x, pw):
    flat = tuple(v for pair in reversed(pw) for v in pair)
    return torch.nn.functional.pad(x, flat)

kernel_name = 'standalone__linear_kernel_rfa__prompt'

def setup_inputs():

    _w = (lambda a: (lambda t: t.to(torch.bfloat16) if t.is_floating_point() else t)(torch.as_tensor(a)))
    B, H, Sq, Sk, D, M = 1, 32, 2048, 2048, 192, 16
    return {
        "q": _w((lambda *s: torch.randn(*s, dtype=torch.bfloat16) * 0.1)(B, H, Sq, D)),
        "k": _w((lambda *s: torch.randn(*s, dtype=torch.bfloat16) * 1.6)(B, H, Sk, D)),
        "v": _w((lambda *s: torch.randn(*s, dtype=torch.bfloat16) * 1.6)(B, H, Sk, D)),
        "omega": _w((lambda *s: torch.randn(*s, dtype=torch.bfloat16) * 1.6)(M, D)),
    }

def reference(q, k, v, omega):
    d = q.shape[-1]
    m = omega.shape[0]
    scale = float(d) ** -0.25
    qs = q * scale
    ks = k * scale
    q_proj = torch.einsum("bhsd,md->bhsm", qs, omega)
    k_proj = torch.einsum("bhsd,md->bhsm", ks, omega)
    q_sq = 0.5 * torch.einsum("bhsd,bhsd->bhs", qs, qs)[..., None]
    k_sq = 0.5 * torch.einsum("bhsd,bhsd->bhs", ks, ks)[..., None]
    inv_sqrt_m = float(m) ** -0.5
    phi_q = torch.exp(q_proj - q_sq) * inv_sqrt_m + 1e-6
    phi_k = torch.exp(k_proj - k_sq) * inv_sqrt_m + 1e-6
    kv = torch.einsum("bhsm,bhsd->bhmd", phi_k, v)
    z  = torch.einsum("bhsm->bhm", phi_k)
    num = torch.einsum("bhqm,bhmd->bhqd", phi_q, kv)
    den = torch.einsum("bhqm,bhm->bhq", phi_q, z)[..., None] + 1e-6
    return num / den
```

```cpp
#include <hip/hip_runtime.h>
#include <hip/hip_bf16.h>
#include <stdint.h>

#define EMU 0

typedef unsigned u32;
typedef u32 u32x4 __attribute__((ext_vector_type(4)));
typedef u32 u32x2 __attribute__((ext_vector_type(2)));
typedef float f32x4 __attribute__((ext_vector_type(4)));
typedef float f32x2 __attribute__((ext_vector_type(2)));
typedef __bf16 bf16x8 __attribute__((ext_vector_type(8)));
typedef __bf16 bf16x2 __attribute__((ext_vector_type(2)));
typedef short s16x4 __attribute__((ext_vector_type(4)));

constexpr int H = 32, S = 2048, D = 192, M = 16;
constexpr int CH = 256;
constexpr int NCH = S / CH;
constexpr int NT = 13;

__device__ __forceinline__ float lo_f(u32 x) { return __uint_as_float(x << 16); }
__device__ __forceinline__ float hi_f(u32 x) { return __uint_as_float(x & 0xffff0000u); }
__device__ __forceinline__ u32 pk_bf16(float a, float b) {
  f32x2 v = {a, b};
  bf16x2 r = __builtin_convertvector(v, bf16x2);
  return __builtin_bit_cast(u32, r);
}
__device__ __forceinline__ float rbf(float a) {
  return lo_f(pk_bf16(a, 0.f));
}

__device__ __forceinline__ f32x4 mfma32(u32x4 a, u32x4 b, f32x4 c) {
  return __builtin_amdgcn_mfma_f32_16x16x32_bf16(__builtin_bit_cast(bf16x8, a), __builtin_bit_cast(bf16x8, b), c, 0, 0, 0);
}
__device__ __forceinline__ f32x4 mfma16(u32x2 a, u32x2 b, f32x4 c) {
  return __builtin_amdgcn_mfma_f32_16x16x16bf16_1k(__builtin_bit_cast(s16x4, a), __builtin_bit_cast(s16x4, b), c, 0, 0, 0);
}

__device__ __forceinline__ u32x4 scale_frag(u32x4 x, float scale, float& ss) {
  u32x4 r;
#pragma unroll
  for (int i = 0; i < 4; ++i) {
    u32 p = x[i];
    r[i] = p;
    float a = lo_f(p), b = hi_f(p);
    ss = fmaf(a, a, ss);
    ss = fmaf(b, b, ss);
  }
  return r;
}

__device__ __forceinline__ float phi_fn(float proj, float ss, float scale) {
  float t = proj * scale - 0.5f * scale * scale * ss;
  return __expf(t) * 0.25f + 1e-6f;
}

__global__ __launch_bounds__(512) void k_kv(const u32* __restrict__ K, const u32* __restrict__ V,
                                            const u32* __restrict__ Om, f32x4* __restrict__ part, float scale) {
  __shared__ f32x4 red[8][NT][64];
  const int b = blockIdx.x;
  const int h = b / NCH, j = b % NCH;
  const int w = __builtin_amdgcn_readfirstlane(threadIdx.x >> 6);
  const int l = threadIdx.x & 63, g = l >> 4, c = l & 15;
  const size_t row0 = (size_t)h * S + j * CH + w * 32;
  u32x4 kf[2][6], om[6], va[2][4];
  u32x2 vb[2][4];
#pragma unroll
  for (int u = 0; u < 2; ++u)
#pragma unroll
    for (int kk = 0; kk < 6; ++kk)
      kf[u][kk] = *(const u32x4*)(K + (row0 + 16 * u + c) * 96 + 16 * kk + 4 * g);
#pragma unroll
  for (int kk = 0; kk < 6; ++kk) om[kk] = *(const u32x4*)(Om + c * 96 + 16 * kk + 4 * g);
#pragma unroll
  for (int u = 0; u < 2; ++u)
#pragma unroll
    for (int i = 0; i < 4; ++i) {
      const u32* p = V + (row0 + 16 * u + 4 * g + i) * 96;
      va[u][i] = *(const u32x4*)(p + 4 * c);
      vb[u][i] = *(const u32x2*)(p + 64 + 2 * c);
    }
  u32x4 phiT;
#pragma unroll
  for (int u = 0; u < 2; ++u) {
    float ss = 0.f;
    f32x4 pr = {0, 0, 0, 0};
#pragma unroll
    for (int kk = 0; kk < 6; ++kk) {
      u32x4 ks = scale_frag(kf[u][kk], scale, ss);
      pr = mfma32(ks, om[kk], pr);
    }
    f32x4 sq = __builtin_amdgcn_mfma_f32_16x16x4f32(ss, 1.0f, (f32x4){0, 0, 0, 0}, 0, 0, 0);
    phiT[2 * u] = pk_bf16(phi_fn(pr[0], sq[0], scale), phi_fn(pr[1], sq[1], scale));
    phiT[2 * u + 1] = pk_bf16(phi_fn(pr[2], sq[2], scale), phi_fn(pr[3], sq[3], scale));
  }
#pragma unroll
  for (int t = 0; t < 12; ++t) {
    u32x4 bf;
    const u32 sel = (t & 1) ? 0x07060302u : 0x05040100u;
#pragma unroll
    for (int u = 0; u < 2; ++u)
#pragma unroll
      for (int ih = 0; ih < 2; ++ih) {
        u32 x0 = t < 8 ? va[u][2 * ih][t >> 1] : vb[u][2 * ih][(t - 8) >> 1];
        u32 x1 = t < 8 ? va[u][2 * ih + 1][t >> 1] : vb[u][2 * ih + 1][(t - 8) >> 1];
        bf[2 * u + ih] = __builtin_amdgcn_perm(x1, x0, sel);
      }
    red[w][t][l] = mfma32(phiT, bf, (f32x4){0, 0, 0, 0});
  }
  {
    u32x4 ones = {0x3f803f80u, 0x3f803f80u, 0x3f803f80u, 0x3f803f80u};
    red[w][12][l] = mfma32(phiT, ones, (f32x4){0, 0, 0, 0});
  }
  __syncthreads();
  for (int t = w; t < NT; t += 8) {
    f32x4 s = red[0][t][l];
#pragma unroll
    for (int ww = 1; ww < 8; ++ww) s += red[ww][t][l];
    part[((size_t)b * NT + t) * 64 + l] = s;
  }
}

__global__ __launch_bounds__(512) void k_out(const u32* __restrict__ Q, const u32* __restrict__ Om,
                                             const f32x4* __restrict__ part, u32* __restrict__ O, float scale) {
  __shared__ u32x2 kvs[NT][64];
  const int b = blockIdx.x;
  const int h = b / NCH, j = b % NCH;
  const int w = __builtin_amdgcn_readfirstlane(threadIdx.x >> 6);
  const int l = threadIdx.x & 63, g = l >> 4, c = l & 15;
  const size_t row0 = (size_t)h * S + j * CH + w * 32;
  for (int t = w; t < NT; t += 8) {
    f32x4 s = part[((size_t)(h * NCH) * NT + t) * 64 + l];
#pragma unroll
    for (int jj = 1; jj < NCH; ++jj) s += part[((size_t)(h * NCH + jj) * NT + t) * 64 + l];
    kvs[t][l] = (u32x2){pk_bf16(s[0], s[1]), pk_bf16(s[2], s[3])};
  }
  u32x4 qf[2][6], om[6];
#pragma unroll
  for (int u = 0; u < 2; ++u)
#pragma unroll
    for (int kk = 0; kk < 6; ++kk)
      qf[u][kk] = *(const u32x4*)(Q + (row0 + 16 * u + c) * 96 + 16 * kk + 4 * g);
#pragma unroll
  for (int kk = 0; kk < 6; ++kk) om[kk] = *(const u32x4*)(Om + c * 96 + 16 * kk + 4 * g);
  u32x2 phq[2];
#pragma unroll
  for (int u = 0; u < 2; ++u) {
    float ss = 0.f;
    f32x4 pr = {0, 0, 0, 0};
#pragma unroll
    for (int kk = 0; kk < 6; ++kk) {
      u32x4 qs = scale_frag(qf[u][kk], scale, ss);
      pr = mfma32(om[kk], qs, pr);
    }
    f32x4 sq = __builtin_amdgcn_mfma_f32_16x16x4f32(1.0f, ss, (f32x4){0, 0, 0, 0}, 0, 0, 0);
    phq[u][0] = pk_bf16(phi_fn(pr[0], sq[0], scale), phi_fn(pr[1], sq[0], scale));
    phq[u][1] = pk_bf16(phi_fn(pr[2], sq[0], scale), phi_fn(pr[3], sq[0], scale));
  }
  __syncthreads();
  u32x2 kvf[NT];
#pragma unroll
  for (int t = 0; t < NT; ++t) kvf[t] = kvs[t][l];
#pragma unroll
  for (int u = 0; u < 2; ++u) {
    f32x4 den = mfma16(phq[u], kvf[12], (f32x4){0, 0, 0, 0});
    f32x4 inv;
#pragma unroll
    for (int r = 0; r < 4; ++r) {
      inv[r] = 1.0f / (den[r] + 1e-6f);
    }
    f32x4 num[12];
#pragma unroll
    for (int t = 0; t < 12; ++t) num[t] = mfma16(phq[u], kvf[t], (f32x4){0, 0, 0, 0});
#pragma unroll
    for (int r = 0; r < 4; ++r) {
      u32 o[6];
#pragma unroll
      for (int t2 = 0; t2 < 6; ++t2) {
        o[t2] = pk_bf16(num[2 * t2][r] * inv[r], num[2 * t2 + 1][r] * inv[r]);
      }
      u32* p = O + (row0 + 16 * u + 4 * g + r) * 96;
      *(u32x4*)(p + 4 * c) = (u32x4){o[0], o[1], o[2], o[3]};
      *(u32x2*)(p + 64 + 2 * c) = (u32x2){o[4], o[5]};
    }
  }
}

extern "C" void kernel_launch(void* const* d_in, const int* in_sizes, int n_in, void* d_out, int out_size,
                              void* d_ws, size_t ws_size, hipStream_t stream) {
  const u32* q = (const u32*)d_in[0];
  const u32* k = (const u32*)d_in[1];
  const u32* v = (const u32*)d_in[2];
  const u32* om = (const u32*)d_in[3];
  const float scale = 0.26864248f;
  k_kv<<<H * NCH, 512, 0, stream>>>(k, v, om, (f32x4*)d_ws, scale);
  k_out<<<H * NCH, 512, 0, stream>>>(q, om, (const f32x4*)d_ws, (u32*)d_out, scale);
}
```

```cpp
#include <hip/hip_runtime.h>
#include <hip/hip_bf16.h>
#include <stdint.h>

#define LDAUX 2
#define STAUX 16

typedef unsigned u32;
typedef u32 u32x4 __attribute__((ext_vector_type(4)));
typedef u32 u32x2 __attribute__((ext_vector_type(2)));
typedef float f32x4 __attribute__((ext_vector_type(4)));
typedef float f32x2 __attribute__((ext_vector_type(2)));
typedef __bf16 bf16x8 __attribute__((ext_vector_type(8)));
typedef __bf16 bf16x2 __attribute__((ext_vector_type(2)));
typedef short s16x4 __attribute__((ext_vector_type(4)));

constexpr int H = 32, S = 2048, D = 192, M = 16;
constexpr int CH = 256;
constexpr int NCH = S / CH;
constexpr int NT = 13;
constexpr u32 TBYTES = H * S * D * 2;

__device__ __forceinline__ float lo_f(u32 x) { return __uint_as_float(x << 16); }
__device__ __forceinline__ float hi_f(u32 x) { return __uint_as_float(x & 0xffff0000u); }
__device__ __forceinline__ u32 pk_bf16(float a, float b) {
  f32x2 v = {a, b};
  bf16x2 r = __builtin_convertvector(v, bf16x2);
  return __builtin_bit_cast(u32, r);
}
__device__ __forceinline__ float rbf(float a) { return lo_f(pk_bf16(a, 0.f)); }

__device__ __forceinline__ f32x4 mfma32(u32x4 a, u32x4 b, f32x4 c) {
  return __builtin_amdgcn_mfma_f32_16x16x32_bf16(__builtin_bit_cast(bf16x8, a), __builtin_bit_cast(bf16x8, b), c, 0, 0, 0);
}
__device__ __forceinline__ f32x4 mfma16(u32x2 a, u32x2 b, f32x4 c) {
  return __builtin_amdgcn_mfma_f32_16x16x16bf16_1k(__builtin_bit_cast(s16x4, a), __builtin_bit_cast(s16x4, b), c, 0, 0, 0);
}
__device__ __forceinline__ u32x4 scale_frag(u32x4 x, float scale, float& ss) {
  u32x4 r;
#pragma unroll
  for (int i = 0; i < 4; ++i) {
    u32 p = pk_bf16(lo_f(x[i]) * scale, hi_f(x[i]) * scale);
    r[i] = p;
    float a = lo_f(p), b = hi_f(p);
    ss = fmaf(a, a, ss);
    ss = fmaf(b, b, ss);
  }
  return r;
}
__device__ __forceinline__ float phi_fn(float proj, float ss) {
  float t = rbf(rbf(proj) - 0.5f * rbf(ss));
  float e = rbf(__expf(t));
  return e * 0.25f + 1e-6f;
}

__global__ __launch_bounds__(512) void rfa_fused(const u32* __restrict__ Q, const u32* __restrict__ K, const u32* __restrict__ V,
                                                 const u32* __restrict__ Om, u32* __restrict__ O, u32* ws, float scale) {
  __shared__ f32x4 red[8][NT][64];
  __shared__ u32x2 kvs[NT][64];
  const int b = blockIdx.x;
  const int h = (b & 7) * 4 + (b >> 6), j = (b >> 3) & 7;
  const int w = __builtin_amdgcn_readfirstlane(threadIdx.x >> 6);
  const int l = threadIdx.x & 63, g = l >> 4, c = l & 15;
  const u32 row0 = h * S + j * CH + w * 32;
  auto rq = __builtin_amdgcn_make_buffer_rsrc((void*)Q, 0, TBYTES, 0x00020000);
  auto rk = __builtin_amdgcn_make_buffer_rsrc((void*)K, 0, TBYTES, 0x00020000);
  auto rv = __builtin_amdgcn_make_buffer_rsrc((void*)V, 0, TBYTES, 0x00020000);
  auto ro = __builtin_amdgcn_make_buffer_rsrc((void*)O, 0, TBYTES, 0x00020000);
  auto rw = __builtin_amdgcn_make_buffer_rsrc((void*)ws, 0, 65536 + H * NCH * NT * 1024, 0x00020000);
  const u32 epoch = __builtin_amdgcn_raw_buffer_load_b32(rw, h * 256, 0, 16);
  u32x4 kf[2][6], om[6], va[2][4], qf[2][6];
  u32x2 vb[2][4];
#pragma unroll
  for (int kk = 0; kk < 6; ++kk) om[kk] = *(const u32x4*)(Om + c * 96 + 16 * kk + 4 * g);
#pragma unroll
  for (int u = 0; u < 2; ++u)
#pragma unroll
    for (int kk = 0; kk < 6; ++kk)
      kf[u][kk] = __builtin_amdgcn_raw_buffer_load_b128(rk, (row0 + 16 * u + c) * 384 + 64 * kk + 16 * g, 0, LDAUX);
#pragma unroll
  for (int u = 0; u < 2; ++u)
#pragma unroll
    for (int i = 0; i < 4; ++i) {
      const u32 off = (row0 + 16 * u + 4 * g + i) * 384;
      va[u][i] = __builtin_amdgcn_raw_buffer_load_b128(rv, off + 16 * c, 0, LDAUX);
      vb[u][i] = __builtin_amdgcn_raw_buffer_load_b64(rv, off + 256 + 8 * c, 0, LDAUX);
    }
  u32x4 phiT;
#pragma unroll
  for (int u = 0; u < 2; ++u) {
    float ss = 0.f;
    f32x4 pr = {0, 0, 0, 0};
#pragma unroll
    for (int kk = 0; kk < 6; ++kk) {
      u32x4 ks = scale_frag(kf[u][kk], scale, ss);
      pr = mfma32(ks, om[kk], pr);
    }
    f32x4 sq = __builtin_amdgcn_mfma_f32_16x16x4f32(ss, 1.0f, (f32x4){0, 0, 0, 0}, 0, 0, 0);
    phiT[2 * u] = pk_bf16(phi_fn(pr[0], sq[0]), phi_fn(pr[1], sq[1]));
    phiT[2 * u + 1] = pk_bf16(phi_fn(pr[2], sq[2]), phi_fn(pr[3], sq[3]));
  }
#pragma unroll
  for (int t = 0; t < 12; ++t) {
    u32x4 bf;
    const u32 sel = (t & 1) ? 0x07060302u : 0x05040100u;
#pragma unroll
    for (int u = 0; u < 2; ++u)
#pragma unroll
      for (int ih = 0; ih < 2; ++ih) {
        u32 x0 = t < 8 ? va[u][2 * ih][t >> 1] : vb[u][2 * ih][(t - 8) >> 1];
        u32 x1 = t < 8 ? va[u][2 * ih + 1][t >> 1] : vb[u][2 * ih + 1][(t - 8) >> 1];
        bf[2 * u + ih] = __builtin_amdgcn_perm(x1, x0, sel);
      }
    red[w][t][l] = mfma32(phiT, bf, (f32x4){0, 0, 0, 0});
  }
  {
    u32x4 ones = {0x3f803f80u, 0x3f803f80u, 0x3f803f80u, 0x3f803f80u};
    red[w][12][l] = mfma32(phiT, ones, (f32x4){0, 0, 0, 0});
  }
#pragma unroll
  for (int u = 0; u < 2; ++u)
#pragma unroll
    for (int kk = 0; kk < 6; ++kk)
      qf[u][kk] = __builtin_amdgcn_raw_buffer_load_b128(rq, (row0 + 16 * u + c) * 384 + 64 * kk + 16 * g, 0, LDAUX);
  __syncthreads();
  const u32 tag = epoch + 0x01010101u;
  const u32x4 tagv = {tag & 0xff, (tag >> 8) & 0xff, (tag >> 16) & 0xff, tag >> 24};
  for (int t = w; t < NT; t += 8) {
    f32x4 s = red[0][t][l];
#pragma unroll
    for (int ww = 1; ww < 8; ++ww) s += red[ww][t][l];
    u32x4 x = __builtin_bit_cast(u32x4, s);
    x = ((x + 0x80u) & 0xffffff00u) | tagv;
    __builtin_amdgcn_raw_buffer_store_b128(x, rw, 65536 + (((h * NCH + j) * NT + t) * 64 + l) * 16, 0, 16);
  }
  u32x2 phq[2];
#pragma unroll
  for (int u = 0; u < 2; ++u) {
    float ss = 0.f;
    f32x4 pr = {0, 0, 0, 0};
#pragma unroll
    for (int kk = 0; kk < 6; ++kk) {
      u32x4 qs = scale_frag(qf[u][kk], scale, ss);
      pr = mfma32(om[kk], qs, pr);
    }
    f32x4 sq = __builtin_amdgcn_mfma_f32_16x16x4f32(1.0f, ss, (f32x4){0, 0, 0, 0}, 0, 0, 0);
    phq[u][0] = pk_bf16(phi_fn(pr[0], sq[0]), phi_fn(pr[1], sq[0]));
    phq[u][1] = pk_bf16(phi_fn(pr[2], sq[0]), phi_fn(pr[3], sq[0]));
  }
  for (int t = w; t < NT; t += 8) {
    u32x4 x[NCH];
    for (int spin = 0; spin < (1 << 20); ++spin) {
      bool ok = true;
#pragma unroll
      for (int jj = 0; jj < NCH; ++jj)
        x[jj] = __builtin_amdgcn_raw_buffer_load_b128(rw, 65536 + (((h * NCH + jj) * NT + t) * 64 + l) * 16, 0, 16);
#pragma unroll
      for (int jj = 0; jj < NCH; ++jj) {
        u32x4 d = (x[jj] & 0xffu) ^ tagv;
        ok = ok && ((d[0] | d[1] | d[2] | d[3]) == 0);
      }
      if (__all(ok)) break;
      __builtin_amdgcn_s_sleep(2);
      asm volatile("" ::: "memory");
    }
    f32x4 s = __builtin_bit_cast(f32x4, x[0] & 0xffffff00u);
#pragma unroll
    for (int jj = 1; jj < NCH; ++jj) s += __builtin_bit_cast(f32x4, x[jj] & 0xffffff00u);
    kvs[t][l] = (u32x2){pk_bf16(s[0], s[1]), pk_bf16(s[2], s[3])};
  }
  if (j == 0 && threadIdx.x == 0) __builtin_amdgcn_raw_buffer_store_b32(tag, rw, h * 256, 0, 16);
  __syncthreads();
  u32x2 kvf[NT];
#pragma unroll
  for (int t = 0; t < NT; ++t) kvf[t] = kvs[t][l];
#pragma unroll
  for (int u = 0; u < 2; ++u) {
    f32x4 den = mfma16(phq[u], kvf[12], (f32x4){0, 0, 0, 0});
    f32x4 inv;
#pragma unroll
    for (int r = 0; r < 4; ++r) inv[r] = __builtin_amdgcn_rcpf(rbf(rbf(den[r]) + 1e-6f));
    f32x4 num[12];
#pragma unroll
    for (int t = 0; t < 12; ++t) num[t] = mfma16(phq[u], kvf[t], (f32x4){0, 0, 0, 0});
#pragma unroll
    for (int r = 0; r < 4; ++r) {
      u32 o[6];
#pragma unroll
      for (int t2 = 0; t2 < 6; ++t2) o[t2] = pk_bf16(num[2 * t2][r] * inv[r], num[2 * t2 + 1][r] * inv[r]);
      const u32 off = (row0 + 16 * u + 4 * g + r) * 384;
      __builtin_amdgcn_raw_buffer_store_b128((u32x4){o[0], o[1], o[2], o[3]}, ro, off + 16 * c, 0, STAUX);
      __builtin_amdgcn_raw_buffer_store_b64((u32x2){o[4], o[5]}, ro, off + 256 + 8 * c, 0, STAUX);
    }
  }
}

extern "C" void kernel_launch(void* const* d_in, const int* in_sizes, int n_in, void* d_out, int out_size,
                              void* d_ws, size_t ws_size, hipStream_t stream) {
  const float scale = 0.26864248f;
  rfa_fused<<<H * NCH, 512, 0, stream>>>((const u32*)d_in[0], (const u32*)d_in[1], (const u32*)d_in[2], (const u32*)d_in[3],
                                        (u32*)d_out, (u32*)d_ws, scale);
}
```

```cpp
#include <hip/hip_runtime.h>
#include <hip/hip_bf16.h>
#include <stdint.h>

#define LDAUX 2
#define STAUX 16

typedef unsigned u32;
typedef u32 u32x4 __attribute__((ext_vector_type(4)));
typedef u32 u32x2 __attribute__((ext_vector_type(2)));
typedef float f32x4 __attribute__((ext_vector_type(4)));
typedef float f32x2 __attribute__((ext_vector_type(2)));
typedef __bf16 bf16x8 __attribute__((ext_vector_type(8)));
typedef __bf16 bf16x2 __attribute__((ext_vector_type(2)));
typedef short s16x4 __attribute__((ext_vector_type(4)));

constexpr int H = 32, S = 2048, D = 192, M = 16;
constexpr int CH = 256;
constexpr int NCH = S / CH;
constexpr int NT = 13;
constexpr u32 TBYTES = H * S * D * 2;

__device__ __forceinline__ float lo_f(u32 x) { return __uint_as_float(x << 16); }
__device__ __forceinline__ float hi_f(u32 x) { return __uint_as_float(x & 0xffff0000u); }
__device__ __forceinline__ u32 pk_bf16(float a, float b) {
  f32x2 v = {a, b};
  bf16x2 r = __builtin_convertvector(v, bf16x2);
  return __builtin_bit_cast(u32, r);
}
__device__ __forceinline__ float rbf(float a) { return lo_f(pk_bf16(a, 0.f)); }

__device__ __forceinline__ f32x4 mfma32(u32x4 a, u32x4 b, f32x4 c) {
  return __builtin_amdgcn_mfma_f32_16x16x32_bf16(__builtin_bit_cast(bf16x8, a), __builtin_bit_cast(bf16x8, b), c, 0, 0, 0);
}
__device__ __forceinline__ f32x4 mfma16(u32x2 a, u32x2 b, f32x4 c) {
  return __builtin_amdgcn_mfma_f32_16x16x16bf16_1k(__builtin_bit_cast(s16x4, a), __builtin_bit_cast(s16x4, b), c, 0, 0, 0);
}
__device__ __forceinline__ u32x4 scale_frag(u32x4 x, float scale, float& ss) {
  u32x4 r;
#pragma unroll
  for (int i = 0; i < 4; ++i) {
    u32 p = pk_bf16(lo_f(x[i]) * scale, hi_f(x[i]) * scale);
    r[i] = p;
    float a = lo_f(p), b = hi_f(p);
    ss = fmaf(a, a, ss);
    ss = fmaf(b, b, ss);
  }
  return r;
}
__device__ __forceinline__ float phi_fn(float proj, float ss) {
  float t = rbf(rbf(proj) - 0.5f * rbf(ss));
  float e = rbf(__expf(t));
  return e * 0.25f + 1e-6f;
}

typedef unsigned long long u64;
__device__ __forceinline__ u64 rt() { u64 t; __builtin_amdgcn_sched_barrier(0); asm volatile("s_memrealtime %0\n\ts_waitcnt lgkmcnt(0)" : "=s"(t) :: "memory"); __builtin_amdgcn_sched_barrier(0); return t; }
#define SEL 0
#define STAMP(i) if (SEL == i || SEL == -i) tx = rt();
__global__ __launch_bounds__(512) void rfa_fused(const u32* __restrict__ Q, const u32* __restrict__ K, const u32* __restrict__ V,
                                                 const u32* __restrict__ Om, u32* __restrict__ O, u32* ws, float scale) {
  const u64 t0 = rt();
  u64 tx = t0;
  __shared__ f32x4 red[8][NT][64];
  __shared__ u32x2 kvs[NT][64];
  const int b = blockIdx.x;
  const int h = (b & 7) * 4 + (b >> 6), j = (b >> 3) & 7;
  const int w = __builtin_amdgcn_readfirstlane(threadIdx.x >> 6);
  const int l = threadIdx.x & 63, g = l >> 4, c = l & 15;
  const u32 row0 = h * S + j * CH + w * 32;
  auto rq = __builtin_amdgcn_make_buffer_rsrc((void*)Q, 0, TBYTES, 0x00020000);
  auto rk = __builtin_amdgcn_make_buffer_rsrc((void*)K, 0, TBYTES, 0x00020000);
  auto rv = __builtin_amdgcn_make_buffer_rsrc((void*)V, 0, TBYTES, 0x00020000);
  auto ro = __builtin_amdgcn_make_buffer_rsrc((void*)O, 0, TBYTES, 0x00020000);
  auto rw = __builtin_amdgcn_make_buffer_rsrc((void*)ws, 0, 65536 + H * NCH * NT * 1024, 0x00020000);
  const u32 epoch = __builtin_amdgcn_raw_buffer_load_b32(rw, h * 256, 0, 16);
  u32x4 kf[2][6], om[6], va[2][4], qf[2][6];
  u32x2 vb[2][4];
#pragma unroll
  for (int kk = 0; kk < 6; ++kk) om[kk] = *(const u32x4*)(Om + c * 96 + 16 * kk + 4 * g);
#pragma unroll
  for (int u = 0; u < 2; ++u)
#pragma unroll
    for (int kk = 0; kk < 6; ++kk)
      kf[u][kk] = __builtin_amdgcn_raw_buffer_load_b128(rk, (row0 + 16 * u + c) * 384 + 64 * kk + 16 * g, 0, LDAUX);
#pragma unroll
  for (int u = 0; u < 2; ++u)
#pragma unroll
    for (int i = 0; i < 4; ++i) {
      const u32 off = (row0 + 16 * u + 4 * g + i) * 384;
      va[u][i] = __builtin_amdgcn_raw_buffer_load_b128(rv, off + 16 * c, 0, LDAUX);
      vb[u][i] = __builtin_amdgcn_raw_buffer_load_b64(rv, off + 256 + 8 * c, 0, LDAUX);
    }
  STAMP(1)
  u32x4 phiT;
#pragma unroll
  for (int u = 0; u < 2; ++u) {
    float ss = 0.f;
    f32x4 pr = {0, 0, 0, 0};
#pragma unroll
    for (int kk = 0; kk < 6; ++kk) {
      u32x4 ks = scale_frag(kf[u][kk], scale, ss);
      pr = mfma32(ks, om[kk], pr);
    }
    f32x4 sq = __builtin_amdgcn_mfma_f32_16x16x4f32(ss, 1.0f, (f32x4){0, 0, 0, 0}, 0, 0, 0);
    phiT[2 * u] = pk_bf16(phi_fn(pr[0], sq[0]), phi_fn(pr[1], sq[1]));
    phiT[2 * u + 1] = pk_bf16(phi_fn(pr[2], sq[2]), phi_fn(pr[3], sq[3]));
  }
  asm volatile("" : "+v"(phiT));
  STAMP(2)
#pragma unroll
  for (int t = 0; t < 12; ++t) {
    u32x4 bf;
    const u32 sel = (t & 1) ? 0x07060302u : 0x05040100u;
#pragma unroll
    for (int u = 0; u < 2; ++u)
#pragma unroll
      for (int ih = 0; ih < 2; ++ih) {
        u32 x0 = t < 8 ? va[u][2 * ih][t >> 1] : vb[u][2 * ih][(t - 8) >> 1];
        u32 x1 = t < 8 ? va[u][2 * ih + 1][t >> 1] : vb[u][2 * ih + 1][(t - 8) >> 1];
        bf[2 * u + ih] = __builtin_amdgcn_perm(x1, x0, sel);
      }
    red[w][t][l] = mfma32(phiT, bf, (f32x4){0, 0, 0, 0});
  }
  {
    u32x4 ones = {0x3f803f80u, 0x3f803f80u, 0x3f803f80u, 0x3f803f80u};
    red[w][12][l] = mfma32(phiT, ones, (f32x4){0, 0, 0, 0});
  }
  STAMP(3)
#pragma unroll
  for (int u = 0; u < 2; ++u)
#pragma unroll
    for (int kk = 0; kk < 6; ++kk)
      qf[u][kk] = __builtin_amdgcn_raw_buffer_load_b128(rq, (row0 + 16 * u + c) * 384 + 64 * kk + 16 * g, 0, LDAUX);
  __syncthreads();
  STAMP(4)
  const u32 tag = epoch + 0x01010101u;
  const u32x4 tagv = {tag & 0xff, (tag >> 8) & 0xff, (tag >> 16) & 0xff, tag >> 24};
  for (int t = w; t < NT; t += 8) {
    f32x4 s = red[0][t][l];
#pragma unroll
    for (int ww = 1; ww < 8; ++ww) s += red[ww][t][l];
    u32x4 x = __builtin_bit_cast(u32x4, s);
    x = ((x + 0x80u) & 0xffffff00u) | tagv;
    __builtin_amdgcn_raw_buffer_store_b128(x, rw, 65536 + (((h * NCH + j) * NT + t) * 64 + l) * 16, 0, 16);
  }
  u32x2 phq[2];
#pragma unroll
  for (int u = 0; u < 2; ++u) {
    float ss = 0.f;
    f32x4 pr = {0, 0, 0, 0};
#pragma unroll
    for (int kk = 0; kk < 6; ++kk) {
      u32x4 qs = scale_frag(qf[u][kk], scale, ss);
      pr = mfma32(om[kk], qs, pr);
    }
    f32x4 sq = __builtin_amdgcn_mfma_f32_16x16x4f32(1.0f, ss, (f32x4){0, 0, 0, 0}, 0, 0, 0);
    phq[u][0] = pk_bf16(phi_fn(pr[0], sq[0]), phi_fn(pr[1], sq[0]));
    phq[u][1] = pk_bf16(phi_fn(pr[2], sq[0]), phi_fn(pr[3], sq[0]));
  }
  asm volatile("" : "+v"(phq[0]), "+v"(phq[1]));
  STAMP(5)
  for (int t = w; t < NT; t += 8) {
    u32x4 x[NCH];
    for (int spin = 0; spin < (1 << 20); ++spin) {
      bool ok = true;
#pragma unroll
      for (int jj = 0; jj < NCH; ++jj)
        x[jj] = __builtin_amdgcn_raw_buffer_load_b128(rw, 65536 + (((h * NCH + jj) * NT + t) * 64 + l) * 16, 0, 16);
#pragma unroll
      for (int jj = 0; jj < NCH; ++jj) {
        u32x4 d = (x[jj] & 0xffu) ^ tagv;
        ok = ok && ((d[0] | d[1] | d[2] | d[3]) == 0);
      }
      if (__all(ok)) break;
      __builtin_amdgcn_s_sleep(2);
      asm volatile("" ::: "memory");
    }
    f32x4 s = __builtin_bit_cast(f32x4, x[0] & 0xffffff00u);
#pragma unroll
    for (int jj = 1; jj < NCH; ++jj) s += __builtin_bit_cast(f32x4, x[jj] & 0xffffff00u);
    kvs[t][l] = (u32x2){pk_bf16(s[0], s[1]), pk_bf16(s[2], s[3])};
  }
  if (j == 0 && threadIdx.x == 0) __builtin_amdgcn_raw_buffer_store_b32(tag, rw, h * 256, 0, 16);
  __syncthreads();
  STAMP(6)
  u32x2 kvf[NT];
#pragma unroll
  for (int t = 0; t < NT; ++t) kvf[t] = kvs[t][l];
#pragma unroll
  for (int u = 0; u < 2; ++u) {
    f32x4 den = mfma16(phq[u], kvf[12], (f32x4){0, 0, 0, 0});
    f32x4 inv;
#pragma unroll
    for (int r = 0; r < 4; ++r) inv[r] = __builtin_amdgcn_rcpf(rbf(rbf(den[r]) + 1e-6f));
    f32x4 num[12];
#pragma unroll
    for (int t = 0; t < 12; ++t) num[t] = mfma16(phq[u], kvf[t], (f32x4){0, 0, 0, 0});
#pragma unroll
    for (int r = 0; r < 4; ++r) {
      u32 o[6];
#pragma unroll
      for (int t2 = 0; t2 < 6; ++t2) o[t2] = pk_bf16(num[2 * t2][r] * inv[r], num[2 * t2 + 1][r] * inv[r]);
      const u32 off = (row0 + 16 * u + 4 * g + r) * 384;
      __builtin_amdgcn_raw_buffer_store_b128((u32x4){o[0], o[1], o[2], o[3]}, ro, off + 16 * c, 0, STAUX);
      __builtin_amdgcn_raw_buffer_store_b64((u32x2){o[4], o[5]}, ro, off + 256 + 8 * c, 0, STAUX);
    }
  }
  STAMP(7)
  asm volatile("s_waitcnt vmcnt(0)" ::: "memory");
  STAMP(8)
  {
    const u64 target = SEL >= 0 ? t0 + 6000 + (tx - t0) : t0 + 10000 - (tx - t0);
    while (rt() < target) __builtin_amdgcn_s_sleep(1);
  }
}

extern "C" void kernel_launch(void* const* d_in, const int* in_sizes, int n_in, void* d_out, int out_size,
                              void* d_ws, size_t ws_size, hipStream_t stream) {
  const float scale = 0.26864248f;
  rfa_fused<<<H * NCH, 512, 0, stream>>>((const u32*)d_in[0], (const u32*)d_in[1], (const u32*)d_in[2], (const u32*)d_in[3],
                                        (u32*)d_out, (u32*)d_ws, scale);
}
```

```cpp
#include <hip/hip_runtime.h>
#include <hip/hip_bf16.h>
#include <stdint.h>

#define LDAUX 2
#define STAUX 16

typedef unsigned u32;
typedef u32 u32x4 __attribute__((ext_vector_type(4)));
typedef u32 u32x2 __attribute__((ext_vector_type(2)));
typedef float f32x4 __attribute__((ext_vector_type(4)));
typedef float f32x2 __attribute__((ext_vector_type(2)));
typedef __bf16 bf16x8 __attribute__((ext_vector_type(8)));
typedef __bf16 bf16x2 __attribute__((ext_vector_type(2)));
typedef short s16x4 __attribute__((ext_vector_type(4)));

constexpr int H = 32, S = 2048, D = 192, M = 16;
constexpr int CH = 256;
constexpr int NCH = S / CH;
constexpr int NT = 13;
constexpr u32 TBYTES = H * S * D * 2;

__device__ __forceinline__ float lo_f(u32 x) { return __uint_as_float(x << 16); }
__device__ __forceinline__ float hi_f(u32 x) { return __uint_as_float(x & 0xffff0000u); }
__device__ __forceinline__ u32 pk_bf16(float a, float b) {
  f32x2 v = {a, b};
  bf16x2 r = __builtin_convertvector(v, bf16x2);
  return __builtin_bit_cast(u32, r);
}
__device__ __forceinline__ float rbf(float a) { return lo_f(pk_bf16(a, 0.f)); }

__device__ __forceinline__ f32x4 mfma32(u32x4 a, u32x4 b, f32x4 c) {
  return __builtin_amdgcn_mfma_f32_16x16x32_bf16(__builtin_bit_cast(bf16x8, a), __builtin_bit_cast(bf16x8, b), c, 0, 0, 0);
}
__device__ __forceinline__ f32x4 mfma16(u32x2 a, u32x2 b, f32x4 c) {
  return __builtin_amdgcn_mfma_f32_16x16x16bf16_1k(__builtin_bit_cast(s16x4, a), __builtin_bit_cast(s16x4, b), c, 0, 0, 0);
}
__device__ __forceinline__ u32x4 scale_frag(u32x4 x, float scale, float& ss) {
  u32x4 r;
#pragma unroll
  for (int i = 0; i < 4; ++i) {
    u32 p = pk_bf16(lo_f(x[i]) * scale, hi_f(x[i]) * scale);
    r[i] = p;
    float a = lo_f(p), b = hi_f(p);
    ss = fmaf(a, a, ss);
    ss = fmaf(b, b, ss);
  }
  return r;
}
__device__ __forceinline__ float phi_fn(float proj, float ss) {
  float t = rbf(rbf(proj) - 0.5f * rbf(ss));
  float e = rbf(__expf(t));
  return e * 0.25f + 1e-6f;
}

typedef unsigned long long u64;
__device__ __forceinline__ u64 rt() { u64 t; __builtin_amdgcn_sched_barrier(0); asm volatile("s_memrealtime %0\n\ts_waitcnt lgkmcnt(0)" : "=s"(t) :: "memory"); __builtin_amdgcn_sched_barrier(0); return t; }
#define SEL 0
#define STAMP(i) if (SEL == i || SEL == -i) tx = rt();
__global__ __launch_bounds__(512) void rfa_fused(const u32* __restrict__ Q, const u32* __restrict__ K, const u32* __restrict__ V,
                                                 const u32* __restrict__ Om, u32* __restrict__ O, u32* ws, float scale) {
  const u64 t0 = rt();
  u64 tx = t0;
  __shared__ f32x4 red[8][NT][64];
  __shared__ u32x2 kvs[NT][64];
  const int b = blockIdx.x;
  const int h = (b & 7) * 4 + (b >> 6), j = (b >> 3) & 7;
  const int w = __builtin_amdgcn_readfirstlane(threadIdx.x >> 6);
  const int l = threadIdx.x & 63, g = l >> 4, c = l & 15;
  const u32 row0 = h * S + j * CH + w * 32;
  auto rq = __builtin_amdgcn_make_buffer_rsrc((void*)Q, 0, TBYTES, 0x00020000);
  auto rk = __builtin_amdgcn_make_buffer_rsrc((void*)K, 0, TBYTES, 0x00020000);
  auto rv = __builtin_amdgcn_make_buffer_rsrc((void*)V, 0, TBYTES, 0x00020000);
  auto ro = __builtin_amdgcn_make_buffer_rsrc((void*)O, 0, TBYTES, 0x00020000);
  auto rw = __builtin_amdgcn_make_buffer_rsrc((void*)ws, 0, 65536 + H * NCH * NT * 1024, 0x00020000);
  const u32 epoch = __builtin_amdgcn_raw_buffer_load_b32(rw, h * 256, 0, 16);
  u32x4 kf[2][6], om[6], va[2][4], qf[2][6];
  u32x2 vb[2][4];
#pragma unroll
  for (int kk = 0; kk < 6; ++kk) om[kk] = *(const u32x4*)(Om + c * 96 + 16 * kk + 4 * g);
#pragma unroll
  for (int u = 0; u < 2; ++u)
#pragma unroll
    for (int kk = 0; kk < 6; ++kk)
      kf[u][kk] = __builtin_amdgcn_raw_buffer_load_b128(rk, (row0 + 16 * u + c) * 384 + 64 * kk + 16 * g, 0, LDAUX);
#pragma unroll
  for (int u = 0; u < 2; ++u)
#pragma unroll
    for (int i = 0; i < 4; ++i) {
      const u32 off = (row0 + 16 * u + 4 * g + i) * 384;
      va[u][i] = __builtin_amdgcn_raw_buffer_load_b128(rv, off + 16 * c, 0, LDAUX);
      vb[u][i] = __builtin_amdgcn_raw_buffer_load_b64(rv, off + 256 + 8 * c, 0, LDAUX);
    }
  STAMP(1)
  u32x4 phiT;
#pragma unroll
  for (int u = 0; u < 2; ++u) {
    float ss = 0.f;
    f32x4 pr = {0, 0, 0, 0};
#pragma unroll
    for (int kk = 0; kk < 6; ++kk) {
      u32x4 ks = scale_frag(kf[u][kk], scale, ss);
      pr = mfma32(ks, om[kk], pr);
    }
    f32x4 sq = __builtin_amdgcn_mfma_f32_16x16x4f32(ss, 1.0f, (f32x4){0, 0, 0, 0}, 0, 0, 0);
    phiT[2 * u] = pk_bf16(phi_fn(pr[0], sq[0]), phi_fn(pr[1], sq[1]));
    phiT[2 * u + 1] = pk_bf16(phi_fn(pr[2], sq[2]), phi_fn(pr[3], sq[3]));
  }
  asm volatile("" : "+v"(phiT));
  STAMP(2)
#pragma unroll
  for (int t = 0; t < 12; ++t) {
    u32x4 bf;
    const u32 sel = (t & 1) ? 0x07060302u : 0x05040100u;
#pragma unroll
    for (int u = 0; u < 2; ++u)
#pragma unroll
      for (int ih = 0; ih < 2; ++ih) {
        u32 x0 = t < 8 ? va[u][2 * ih][t >> 1] : vb[u][2 * ih][(t - 8) >> 1];
        u32 x1 = t < 8 ? va[u][2 * ih + 1][t >> 1] : vb[u][2 * ih + 1][(t - 8) >> 1];
        bf[2 * u + ih] = __builtin_amdgcn_perm(x1, x0, sel);
      }
    red[w][t][l] = mfma32(phiT, bf, (f32x4){0, 0, 0, 0});
  }
  {
    u32x4 ones = {0x3f803f80u, 0x3f803f80u, 0x3f803f80u, 0x3f803f80u};
    red[w][12][l] = mfma32(phiT, ones, (f32x4){0, 0, 0, 0});
  }
  STAMP(3)
#pragma unroll
  for (int u = 0; u < 2; ++u)
#pragma unroll
    for (int kk = 0; kk < 6; ++kk)
      qf[u][kk] = __builtin_amdgcn_raw_buffer_load_b128(rq, (row0 + 16 * u + c) * 384 + 64 * kk + 16 * g, 0, LDAUX);
  __syncthreads();
  STAMP(4)
  const u32 tag = epoch + 0x01010101u;
  const u32x4 tagv = {tag & 0xff, (tag >> 8) & 0xff, (tag >> 16) & 0xff, tag >> 24};
  for (int t = w; t < NT; t += 8) {
    f32x4 s = red[0][t][l];
#pragma unroll
    for (int ww = 1; ww < 8; ++ww) s += red[ww][t][l];
    u32x4 x = __builtin_bit_cast(u32x4, s);
    x = ((x + 0x80u) & 0xffffff00u) | tagv;
    __builtin_amdgcn_raw_buffer_store_b128(x, rw, 65536 + (((h * NCH + j) * NT + t) * 64 + l) * 16, 0, 16);
  }
  u32x2 phq[2];
#pragma unroll
  for (int u = 0; u < 2; ++u) {
    float ss = 0.f;
    f32x4 pr = {0, 0, 0, 0};
#pragma unroll
    for (int kk = 0; kk < 6; ++kk) {
      u32x4 qs = scale_frag(qf[u][kk], scale, ss);
      pr = mfma32(om[kk], qs, pr);
    }
    f32x4 sq = __builtin_amdgcn_mfma_f32_16x16x4f32(1.0f, ss, (f32x4){0, 0, 0, 0}, 0, 0, 0);
    phq[u][0] = pk_bf16(phi_fn(pr[0], sq[0]), phi_fn(pr[1], sq[0]));
    phq[u][1] = pk_bf16(phi_fn(pr[2], sq[0]), phi_fn(pr[3], sq[0]));
  }
  asm volatile("" : "+v"(phq[0]), "+v"(phq[1]));
  STAMP(5)
  for (int t = w; t < NT; t += 8) {
    u32x4 x[NCH];
    for (int spin = 0; spin < (1 << 20); ++spin) {
      bool ok = true;
#pragma unroll
      for (int jj = 0; jj < NCH; ++jj)
        x[jj] = __builtin_amdgcn_raw_buffer_load_b128(rw, 65536 + (((h * NCH + jj) * NT + t) * 64 + l) * 16, 0, 16);
#pragma unroll
      for (int jj = 0; jj < NCH; ++jj) {
        u32x4 d = (x[jj] & 0xffu) ^ tagv;
        ok = ok && ((d[0] | d[1] | d[2] | d[3]) == 0);
      }
      if (__all(ok)) break;
      __builtin_amdgcn_s_sleep(2);
      asm volatile("" ::: "memory");
    }
    f32x4 s = __builtin_bit_cast(f32x4, x[0] & 0xffffff00u);
#pragma unroll
    for (int jj = 1; jj < NCH; ++jj) s += __builtin_bit_cast(f32x4, x[jj] & 0xffffff00u);
    kvs[t][l] = (u32x2){pk_bf16(s[0], s[1]), pk_bf16(s[2], s[3])};
  }
  if (j == 0 && threadIdx.x == 0) __builtin_amdgcn_raw_buffer_store_b32(tag, rw, h * 256, 0, 16);
  __syncthreads();
  STAMP(6)
  u32x2 kvf[NT];
#pragma unroll
  for (int t = 0; t < NT; ++t) kvf[t] = kvs[t][l];
#pragma unroll
  for (int u = 0; u < 2; ++u) {
    f32x4 den = mfma16(phq[u], kvf[12], (f32x4){0, 0, 0, 0});
    f32x4 inv;
#pragma unroll
    for (int r = 0; r < 4; ++r) inv[r] = __builtin_amdgcn_rcpf(rbf(rbf(den[r]) + 1e-6f));
    f32x4 num[12];
#pragma unroll
    for (int t = 0; t < 12; ++t) num[t] = mfma16(phq[u], kvf[t], (f32x4){0, 0, 0, 0});
#pragma unroll
    for (int r = 0; r < 4; ++r) {
      u32 o[6];
#pragma unroll
      for (int t2 = 0; t2 < 6; ++t2) o[t2] = pk_bf16(num[2 * t2][r] * inv[r], num[2 * t2 + 1][r] * inv[r]);
      const u32 off = (row0 + 16 * u + 4 * g + r) * 384;
      __builtin_amdgcn_raw_buffer_store_b128((u32x4){o[0], o[1], o[2], o[3]}, ro, off + 16 * c, 0, STAUX);
      __builtin_amdgcn_raw_buffer_store_b64((u32x2){o[4], o[5]}, ro, off + 256 + 8 * c, 0, STAUX);
    }
  }
  STAMP(7)
  asm volatile("s_waitcnt vmcnt(0)" ::: "memory");
  STAMP(8)
  {
    const u64 target = SEL >= 0 ? t0 + 6000 + (tx - t0) : t0 + 10000 - (tx - t0);
    if (blockIdx.x == 0 && threadIdx.x == 0) *(volatile u64*)(ws + 8192) = t0;
 if (blockIdx.x == 100000) while (rt() < target) __builtin_amdgcn_s_sleep(1);
  }
}

__global__ void spink(u64* p) { u64 tb = rt(); u64 t0 = *(volatile u64*)p; while (rt() < t0 + 6000 && rt() < tb + 20000) __builtin_amdgcn_s_sleep(1); }
extern "C" void kernel_launch(void* const* d_in, const int* in_sizes, int n_in, void* d_out, int out_size,
                              void* d_ws, size_t ws_size, hipStream_t stream) {
  const float scale = 0.26864248f;
  rfa_fused<<<H * NCH, 512, 0, stream>>>((const u32*)d_in[0], (const u32*)d_in[1], (const u32*)d_in[2], (const u32*)d_in[3],
                                        (u32*)d_out, (u32*)d_ws, scale);
  spink<<<1, 64, 0, stream>>>((u64*)((u32*)d_ws + 8192));
}
```

```cpp
#include <hip/hip_runtime.h>
#include <hip/hip_bf16.h>
#include <stdint.h>

#define LDAUX 2
#define STAUX 16

typedef unsigned u32;
typedef u32 u32x4 __attribute__((ext_vector_type(4)));
typedef u32 u32x2 __attribute__((ext_vector_type(2)));
typedef float f32x4 __attribute__((ext_vector_type(4)));
typedef float f32x2 __attribute__((ext_vector_type(2)));
typedef __bf16 bf16x8 __attribute__((ext_vector_type(8)));
typedef __bf16 bf16x2 __attribute__((ext_vector_type(2)));
typedef short s16x4 __attribute__((ext_vector_type(4)));

constexpr int H = 32, S = 2048, D = 192, M = 16;
constexpr int CH = 256;
constexpr int NCH = S / CH;
constexpr int NT = 13;
constexpr u32 TBYTES = H * S * D * 2;

__device__ __forceinline__ float lo_f(u32 x) { return __uint_as_float(x << 16); }
__device__ __forceinline__ float hi_f(u32 x) { return __uint_as_float(x & 0xffff0000u); }
__device__ __forceinline__ u32 pk_bf16(float a, float b) {
  f32x2 v = {a, b};
  bf16x2 r = __builtin_convertvector(v, bf16x2);
  return __builtin_bit_cast(u32, r);
}
__device__ __forceinline__ float rbf(float a) { return lo_f(pk_bf16(a, 0.f)); }

__device__ __forceinline__ f32x4 mfma32(u32x4 a, u32x4 b, f32x4 c) {
  return __builtin_amdgcn_mfma_f32_16x16x32_bf16(__builtin_bit_cast(bf16x8, a), __builtin_bit_cast(bf16x8, b), c, 0, 0, 0);
}
__device__ __forceinline__ f32x4 mfma16(u32x2 a, u32x2 b, f32x4 c) {
  return __builtin_amdgcn_mfma_f32_16x16x16bf16_1k(__builtin_bit_cast(s16x4, a), __builtin_bit_cast(s16x4, b), c, 0, 0, 0);
}
__device__ __forceinline__ u32x4 scale_frag(u32x4 x, float scale, float& ss) {
  u32x4 r;
#pragma unroll
  for (int i = 0; i < 4; ++i) {
    u32 p = pk_bf16(lo_f(x[i]) * scale, hi_f(x[i]) * scale);
    r[i] = p;
    float a = lo_f(p), b = hi_f(p);
    ss = fmaf(a, a, ss);
    ss = fmaf(b, b, ss);
  }
  return r;
}
__device__ __forceinline__ float phi_fn(float proj, float ss) {
  float t = rbf(rbf(proj) - 0.5f * rbf(ss));
  float e = rbf(__expf(t));
  return e * 0.25f + 1e-6f;
}

__global__ __launch_bounds__(512) void rfa_fused(const u32* __restrict__ Q, const u32* __restrict__ K, const u32* __restrict__ V,
                                                 const u32* __restrict__ Om, u32* __restrict__ O, u32* ws, float scale) {
  __shared__ f32x4 red[8][NT][64];
  __shared__ u32x2 kvs[NT][64];
  const int b = blockIdx.x;
  const int h = (b & 7) * 4 + (b >> 6), j = (b >> 3) & 7;
  const int w = __builtin_amdgcn_readfirstlane(threadIdx.x >> 6);
  const int l = threadIdx.x & 63, g = l >> 4, c = l & 15;
  const u32 row0 = h * S + j * CH + w * 32;
  auto rq = __builtin_amdgcn_make_buffer_rsrc((void*)Q, 0, TBYTES, 0x00020000);
  auto rk = __builtin_amdgcn_make_buffer_rsrc((void*)K, 0, TBYTES, 0x00020000);
  auto rv = __builtin_amdgcn_make_buffer_rsrc((void*)V, 0, TBYTES, 0x00020000);
  auto ro = __builtin_amdgcn_make_buffer_rsrc((void*)O, 0, TBYTES, 0x00020000);
  auto rw = __builtin_amdgcn_make_buffer_rsrc((void*)ws, 0, 65536 + H * NCH * NT * 1024, 0x00020000);
  const u32 epoch = __builtin_amdgcn_raw_buffer_load_b32(rw, h * 256, 0, 16);
  u32x4 kf[2][6], om[6], va[2][4], qf[2][6];
  u32x2 vb[2][4];
#pragma unroll
  for (int kk = 0; kk < 6; ++kk) om[kk] = *(const u32x4*)(Om + c * 96 + 16 * kk + 4 * g);
#pragma unroll
  for (int u = 0; u < 2; ++u)
#pragma unroll
    for (int kk = 0; kk < 6; ++kk)
      kf[u][kk] = __builtin_amdgcn_raw_buffer_load_b128(rk, (row0 + 16 * u + c) * 384 + 64 * kk + 16 * g, 0, LDAUX);
#pragma unroll
  for (int u = 0; u < 2; ++u)
#pragma unroll
    for (int i = 0; i < 4; ++i) {
      const u32 off = (row0 + 16 * u + 4 * g + i) * 384;
      va[u][i] = __builtin_amdgcn_raw_buffer_load_b128(rv, off + 16 * c, 0, LDAUX);
      vb[u][i] = __builtin_amdgcn_raw_buffer_load_b64(rv, off + 256 + 8 * c, 0, LDAUX);
    }
  u32x4 phiT;
#pragma unroll
  for (int u = 0; u < 2; ++u) {
    float ss = 0.f;
    f32x4 pr = {0, 0, 0, 0};
#pragma unroll
    for (int kk = 0; kk < 6; ++kk) {
      u32x4 ks = scale_frag(kf[u][kk], scale, ss);
      pr = mfma32(ks, om[kk], pr);
    }
    f32x4 sq = __builtin_amdgcn_mfma_f32_16x16x4f32(ss, 1.0f, (f32x4){0, 0, 0, 0}, 0, 0, 0);
    phiT[2 * u] = pk_bf16(phi_fn(pr[0], sq[0]), phi_fn(pr[1], sq[1]));
    phiT[2 * u + 1] = pk_bf16(phi_fn(pr[2], sq[2]), phi_fn(pr[3], sq[3]));
  }
#pragma unroll
  for (int t = 0; t < 12; ++t) {
    u32x4 bf;
    const u32 sel = (t & 1) ? 0x07060302u : 0x05040100u;
#pragma unroll
    for (int u = 0; u < 2; ++u)
#pragma unroll
      for (int ih = 0; ih < 2; ++ih) {
        u32 x0 = t < 8 ? va[u][2 * ih][t >> 1] : vb[u][2 * ih][(t - 8) >> 1];
        u32 x1 = t < 8 ? va[u][2 * ih + 1][t >> 1] : vb[u][2 * ih + 1][(t - 8) >> 1];
        bf[2 * u + ih] = __builtin_amdgcn_perm(x1, x0, sel);
      }
    red[w][t][l] = mfma32(phiT, bf, (f32x4){0, 0, 0, 0});
  }
  {
    u32x4 ones = {0x3f803f80u, 0x3f803f80u, 0x3f803f80u, 0x3f803f80u};
    red[w][12][l] = mfma32(phiT, ones, (f32x4){0, 0, 0, 0});
  }
#pragma unroll
  for (int u = 0; u < 2; ++u)
#pragma unroll
    for (int kk = 0; kk < 6; ++kk)
      qf[u][kk] = __builtin_amdgcn_raw_buffer_load_b128(rq, (row0 + 16 * u + c) * 384 + 64 * kk + 16 * g, 0, LDAUX);
  __syncthreads();
  const u32 tag = epoch + 0x01010101u;
  const u32x4 tagv = {tag & 0xff, (tag >> 8) & 0xff, (tag >> 16) & 0xff, tag >> 24};
  for (int t = w; t < NT; t += 8) {
    f32x4 s = red[0][t][l];
#pragma unroll
    for (int ww = 1; ww < 8; ++ww) s += red[ww][t][l];
    u32x4 x = __builtin_bit_cast(u32x4, s);
    x = ((x + 0x80u) & 0xffffff00u) | tagv;
    __builtin_amdgcn_raw_buffer_store_b128(x, rw, 65536 + (((h * NCH + j) * NT + t) * 64 + l) * 16, 0, 16);
  }
  u32x2 phq[2];
#pragma unroll
  for (int u = 0; u < 2; ++u) {
    float ss = 0.f;
    f32x4 pr = {0, 0, 0, 0};
#pragma unroll
    for (int kk = 0; kk < 6; ++kk) {
      u32x4 qs = scale_frag(qf[u][kk], scale, ss);
      pr = mfma32(om[kk], qs, pr);
    }
    f32x4 sq = __builtin_amdgcn_mfma_f32_16x16x4f32(1.0f, ss, (f32x4){0, 0, 0, 0}, 0, 0, 0);
    phq[u][0] = pk_bf16(phi_fn(pr[0], sq[0]), phi_fn(pr[1], sq[0]));
    phq[u][1] = pk_bf16(phi_fn(pr[2], sq[0]), phi_fn(pr[3], sq[0]));
  }
  for (int t = w; t < NT; t += 8) {
    u32x4 x[NCH];
    for (int spin = 0; spin < (1 << 20); ++spin) {
      bool ok = true;
#pragma unroll
      for (int jj = 0; jj < NCH; ++jj)
        x[jj] = __builtin_amdgcn_raw_buffer_load_b128(rw, 65536 + (((h * NCH + jj) * NT + t) * 64 + l) * 16, 0, 16);
#pragma unroll
      for (int jj = 0; jj < NCH; ++jj) {
        u32x4 d = (x[jj] & 0xffu) ^ tagv;
        ok = ok && ((d[0] | d[1] | d[2] | d[3]) == 0);
      }
      if (__all(ok)) break;
      __builtin_amdgcn_s_sleep(2);
      asm volatile("" ::: "memory");
    }
    f32x4 s = __builtin_bit_cast(f32x4, x[0] & 0xffffff00u);
#pragma unroll
    for (int jj = 1; jj < NCH; ++jj) s += __builtin_bit_cast(f32x4, x[jj] & 0xffffff00u);
    kvs[t][l] = (u32x2){pk_bf16(s[0], s[1]), pk_bf16(s[2], s[3])};
  }
  if (j == 0 && threadIdx.x == 0) __builtin_amdgcn_raw_buffer_store_b32(tag, rw, h * 256, 0, 16);
  __syncthreads();
  u32x2 kvf[NT];
#pragma unroll
  for (int t = 0; t < NT; ++t) kvf[t] = kvs[t][l];
#pragma unroll
  for (int u = 0; u < 2; ++u) {
    f32x4 den = mfma16(phq[u], kvf[12], (f32x4){0, 0, 0, 0});
    f32x4 inv;
#pragma unroll
    for (int r = 0; r < 4; ++r) inv[r] = __builtin_amdgcn_rcpf(rbf(rbf(den[r]) + 1e-6f));
    f32x4 num[12];
#pragma unroll
    for (int t = 0; t < 12; ++t) num[t] = mfma16(phq[u], kvf[t], (f32x4){0, 0, 0, 0});
#pragma unroll
    for (int r = 0; r < 4; ++r) {
      u32 o[6];
#pragma unroll
      for (int t2 = 0; t2 < 6; ++t2) o[t2] = pk_bf16(num[2 * t2][r] * inv[r], num[2 * t2 + 1][r] * inv[r]);
      const u32 off = (row0 + 16 * u + 4 * g + r) * 384;
      __builtin_amdgcn_raw_buffer_store_b128((u32x4){o[0], o[1], o[2], o[3]}, ro, off + 16 * c, 0, STAUX);
      __builtin_amdgcn_raw_buffer_store_b64((u32x2){o[4], o[5]}, ro, off + 256 + 8 * c, 0, STAUX);
    }
  }
}

typedef unsigned long long u64;
__device__ __forceinline__ u64 rt() { u64 t; __builtin_amdgcn_sched_barrier(0); asm volatile("s_memrealtime %0\n\ts_waitcnt lgkmcnt(0)" : "=s"(t) :: "memory"); __builtin_amdgcn_sched_barrier(0); return t; }
#define P_LA 0
#define P_SA 0
#define P_NTEN 0
#define P_WR 0
__global__ __launch_bounds__(512) void probe_k(const u32* K, const u32* V, const u32* Q, u32* O, u64* ts) {
  u64 t0 = rt();
  const int b = blockIdx.x;
  const int w = __builtin_amdgcn_readfirstlane(threadIdx.x >> 6);
  const int l = threadIdx.x & 63;
  const u32 base = (b * 256 + w * 32) * 384;
  __amdgpu_buffer_rsrc_t r[3] = {__builtin_amdgcn_make_buffer_rsrc((void*)K, 0, 25165824, 0x00020000),
    __builtin_amdgcn_make_buffer_rsrc((void*)V, 0, 25165824, 0x00020000),
    __builtin_amdgcn_make_buffer_rsrc((void*)Q, 0, 25165824, 0x00020000)};
  auto ro = __builtin_amdgcn_make_buffer_rsrc((void*)O, 0, 25165824, 0x00020000);
  u32x4 a[3][12];
  u32x4 s = {1, 2, 3, (u32)l};
#pragma unroll
  for (int t = 0; t < P_NTEN; ++t) {
#pragma unroll
      for (int i = 0; i < 12; ++i) a[t][i] = __builtin_amdgcn_raw_buffer_load_b128(r[t], base + i * 1024 + l * 16, 0, P_LA);
  }
#pragma unroll
  for (int t = 0; t < P_NTEN; ++t)
#pragma unroll
    for (int i = 0; i < 12; ++i) s ^= a[t][i];
  asm volatile("" : "+v"(s));
  if (P_WR) {
    __syncthreads();
#pragma unroll
    for (int i = 0; i < 12; ++i) __builtin_amdgcn_raw_buffer_store_b128(s + i, ro, base + i * 1024 + l * 16, 0, P_SA);
  }
  if (threadIdx.x == 0) ts[b] = t0;
}
__global__ void next_k(u64* ts) { if (threadIdx.x == 0) ts[256] = rt(); }
__global__ void spin_k(u64* ts) {
  u64 tb = rt();
  u64 mn = ~0ull;
  for (int i = 0; i < 256; ++i) { u64 x = ts[i]; mn = x < mn ? x : mn; }
  u64 span = ts[256] - mn;
  u64 target = mn + 10000 + span;
  while (rt() < target && rt() < tb + 30000) __builtin_amdgcn_s_sleep(1);
}
extern "C" void kernel_launch(void* const* d_in, const int* in_sizes, int n_in, void* d_out, int out_size,
                              void* d_ws, size_t ws_size, hipStream_t stream) {
  const float scale = 0.26864248f;
  u64* ts = (u64*)((char*)d_ws + (64 << 20));
  probe_k<<<256, 512, 0, stream>>>((const u32*)d_in[1], (const u32*)d_in[2], (const u32*)d_in[0], (u32*)d_out, ts);
  next_k<<<1, 64, 0, stream>>>(ts);
  rfa_fused<<<H * NCH, 512, 0, stream>>>((const u32*)d_in[0], (const u32*)d_in[1], (const u32*)d_in[2], (const u32*)d_in[3],
                                        (u32*)d_out, (u32*)d_ws, scale);
  spin_k<<<1, 64, 0, stream>>>(ts);
}
```

```cpp
#define STAGGER 0
#define WQ 4
#define XMAP 1
#include <hip/hip_runtime.h>
#include <stdint.h>

#define LDAUX 2
#define STAUX 2
#define EMU 1
#define TLBPF 1
#define HANDOVER 8
#define POLLSLEEP 4

typedef unsigned u32;
typedef unsigned long long u64;
typedef u32 u32x4 __attribute__((ext_vector_type(4)));
typedef u32 u32x2 __attribute__((ext_vector_type(2)));
typedef float f32x4 __attribute__((ext_vector_type(4)));
typedef float f32x2 __attribute__((ext_vector_type(2)));
typedef __bf16 bf16x8 __attribute__((ext_vector_type(8)));
typedef __bf16 bf16x2 __attribute__((ext_vector_type(2)));
typedef short s16x4 __attribute__((ext_vector_type(4)));
#define LAS __attribute__((address_space(3)))

constexpr int H = 32, S = 2048, D = 192, M = 16;
constexpr int CH = 256;
constexpr int NCH = S / CH;
constexpr u32 TBYTES = H * S * D * 2;
constexpr u32 PART_BYTES = 6 * 1024;
constexpr u32 FLAG_OFF = NCH * PART_BYTES;
constexpr u32 EPOCH_OFF = FLAG_OFF + NCH * 128;
constexpr u32 HEAD_BYTES = 50 * 1024;

#define STAMP(i)

__device__ __forceinline__ float lo_f(u32 x) { return __uint_as_float(x << 16); }
__device__ __forceinline__ float hi_f(u32 x) { return __uint_as_float(x & 0xffff0000u); }
__device__ __forceinline__ u32 pk_bf16(float a, float b) {
  f32x2 v = {a, b};
  bf16x2 r = __builtin_convertvector(v, bf16x2);
  return __builtin_bit_cast(u32, r);
}
__device__ __forceinline__ float rbf(float a) { return lo_f(pk_bf16(a, 0.f)); }
__device__ __forceinline__ f32x4 mfma32(u32x4 a, u32x4 b, f32x4 c) {
  return __builtin_amdgcn_mfma_f32_16x16x32_bf16(__builtin_bit_cast(bf16x8, a), __builtin_bit_cast(bf16x8, b), c, 0, 0, 0);
}
__device__ __forceinline__ f32x4 mfma16(u32x2 a, u32x2 b, f32x4 c) {
  return __builtin_amdgcn_mfma_f32_16x16x16bf16_1k(__builtin_bit_cast(s16x4, a), __builtin_bit_cast(s16x4, b), c, 0, 0, 0);
}
template <bool E>
__device__ __forceinline__ u32x4 scale_frag(u32x4 x, float scale, float& ss) {
  u32x4 r;
#pragma unroll
  for (int i = 0; i < 4; ++i) {
    u32 p = E ? pk_bf16(lo_f(x[i]) * scale, hi_f(x[i]) * scale) : x[i];
    r[i] = p;
    ss = __builtin_amdgcn_fdot2_f32_bf16(__builtin_bit_cast(bf16x2, p), __builtin_bit_cast(bf16x2, p), ss, false);
  }
  return r;
}
template <bool E>
__device__ __forceinline__ u32 phi2(float p0, float s0, float p1, float s1, float scale) {
  if (E) {
  u32 pp = pk_bf16(p0, p1), sp = pk_bf16(s0, s1);
  u32 tp = pk_bf16(fmaf(lo_f(sp), -0.5f, lo_f(pp)), fmaf(hi_f(sp), -0.5f, hi_f(pp)));
  u32 ep = pk_bf16(__expf(lo_f(tp)), __expf(hi_f(tp)));
  return pk_bf16(fmaf(lo_f(ep), 0.25f, 1e-6f), fmaf(hi_f(ep), 0.25f, 1e-6f));
  } else {
  const float sl = scale * 1.44269504f, hs = -0.5f * scale * sl;
  return pk_bf16(fmaf(__builtin_amdgcn_exp2f(fmaf(p0, sl, s0 * hs)), 0.25f, 1e-6f), fmaf(__builtin_amdgcn_exp2f(fmaf(p1, sl, s1 * hs)), 0.25f, 1e-6f));
  }
}
__device__ __forceinline__ float swap1(float x) {
  return __builtin_bit_cast(float, __builtin_amdgcn_update_dpp(0, __builtin_bit_cast(int, x), 0xB1, 0xf, 0xf, true));
}
__device__ __forceinline__ void lds_signal(LAS u32* p) {
  asm volatile("s_waitcnt lgkmcnt(0)" ::: "memory");
  __hip_atomic_fetch_add(p, 1u, __ATOMIC_RELAXED, __HIP_MEMORY_SCOPE_WORKGROUP);
}
__device__ __forceinline__ void lds_wait(LAS u32* p, u32 want) {
  while (__builtin_amdgcn_readfirstlane(__hip_atomic_load(p, __ATOMIC_RELAXED, __HIP_MEMORY_SCOPE_WORKGROUP)) < want)
    __builtin_amdgcn_s_sleep(1);
  asm volatile("" ::: "memory");
}
__device__ __forceinline__ u32x4 tag_pack(f32x4 v, u32x4 tagv) {
  return ((__builtin_bit_cast(u32x4, v) + 0x80u) & 0xffffff00u) | tagv;
}

struct __attribute__((aligned(16))) Smem {
  u32x4 om[6][64];
  u32x4 phik[8][64];
  u32x2 kv[48 * 17];
  u32x2 z[4];
  u32 cnt[8];
};
#define CNT(i) ((LAS u32*)&sm.cnt[i])
__device__ __forceinline__ void lds_add(LAS u32* p) {
  __hip_atomic_fetch_add(p, 1u, __ATOMIC_RELAXED, __HIP_MEMORY_SCOPE_WORKGROUP);
}

__global__ __launch_bounds__(768) void rfa_fused(const u32* __restrict__ Q, const u32* __restrict__ K, const u32* __restrict__ V,
                                                 const u32* __restrict__ Om, u32* __restrict__ O, u32* ws, float scale) {
  __shared__ Smem sm;
  STAMP(0)
  const int b = blockIdx.x;
  const int h = b >> 3, j = b & 7;
  const int w = __builtin_amdgcn_readfirstlane(threadIdx.x >> 6);
  const int l = threadIdx.x & 63, g = l >> 4, c = l & 15;
  const u32 rowb = h * S + j * CH;
  if (w >= 1 && w <= 8) {
    const int wa = w - 1;
    const u32 row0 = rowb + wa * 32;
    const u32 off_frag = (row0 + (c >> 1)) * 384 + (2 * g + (c & 1)) * 16;
    auto rk = __builtin_amdgcn_make_buffer_rsrc((void*)K, 0, TBYTES, 0x00020000);
    auto rq = __builtin_amdgcn_make_buffer_rsrc((void*)Q, 0, TBYTES, 0x00020000);
    auto ro = __builtin_amdgcn_make_buffer_rsrc((void*)O, 0, TBYTES, 0x00020000);
    u32x4 om[3][2], xf[12];
    if (wa >= 4) __builtin_amdgcn_s_barrier();
    __builtin_amdgcn_sched_barrier(0);
#pragma unroll
    for (int n = 0; n < 12; ++n) xf[n] = __builtin_amdgcn_raw_buffer_load_b128(rk, off_frag + (n % 3) * 128, (n / 3) * 3072, LDAUX);
    __builtin_amdgcn_sched_barrier(0);
    if (wa < 4) __builtin_amdgcn_s_barrier();
    else if (l == 0) lds_add(CNT(0));
    STAMP(1)
    lds_wait(CNT(1), 1);
#pragma unroll
    for (int i = 0; i < 3; ++i)
#pragma unroll
      for (int p = 0; p < 2; ++p) om[i][p] = sm.om[2 * i + p][l];
    {
      u32x4 phiT;
#pragma unroll
      for (int a = 0; a < 4; ++a) {
        float ss = 0.f;
        f32x4 d0 = {0, 0, 0, 0}, d1 = {0, 0, 0, 0};
#pragma unroll
        for (int i = 0; i < 3; ++i) {
          u32x4 ks = scale_frag<(EMU & 1) != 0>(xf[3 * a + i], scale, ss);
          d0 = mfma32(ks, om[i][0], d0);
          d1 = mfma32(ks, om[i][1], d1);
        }
        f32x4 sq = __builtin_amdgcn_mfma_f32_16x16x4f32(ss, 1.0f, (f32x4){0, 0, 0, 0}, 0, 0, 0);
        phiT[a] = phi2<(EMU & 1) != 0>(d0[0] + d1[1], sq[0] + sq[1], d0[2] + d1[3], sq[2] + sq[3], scale);
      }
      sm.phik[wa][l] = phiT;
    }
    if (l == 0) lds_signal(CNT(2));
    STAMP(2)
    if (!STAGGER || wa < 4) lds_wait(CNT(0), 7);
    else lds_wait(CNT(6), 4);
    __builtin_amdgcn_sched_barrier(0);
#pragma unroll
    for (int n = 0; n < WQ; ++n) xf[n] = __builtin_amdgcn_raw_buffer_load_b128(rq, off_frag + (n % 3) * 128, (n / 3) * 3072, LDAUX);
    __builtin_amdgcn_sched_barrier(0);
    STAMP(3)
    const bool odd = c & 1;
    u32x2 phq[2];
#pragma unroll
    for (int u = 0; u < 2; ++u) {
      float ss[2];
      f32x4 d[2][2];
#pragma unroll
      for (int a = 0; a < 2; ++a) {
        ss[a] = 0.f;
        d[a][0] = (f32x4){0, 0, 0, 0};
        d[a][1] = (f32x4){0, 0, 0, 0};
#pragma unroll
        for (int i = 0; i < 3; ++i) {
          const int n = 6 * u + 3 * a + i;
          if (n + WQ < 12) {
            asm volatile("" : "+v"(xf[n]));
            __builtin_amdgcn_sched_barrier(0);
            xf[n + WQ] = __builtin_amdgcn_raw_buffer_load_b128(rq, off_frag + ((n + WQ) % 3) * 128, ((n + WQ) / 3) * 3072, LDAUX);
            __builtin_amdgcn_sched_barrier(0);
          }
          if (STAGGER && n == HANDOVER) {
            asm volatile("" : "+v"(xf[n]));
            if (l == 0) lds_add(CNT(6));
          }
          u32x4 qs = scale_frag<(EMU & 2) != 0>(xf[n], scale, ss[a]);
          d[a][0] = mfma32(om[i][0], qs, d[a][0]);
          d[a][1] = mfma32(om[i][1], qs, d[a][1]);
        }
      }
      const float st = (odd ? ss[1] : ss[0]) + swap1(odd ? ss[0] : ss[1]);
      f32x4 sq = __builtin_amdgcn_mfma_f32_16x16x4f32(1.0f, st, (f32x4){0, 0, 0, 0}, 0, 0, 0);
      float pr[4];
#pragma unroll
      for (int r = 0; r < 4; ++r) pr[r] = (odd ? d[1][1][r] : d[0][0][r]) + swap1(odd ? d[0][1][r] : d[1][0][r]);
      phq[u][0] = phi2<(EMU & 2) != 0>(pr[0], sq[0], pr[1], sq[0], scale);
      phq[u][1] = phi2<(EMU & 2) != 0>(pr[2], sq[0], pr[3], sq[0], scale);
    }
    asm volatile("" : "+v"(phq[0]), "+v"(phq[1]));
    STAMP(4)
    lds_wait(CNT(5), 4);
    STAMP(5)
    u32x2 kvf[13];
#pragma unroll
    for (int t = 0; t < 8; ++t) kvf[t] = sm.kv[4 * (8 * c + t) + g + 2 * c + (t >> 2)];
#pragma unroll
    for (int t = 0; t < 4; ++t) kvf[8 + t] = sm.kv[4 * (128 + 4 * c + t) + g + 32 + c];
    kvf[12] = sm.z[g];
    const u32 off_row = (row0 + 2 * g) * 384;
#pragma unroll
    for (int u = 0; u < 2; ++u) {
      f32x4 den = mfma16(phq[u], kvf[12], (f32x4){0, 0, 0, 0});
      f32x4 inv;
#pragma unroll
      for (int r = 0; r < 4; ++r) inv[r] = __builtin_amdgcn_rcpf(rbf(rbf(den[r]) + 1e-6f));
      f32x4 num[12];
#pragma unroll
      for (int t = 0; t < 12; ++t) num[t] = mfma16(phq[u], kvf[t], (f32x4){0, 0, 0, 0});
#pragma unroll
      for (int r = 0; r < 4; ++r) {
        u32 o[6];
#pragma unroll
        for (int t2 = 0; t2 < 6; ++t2) o[t2] = pk_bf16(num[2 * t2][r] * inv[r], num[2 * t2 + 1][r] * inv[r]);
        const u32 so = (16 * u + 8 * (r & 1)) * 384;
        __builtin_amdgcn_raw_buffer_store_b128((u32x4){o[0], o[1], o[2], o[3]}, ro, off_row + 16 * c + (r >> 1) * 384, so, STAUX);
        __builtin_amdgcn_raw_buffer_store_b64((u32x2){o[4], o[5]}, ro, off_row + 8 * c + 256 + (r >> 1) * 384, so, STAUX);
      }
    }
    STAMP(6)
    STAMP(7)
  } else {
    auto rw = __builtin_amdgcn_make_buffer_rsrc((char*)ws + h * HEAD_BYTES, 0, HEAD_BYTES, 0x00020000);
    if (w > 8) {
      const int cg = w - 9;
      auto rv = __builtin_amdgcn_make_buffer_rsrc((void*)V, 0, TBYTES, 0x00020000);
      const u32 voff = (rowb + 2 * g) * 384 + cg * 128 + 8 * c;
      __builtin_amdgcn_s_barrier();
      lds_wait(CNT(0), 4);
      __builtin_amdgcn_sched_barrier(0);
      STAMP(1)
      u32x2 vf[8][4][2];
#pragma unroll
      for (int s = 0; s < 8; ++s)
#pragma unroll
        for (int a = 0; a < 4; ++a)
#pragma unroll
          for (int bb = 0; bb < 2; ++bb)
            vf[s][a][bb] = __builtin_amdgcn_raw_buffer_load_b64(rv, voff + bb * 384, (s * 4 + a) * 3072, LDAUX);
      __builtin_amdgcn_sched_barrier(0);
      if (l == 0) lds_add(CNT(0));
      __builtin_amdgcn_s_setprio(3);
      STAMP(2)
      lds_wait(CNT(2), 8);
      f32x4 acc[4];
#pragma unroll
      for (int t = 0; t < 4; ++t) acc[t] = (f32x4){0, 0, 0, 0};
#pragma unroll
      for (int s = 0; s < 8; ++s) {
        const u32x4 phiT = sm.phik[s][l];
#pragma unroll
        for (int t = 0; t < 4; ++t) {
          u32x4 bf;
          const u32 sel = (t & 1) ? 0x07060302u : 0x05040100u;
#pragma unroll
          for (int a = 0; a < 4; ++a) bf[a] = __builtin_amdgcn_perm(vf[s][a][1][t >> 1], vf[s][a][0][t >> 1], sel);
          acc[t] = mfma32(phiT, bf, acc[t]);
        }
      }
      STAMP(3)
#pragma unroll
      for (int t = 0; t < 2; ++t) {
        const u32x4 x = {pk_bf16(acc[2 * t][0], acc[2 * t][1]), pk_bf16(acc[2 * t][2], acc[2 * t][3]),
                         pk_bf16(acc[2 * t + 1][0], acc[2 * t + 1][1]), pk_bf16(acc[2 * t + 1][2], acc[2 * t + 1][3])};
        __builtin_amdgcn_raw_buffer_store_b128(x, rw, l * 16 + t * 1024, j * PART_BYTES + cg * 2048, 16);
      }
      asm volatile("s_waitcnt vmcnt(0)" ::: "memory");
      if (l == 0) lds_add(CNT(3));
      STAMP(4)
      lds_wait(CNT(4), 1);
      STAMP(5)
      u32x4 x[7][2];
#pragma unroll
      for (int i = 0; i < 7; ++i) {
        const u32 pb = ((j + 1 + i) & 7) * PART_BYTES + cg * 2048;
#pragma unroll
        for (int t = 0; t < 2; ++t) x[i][t] = __builtin_amdgcn_raw_buffer_load_b128(rw, l * 16 + t * 1024, pb, 16);
      }
#pragma unroll
      for (int t = 0; t < 4; ++t) {
        f32x4 s = acc[t];
#pragma unroll
        for (int i = 0; i < 7; ++i) {
          const u32 p0 = x[i][t >> 1][2 * (t & 1)], p1 = x[i][t >> 1][2 * (t & 1) + 1];
          s += (f32x4){lo_f(p0), hi_f(p0), lo_f(p1), hi_f(p1)};
        }
        sm.kv[4 * (64 * cg + 4 * c + t) + g + 16 * cg + c] = (u32x2){pk_bf16(s[0], s[1]), pk_bf16(s[2], s[3])};
      }
      if (l == 0) lds_signal(CNT(5));
      STAMP(6)
    } else {
      __builtin_amdgcn_s_setprio(3);
      auto rom = __builtin_amdgcn_make_buffer_rsrc((void*)Om, 0, M * D * 2, 0x00020000);
      u32x4 om[6];
#pragma unroll
      for (int i = 0; i < 6; ++i) om[i] = __builtin_amdgcn_raw_buffer_load_b128(rom, c * 384 + 2 * g * 16, (i >> 1) * 128 + (i & 1) * 16, 0);
      u32 vz;
      asm volatile("v_mov_b32 %0, 0" : "=v"(vz));
      const u32 epoch = __builtin_amdgcn_raw_buffer_load_b32(rw, vz, EPOCH_OFF, 16);
      u32 pf = 0;
      {
        const char* base = g == 0 ? (const char*)Q : g == 1 ? (const char*)O : (const char*)V;
        const char* p = base + rowb * 384 + (c < 2 ? c * 98300 : 49152);
        if (g == 3) p = (const char*)ws + h * HEAD_BYTES;
        pf = __builtin_nontemporal_load((const u32*)p);
      }
      if (l < 8) sm.cnt[l] = 0;
      asm volatile("s_waitcnt lgkmcnt(0)" ::: "memory");
      __builtin_amdgcn_s_barrier();
      asm volatile("" ::: "memory");
#pragma unroll
      for (int i = 0; i < 6; ++i) sm.om[i][l] = om[i];
      if (l == 0) lds_signal(CNT(1));
      STAMP(1)
      const u32 tag = epoch + 1u;
      asm volatile("" :: "v"(pf), "v"(tag));
      STAMP(2)
      lds_wait(CNT(2), 8);
      f32x4 acc = {0, 0, 0, 0};
      const u32x4 ones = {0x3f803f80u, 0x3f803f80u, 0x3f803f80u, 0x3f803f80u};
#pragma unroll
      for (int s = 0; s < 8; ++s) acc = mfma32(ones, sm.phik[s][l], acc);
      asm volatile("" : "+v"(acc));
      lds_wait(CNT(3), 3);
      STAMP(3)
      if (g == 0) __builtin_amdgcn_raw_buffer_store_b64((u32x2){tag, __float_as_uint(acc[0])}, rw, c * 8, FLAG_OFF + j * 128, 16);
      STAMP(4)
      u32x4 x;
      int spin = 0;
      for (; spin < (1 << 18); ++spin) {
        x = __builtin_amdgcn_raw_buffer_load_b128(rw, l * 16, FLAG_OFF, 16);
        if (__all(x[0] == tag && x[2] == tag)) break;
        __builtin_amdgcn_s_sleep(POLLSLEEP);
        asm volatile("" ::: "memory");
      }
      if (l == 0) lds_add(CNT(4));
      STAMP(5)
      float z0 = __uint_as_float(x[1]), z1 = __uint_as_float(x[3]);
#pragma unroll
      for (int m = 8; m < 64; m <<= 1) {
        z0 += __shfl_xor(z0, m);
        z1 += __shfl_xor(z1, m);
      }
      if (l < 8) ((u32*)sm.z)[l] = pk_bf16(z0, z1);
      if (l == 0) lds_signal(CNT(5));
      STAMP(6)
      if (j == 0) {
        lds_wait(CNT(5), 4);
        if (l == 0) __builtin_amdgcn_raw_buffer_store_b32(tag, rw, 0, EPOCH_OFF, 16);
      }
    }
  }
}

extern "C" void kernel_launch(void* const* d_in, const int* in_sizes, int n_in, void* d_out, int out_size,
                              void* d_ws, size_t ws_size, hipStream_t stream) {
  const float scale = 0.26864248f;
  rfa_fused<<<H * NCH, 768, 0, stream>>>((const u32*)d_in[0], (const u32*)d_in[1], (const u32*)d_in[2], (const u32*)d_in[3],
                                        (u32*)d_out, (u32*)d_ws, scale);
}
```

```cpp
#define WQ 4
#define QG2 8
#include <hip/hip_runtime.h>
#include <stdint.h>

#define LDAUX 2
#define STAUX 2
#define EMU 1
#define TLBPF 1
#define HANDOVER 8
#define STAGGER 0
#define XMAP 0
#define FIRSTDELAY 16
#define GODELAY 0
#define HSLEEP 8
#define QG01 8
#define ZPRIO 0
#define POLLSLEEP 4

typedef unsigned u32;
typedef unsigned long long u64;
typedef u32 u32x4 __attribute__((ext_vector_type(4)));
typedef u32 u32x2 __attribute__((ext_vector_type(2)));
typedef u32 u32x3 __attribute__((ext_vector_type(3)));
typedef float f32x4 __attribute__((ext_vector_type(4)));
typedef float f32x2 __attribute__((ext_vector_type(2)));
typedef __bf16 bf16x8 __attribute__((ext_vector_type(8)));
typedef __bf16 bf16x2 __attribute__((ext_vector_type(2)));
typedef short s16x4 __attribute__((ext_vector_type(4)));
#define LAS __attribute__((address_space(3)))

constexpr int H = 32, S = 2048, D = 192, M = 16;
constexpr int CH = 256;
constexpr int NCH = S / CH;
constexpr u32 TBYTES = H * S * D * 2;
constexpr u32 CG_BYTES = 2304;
constexpr u32 PART_BYTES = 3 * CG_BYTES;
constexpr u32 FLAG_OFF = NCH * PART_BYTES;
constexpr u32 HINT_OFF = FLAG_OFF + NCH * 256;
constexpr u32 EPOCH_OFF = HINT_OFF + 256;
constexpr u32 HEAD_BYTES = 57 * 1024;

#define STAMP(i)

__device__ __forceinline__ float lo_f(u32 x) { return __uint_as_float(x << 16); }
__device__ __forceinline__ float hi_f(u32 x) { return __uint_as_float(x & 0xffff0000u); }
__device__ __forceinline__ u32 pk_bf16(float a, float b) {
  f32x2 v = {a, b};
  bf16x2 r = __builtin_convertvector(v, bf16x2);
  return __builtin_bit_cast(u32, r);
}
__device__ __forceinline__ float rbf(float a) { return lo_f(pk_bf16(a, 0.f)); }
__device__ __forceinline__ f32x4 mfma32(u32x4 a, u32x4 b, f32x4 c) {
  return __builtin_amdgcn_mfma_f32_16x16x32_bf16(__builtin_bit_cast(bf16x8, a), __builtin_bit_cast(bf16x8, b), c, 0, 0, 0);
}
__device__ __forceinline__ f32x4 mfma16(u32x2 a, u32x2 b, f32x4 c) {
  return __builtin_amdgcn_mfma_f32_16x16x16bf16_1k(__builtin_bit_cast(s16x4, a), __builtin_bit_cast(s16x4, b), c, 0, 0, 0);
}
template <bool E>
__device__ __forceinline__ u32x4 scale_frag(u32x4 x, float scale, float& ss) {
  u32x4 r;
#pragma unroll
  for (int i = 0; i < 4; ++i) {
    u32 p = E ? pk_bf16(lo_f(x[i]) * scale, hi_f(x[i]) * scale) : x[i];
    r[i] = p;
    ss = __builtin_amdgcn_fdot2_f32_bf16(__builtin_bit_cast(bf16x2, p), __builtin_bit_cast(bf16x2, p), ss, false);
  }
  return r;
}
template <bool E>
__device__ __forceinline__ u32 phi2(float p0, float s0, float p1, float s1, float scale) {
  if (E) {
  u32 pp = pk_bf16(p0, p1), sp = pk_bf16(s0, s1);
  u32 tp = pk_bf16(fmaf(lo_f(sp), -0.5f, lo_f(pp)), fmaf(hi_f(sp), -0.5f, hi_f(pp)));
  u32 ep = pk_bf16(__expf(lo_f(tp)), __expf(hi_f(tp)));
  return pk_bf16(fmaf(lo_f(ep), 0.25f, 1e-6f), fmaf(hi_f(ep), 0.25f, 1e-6f));
  } else {
  const float sl = scale * 1.44269504f, hs = -0.5f * scale * sl;
  return pk_bf16(fmaf(__builtin_amdgcn_exp2f(fmaf(p0, sl, s0 * hs)), 0.25f, 1e-6f), fmaf(__builtin_amdgcn_exp2f(fmaf(p1, sl, s1 * hs)), 0.25f, 1e-6f));
  }
}
__device__ __forceinline__ float swap1(float x) {
  return __builtin_bit_cast(float, __builtin_amdgcn_update_dpp(0, __builtin_bit_cast(int, x), 0xB1, 0xf, 0xf, true));
}
__device__ __forceinline__ void lds_signal(LAS u32* p) {
  asm volatile("s_waitcnt lgkmcnt(0)" ::: "memory");
  __hip_atomic_fetch_add(p, 1u, __ATOMIC_RELAXED, __HIP_MEMORY_SCOPE_WORKGROUP);
}
__device__ __forceinline__ void lds_wait(LAS u32* p, u32 want) {
  while (__builtin_amdgcn_readfirstlane(__hip_atomic_load(p, __ATOMIC_RELAXED, __HIP_MEMORY_SCOPE_WORKGROUP)) < want)
    __builtin_amdgcn_s_sleep(1);
  asm volatile("" ::: "memory");
}
__device__ __forceinline__ void lds_spin(LAS u32* p, u32 want) {
  while (__builtin_amdgcn_readfirstlane(__hip_atomic_load(p, __ATOMIC_RELAXED, __HIP_MEMORY_SCOPE_WORKGROUP)) < want) {}
  asm volatile("" ::: "memory");
}
__device__ __forceinline__ u32x4 tag_pack(f32x4 v, u32x4 tagv) {
  return ((__builtin_bit_cast(u32x4, v) + 0x80u) & 0xffffff00u) | tagv;
}

__device__ __forceinline__ u32 xor3(u32 a, u32 b, u32 c) { return __builtin_amdgcn_bitop3_b32(a, b, c, 0x96); }
__device__ __forceinline__ u32 xor9(u32x4 a, u32x4 b, u32 c) {
  return xor3(xor3(a[0], a[1], a[2]), xor3(a[3], b[0], b[1]), xor3(b[2], b[3], c));
}
struct __attribute__((aligned(16))) Smem {
  u32x4 om[6][64];
  u32x4 phik[8][64];
  u32x2 kv[48 * 17];
  u32x2 z[4];
  u32 tag;
  u32 pad[3];
  u32 cnt[8];
};
#define CNT(i) ((LAS u32*)&sm.cnt[i])
__device__ __forceinline__ void lds_add(LAS u32* p) {
  __hip_atomic_fetch_add(p, 1u, __ATOMIC_RELAXED, __HIP_MEMORY_SCOPE_WORKGROUP);
}

__global__ __launch_bounds__(768) void rfa_fused(const u32* __restrict__ Q, const u32* __restrict__ K, const u32* __restrict__ V,
                                                 const u32* __restrict__ Om, u32* __restrict__ O, u32* ws, float scale) {
  __shared__ Smem sm;
  STAMP(0)
  const int b = blockIdx.x;
  const int h = (b & 7) * 4 + (b >> 6), j = (b >> 3) & 7;
  const int w = __builtin_amdgcn_readfirstlane(threadIdx.x >> 6);
  const int l = threadIdx.x & 63, g = l >> 4, c = l & 15;
  const u32 rowb = h * S + j * CH;
  if (w >= 1 && w <= 8) {
    const int wa = w - 1;
    const u32 row0 = rowb + wa * 32;
    const u32 off_frag = (row0 + (c >> 1)) * 384 + (2 * g + (c & 1)) * 16;
    auto rk = __builtin_amdgcn_make_buffer_rsrc((void*)K, 0, TBYTES, 0x00020000);
    auto rq = __builtin_amdgcn_make_buffer_rsrc((void*)Q, 0, TBYTES, 0x00020000);
    auto ro = __builtin_amdgcn_make_buffer_rsrc((void*)O, 0, TBYTES, 0x00020000);
    u32x4 om[3][2], xf[12];
    if (wa >= 4) __builtin_amdgcn_s_barrier();
    __builtin_amdgcn_sched_barrier(0);
#pragma unroll
    for (int n = 0; n < 12; ++n) xf[n] = __builtin_amdgcn_raw_buffer_load_b128(rk, off_frag + (n % 3) * 128, (n / 3) * 3072, LDAUX);
    __builtin_amdgcn_sched_barrier(0);
    if (wa < 4) __builtin_amdgcn_s_barrier();
    else if (l == 0) lds_add(CNT(0));
    STAMP(1)
    lds_wait(CNT(1), 1);
#pragma unroll
    for (int i = 0; i < 3; ++i)
#pragma unroll
      for (int p = 0; p < 2; ++p) om[i][p] = sm.om[2 * i + p][l];
    {
      u32x4 phiT;
#pragma unroll
      for (int a = 0; a < 4; ++a) {
        float ss = 0.f;
        f32x4 d0 = {0, 0, 0, 0}, d1 = {0, 0, 0, 0};
#pragma unroll
        for (int i = 0; i < 3; ++i) {
          u32x4 ks = scale_frag<(EMU & 1) != 0>(xf[3 * a + i], scale, ss);
          d0 = mfma32(ks, om[i][0], d0);
          d1 = mfma32(ks, om[i][1], d1);
        }
        f32x4 sq = __builtin_amdgcn_mfma_f32_16x16x4f32(ss, 1.0f, (f32x4){0, 0, 0, 0}, 0, 0, 0);
        phiT[a] = phi2<(EMU & 1) != 0>(d0[0] + d1[1], sq[0] + sq[1], d0[2] + d1[3], sq[2] + sq[3], scale);
      }
      sm.phik[wa][l] = phiT;
    }
    if (l == 0) lds_signal(CNT(2));
    STAMP(2)
    if (!STAGGER || wa < 4) lds_wait(CNT(0), 7);
    else lds_wait(CNT(6), 4);
    __builtin_amdgcn_sched_barrier(0);
#pragma unroll
    for (int n = 0; n < WQ; ++n) xf[n] = __builtin_amdgcn_raw_buffer_load_b128(rq, off_frag + (n % 3) * 128, (n / 3) * 3072, LDAUX);
    __builtin_amdgcn_sched_barrier(0);
    if (WQ >= 12 && l == 0) lds_add(CNT(3));
    STAMP(3)
    const bool odd = c & 1;
    u32x2 phq[2];
#pragma unroll
    for (int u = 0; u < 2; ++u) {
      float ss[2];
      f32x4 d[2][2];
#pragma unroll
      for (int a = 0; a < 2; ++a) {
        ss[a] = 0.f;
        d[a][0] = (f32x4){0, 0, 0, 0};
        d[a][1] = (f32x4){0, 0, 0, 0};
#pragma unroll
        for (int i = 0; i < 3; ++i) {
          const int n = 6 * u + 3 * a + i;
          if (n + WQ < 12) {
            asm volatile("" : "+v"(xf[n]));
            __builtin_amdgcn_sched_barrier(0);
            xf[n + WQ] = __builtin_amdgcn_raw_buffer_load_b128(rq, off_frag + ((n + WQ) % 3) * 128, ((n + WQ) / 3) * 3072, LDAUX);
            __builtin_amdgcn_sched_barrier(0);
            if (n + WQ == 11 && l == 0) lds_add(CNT(3));
          }
          if (STAGGER && n == HANDOVER) {
            asm volatile("" : "+v"(xf[n]));
            if (l == 0) lds_add(CNT(6));
          }
          u32x4 qs = scale_frag<(EMU & 2) != 0>(xf[n], scale, ss[a]);
          d[a][0] = mfma32(om[i][0], qs, d[a][0]);
          d[a][1] = mfma32(om[i][1], qs, d[a][1]);
        }
      }
      const float st = (odd ? ss[1] : ss[0]) + swap1(odd ? ss[0] : ss[1]);
      f32x4 sq = __builtin_amdgcn_mfma_f32_16x16x4f32(1.0f, st, (f32x4){0, 0, 0, 0}, 0, 0, 0);
      float pr[4];
#pragma unroll
      for (int r = 0; r < 4; ++r) pr[r] = (odd ? d[1][1][r] : d[0][0][r]) + swap1(odd ? d[0][1][r] : d[1][0][r]);
      phq[u][0] = phi2<(EMU & 2) != 0>(pr[0], sq[0], pr[1], sq[0], scale);
      phq[u][1] = phi2<(EMU & 2) != 0>(pr[2], sq[0], pr[3], sq[0], scale);
    }
    asm volatile("" : "+v"(phq[0]), "+v"(phq[1]));
    STAMP(4)
    lds_wait(CNT(7), 1);
    f32x4 inv[2];
    {
      const u32x2 zf = sm.z[g];
#pragma unroll
      for (int u = 0; u < 2; ++u) {
        f32x4 den = mfma16(phq[u], zf, (f32x4){0, 0, 0, 0});
#pragma unroll
        for (int r = 0; r < 4; ++r) inv[u][r] = __builtin_amdgcn_rcpf(rbf(rbf(den[r]) + 1e-6f));
      }
    }
    asm volatile("" : "+v"(inv[0]), "+v"(inv[1]));
    const u32 off_row = (row0 + 2 * g) * 384;
    lds_spin(CNT(6), 1);
    STAMP(5)
    {
      u32x2 kvf[4];
#pragma unroll
      for (int t = 0; t < 4; ++t) kvf[t] = sm.kv[4 * (128 + 4 * c + t) + g + 32 + c];
#pragma unroll
      for (int u = 0; u < 2; ++u) {
        f32x4 num[4];
#pragma unroll
        for (int t = 0; t < 4; ++t) num[t] = mfma16(phq[u], kvf[t], (f32x4){0, 0, 0, 0}) * inv[u];
#pragma unroll
        for (int r = 0; r < 4; ++r)
          __builtin_amdgcn_raw_buffer_store_b64((u32x2){pk_bf16(num[0][r], num[1][r]), pk_bf16(num[2][r], num[3][r])}, ro,
                                                off_row + 8 * c + 256 + (r >> 1) * 384, (16 * u + 8 * (r & 1)) * 384, STAUX);
      }
    }
    lds_spin(CNT(5), 2);
    {
      u32x2 kvf[8];
#pragma unroll
      for (int t = 0; t < 8; ++t) kvf[t] = sm.kv[4 * (8 * c + t) + g + 2 * c + (t >> 2)];
#pragma unroll
      for (int u = 0; u < 2; ++u) {
        f32x4 num[8];
#pragma unroll
        for (int t = 0; t < 8; ++t) num[t] = mfma16(phq[u], kvf[t], (f32x4){0, 0, 0, 0}) * inv[u];
#pragma unroll
        for (int r = 0; r < 4; ++r)
          __builtin_amdgcn_raw_buffer_store_b128((u32x4){pk_bf16(num[0][r], num[1][r]), pk_bf16(num[2][r], num[3][r]),
                                                         pk_bf16(num[4][r], num[5][r]), pk_bf16(num[6][r], num[7][r])}, ro,
                                                 off_row + 16 * c + (r >> 1) * 384, (16 * u + 8 * (r & 1)) * 384, STAUX);
      }
    }
    STAMP(6)
    STAMP(7)
  } else {
    auto rw = __builtin_amdgcn_make_buffer_rsrc((char*)ws + h * HEAD_BYTES, 0, HEAD_BYTES, 0x00020000);
    if (w > 8) {
      const int cg = w - 9;
      auto rv = __builtin_amdgcn_make_buffer_rsrc((void*)V, 0, TBYTES, 0x00020000);
      const u32 voff = (rowb + 2 * g) * 384 + cg * 128 + 8 * c;
      __builtin_amdgcn_s_barrier();
      lds_wait(CNT(0), 4);
      __builtin_amdgcn_sched_barrier(0);
      STAMP(1)
      u32x2 vf[8][4][2];
#pragma unroll
      for (int s = 0; s < 8; ++s)
#pragma unroll
        for (int a = 0; a < 4; ++a)
#pragma unroll
          for (int bb = 0; bb < 2; ++bb)
            vf[s][a][bb] = __builtin_amdgcn_raw_buffer_load_b64(rv, voff + bb * 384, (s * 4 + a) * 3072, LDAUX);
      __builtin_amdgcn_sched_barrier(0);
      if (l == 0) lds_add(CNT(0));
      __builtin_amdgcn_s_setprio(3);
      STAMP(2)
      lds_wait(CNT(2), 9);
      f32x4 acc[4];
#pragma unroll
      for (int t = 0; t < 4; ++t) acc[t] = (f32x4){0, 0, 0, 0};
#pragma unroll
      for (int s = 0; s < 8; ++s) {
        const u32x4 phiT = sm.phik[s][l];
#pragma unroll
        for (int t = 0; t < 4; ++t) {
          u32x4 bf;
          const u32 sel = (t & 1) ? 0x07060302u : 0x05040100u;
#pragma unroll
          for (int a = 0; a < 4; ++a) bf[a] = __builtin_amdgcn_perm(vf[s][a][1][t >> 1], vf[s][a][0][t >> 1], sel);
          acc[t] = mfma32(phiT, bf, acc[t]);
        }
      }
      STAMP(3)
      const u32 tag = sm.tag;
      u32x4 p0, p1;
      {
        p0 = (u32x4){pk_bf16(acc[0][0], acc[0][1]), pk_bf16(acc[0][2], acc[0][3]), pk_bf16(acc[1][0], acc[1][1]), pk_bf16(acc[1][2], acc[1][3])};
        p1 = (u32x4){pk_bf16(acc[2][0], acc[2][1]), pk_bf16(acc[2][2], acc[2][3]), pk_bf16(acc[3][0], acc[3][1]), pk_bf16(acc[3][2], acc[3][3])};
        const u32 chk = xor9(p0, p1, tag);
        const u32 so = j * PART_BYTES + cg * CG_BYTES;
        __builtin_amdgcn_raw_buffer_store_b128(p0, rw, l * 16, so, 16);
        __builtin_amdgcn_raw_buffer_store_b128(p1, rw, l * 16 + 1024, so, 16);
        u32x4 cw;
#pragma unroll
        for (int e = 0; e < 4; ++e) cw[e] = __builtin_amdgcn_ds_bpermute(((4 * l + e) & 63) * 4, chk);
        if (l < 16) __builtin_amdgcn_raw_buffer_store_b128(cw, rw, l * 16 + 2048, so, 16);
      }
      STAMP(4)
      u32x4 sel[2];
      {
        const u32 one = (c & 1) ? 0x3f800000u : 0x00003f80u;
#pragma unroll
        for (int hf = 0; hf < 2; ++hf)
#pragma unroll
          for (int e = 0; e < 4; ++e) sel[hf][e] = (g == 2 * hf + (c >> 3) && ((c & 7) >> 1) == e) ? one : 0u;
      }
      f32x4 own[2][2];
#pragma unroll
      for (int hf = 0; hf < 2; ++hf) {
        own[0][hf] = mfma32(sel[hf], p0, (f32x4){0, 0, 0, 0});
        own[1][hf] = mfma32(sel[hf], p1, (f32x4){0, 0, 0, 0});
      }
      lds_wait(CNT(4), 1);
      lds_wait(CNT(3), cg == 2 ? QG2 : QG01);
      STAMP(5)
      int spin = 0;
      f32x4 d[2][2];
      for (; spin < (1 << 18); ++spin) {
        u32x4 x[7][2];
        u32 xc[7];
#pragma unroll
        for (int i = 0; i < 7; ++i) {
          const u32 pb = ((j + 1 + i) & 7) * PART_BYTES + cg * CG_BYTES;
          x[i][0] = __builtin_amdgcn_raw_buffer_load_b128(rw, l * 16, pb, 16);
          x[i][1] = __builtin_amdgcn_raw_buffer_load_b128(rw, l * 16 + 1024, pb, 16);
          xc[i] = __builtin_amdgcn_raw_buffer_load_b32(rw, l * 4 + 2048, pb, 16);
        }
        __builtin_amdgcn_sched_barrier(0);
        u32 bad = 0;
#pragma unroll
        for (int s2 = 0; s2 < 2; ++s2)
#pragma unroll
          for (int hf = 0; hf < 2; ++hf) d[s2][hf] = own[s2][hf];
#pragma unroll
        for (int i = 0; i < 7; ++i) {
#pragma unroll
          for (int s2 = 0; s2 < 2; ++s2)
#pragma unroll
            for (int hf = 0; hf < 2; ++hf) d[s2][hf] = mfma32(sel[hf], x[i][s2], d[s2][hf]);
          bad |= xor9(x[i][0], x[i][1], xc[i]) ^ tag;
        }
        if (__all(bad == 0)) break;
        __builtin_amdgcn_s_sleep(POLLSLEEP);
        asm volatile("" ::: "memory");
      }
#pragma unroll
      for (int s2 = 0; s2 < 2; ++s2)
#pragma unroll
        for (int hf = 0; hf < 2; ++hf)
          sm.kv[4 * (64 * cg + 4 * c + 2 * s2 + (g & 1)) + 2 * hf + (g >> 1) + 16 * cg + c] =
              (u32x2){pk_bf16(d[s2][hf][0], d[s2][hf][1]), pk_bf16(d[s2][hf][2], d[s2][hf][3])};
      if (l == 0) lds_signal(cg == 2 ? CNT(6) : CNT(5));
      STAMP(6)
    } else {
      __builtin_amdgcn_s_setprio(3);
      auto rom = __builtin_amdgcn_make_buffer_rsrc((void*)Om, 0, M * D * 2, 0x00020000);
      u32x4 om[6];
#pragma unroll
      for (int i = 0; i < 6; ++i) om[i] = __builtin_amdgcn_raw_buffer_load_b128(rom, c * 384 + 2 * g * 16, (i >> 1) * 128 + (i & 1) * 16, 0);
      u32 vz;
      asm volatile("v_mov_b32 %0, 0" : "=v"(vz));
      const u32 epoch = __builtin_amdgcn_raw_buffer_load_b32(rw, vz, EPOCH_OFF, 16);
      u32 pf = 0;
      {
        const char* base = g == 0 ? (const char*)Q : g == 1 ? (const char*)O : (const char*)V;
        const char* p = base + rowb * 384 + (c < 2 ? c * 98300 : 49152);
        if (g == 3) p = (const char*)ws + h * HEAD_BYTES;
        pf = __builtin_nontemporal_load((const u32*)p);
      }
      if (l < 8) sm.cnt[l] = 0;
      asm volatile("s_waitcnt lgkmcnt(0)" ::: "memory");
      __builtin_amdgcn_s_barrier();
      asm volatile("" ::: "memory");
#pragma unroll
      for (int i = 0; i < 6; ++i) sm.om[i][l] = om[i];
      if (l == 0) lds_signal(CNT(1));
      STAMP(1)
      const u32 tag = epoch + 1u;
      if (l == 0) sm.tag = tag;
      if (l == 0) lds_signal(CNT(2));
      asm volatile("" :: "v"(pf));
      STAMP(2)
      lds_wait(CNT(2), 9);
      f32x4 acc = {0, 0, 0, 0};
      const u32x4 ones = {0x3f803f80u, 0x3f803f80u, 0x3f803f80u, 0x3f803f80u};
#pragma unroll
      for (int s = 0; s < 8; ++s) acc = mfma32(ones, sm.phik[s][l], acc);
      asm volatile("" : "+v"(acc));
      lds_wait(CNT(0), 7);
      __builtin_amdgcn_s_setprio(ZPRIO);
      STAMP(3)
      if (g == 0) __builtin_amdgcn_raw_buffer_store_b128((u32x4){tag, __float_as_uint(acc[0]), tag, __float_as_uint(acc[0])}, rw, c * 16, FLAG_OFF + j * 256, 16);
      u32* hint = (u32*)((char*)ws + h * HEAD_BYTES + HINT_OFF);
      if (l == 0) asm volatile("global_atomic_swap %0, %1, off" :: "v"(hint + j), "v"(tag) : "memory");
      int spin;
      u32x2 xa, xb;
      {
        u32 r0, r1, r2, r3;
        u64 sv;
        const u32* hp = hint + (l & 7);
        const u32* zp = (const u32*)((char*)ws + h * HEAD_BYTES + FLAG_OFF) + l * 4;
        const u32 goaddr = (u32)(size_t)CNT(4), one = 1u;
#define STR2(x) #x
#define STR(x) STR2(x)
#define HP(r) "global_atomic_add " r ", %[hp], %[vz], off sc0\n\t"
#define HC(r) "s_waitcnt vmcnt(3)\n\tv_cmp_eq_u32 vcc, " r ", %[tag]\n\ts_cmp_eq_u64 vcc, exec\n\ts_cbranch_scc1 2f\n\t"
        asm volatile("s_mov_b64 %[sv], exec\n\t"
                     "s_mov_b64 exec, 0xff\n\t"
                     "s_mov_b32 %[spin], 0\n\t"
                     HP("%[r0]") "s_sleep " STR(HSLEEP) "\n\t"
                     HP("%[r1]") "s_sleep " STR(HSLEEP) "\n\t"
                     HP("%[r2]") "s_sleep " STR(HSLEEP) "\n"
                     "1:\n\t"
                     HP("%[r3]") HC("%[r0]")
                     HP("%[r0]") HC("%[r1]")
                     HP("%[r1]") HC("%[r2]")
                     HP("%[r2]") HC("%[r3]")
                     "s_add_u32 %[spin], %[spin], 1\n\t"
                     "s_cmp_lt_u32 %[spin], 0x4000\n\t"
                     "s_cbranch_scc1 1b\n"
                     "2:\n\t"
                     "s_sleep " STR(GODELAY) "\n\t"
                     "ds_add_u32 %[go], %[one]\n\t"
                     "s_mov_b64 exec, %[sv]\n\t"
                     "global_load_dwordx2 %[xa], %[zp], off sc1\n\t"
                     "global_load_dwordx2 %[xb], %[zp], off offset:1024 sc1\n\t"
                     "s_waitcnt vmcnt(0) lgkmcnt(0)"
                     : [r0] "=&v"(r0), [r1] "=&v"(r1), [r2] "=&v"(r2), [r3] "=&v"(r3), [spin] "=&s"(spin), [sv] "=&s"(sv),
                       [xa] "=&v"(xa), [xb] "=&v"(xb)
                     : [hp] "v"(hp), [vz] "v"(vz), [tag] "v"(tag), [go] "v"(goaddr), [one] "v"(one), [zp] "v"(zp)
                     : "vcc", "scc", "memory");
      }
      STAMP(4)
      for (int sp2 = 0; sp2 < (1 << 18); ++sp2) {
        if (__all(xa[0] == tag && xb[0] == tag)) break;
        __builtin_amdgcn_s_sleep(POLLSLEEP);
        asm volatile("" ::: "memory");
        xa = __builtin_amdgcn_raw_buffer_load_b64(rw, l * 16, FLAG_OFF, 16);
        xb = __builtin_amdgcn_raw_buffer_load_b64(rw, l * 16 + 1024, FLAG_OFF, 16);
      }
      STAMP(5)
      float z0 = __uint_as_float(xa[1]) + __uint_as_float(xb[1]);
      z0 += __shfl_xor(z0, 16);
      z0 += __shfl_xor(z0, 32);
      if (l < 16) ((unsigned short*)sm.z)[l] = (unsigned short)pk_bf16(z0, 0.f);
      if (l == 0) lds_signal(CNT(7));
      STAMP(6)
      if (j == 0) {
        lds_wait(CNT(5), 2);
        if (l == 0) __builtin_amdgcn_raw_buffer_store_b32(tag, rw, 0, EPOCH_OFF, 16);
      }
    }
  }
}

extern "C" void kernel_launch(void* const* d_in, const int* in_sizes, int n_in, void* d_out, int out_size,
                              void* d_ws, size_t ws_size, hipStream_t stream) {
  const float scale = 0.26864248f;
  rfa_fused<<<H * NCH, 768, 0, stream>>>((const u32*)d_in[0], (const u32*)d_in[1], (const u32*)d_in[2], (const u32*)d_in[3],
                                        (u32*)d_out, (u32*)d_ws, scale);
}
```

```cpp
#define WQ 4
#define SEL 0
#define SELROLE 0
#define SELB 100
#include <hip/hip_runtime.h>
#include <stdint.h>

#define LDAUX 2
#define STAUX 2
#define EMU 1
#define TLBPF 1
#define HANDOVER 8
#define STAGGER 0
#define XMAP 0
#define FIRSTDELAY 16
#define GODELAY 0
#define HSLEEP 8
#define QG2 0
#define QG01 8
#define ZPRIO 0
#define POLLSLEEP 4

typedef unsigned u32;
typedef unsigned long long u64;
typedef u32 u32x4 __attribute__((ext_vector_type(4)));
typedef u32 u32x2 __attribute__((ext_vector_type(2)));
typedef u32 u32x3 __attribute__((ext_vector_type(3)));
typedef float f32x4 __attribute__((ext_vector_type(4)));
typedef float f32x2 __attribute__((ext_vector_type(2)));
typedef __bf16 bf16x8 __attribute__((ext_vector_type(8)));
typedef __bf16 bf16x2 __attribute__((ext_vector_type(2)));
typedef short s16x4 __attribute__((ext_vector_type(4)));
#define LAS __attribute__((address_space(3)))

constexpr int H = 32, S = 2048, D = 192, M = 16;
constexpr int CH = 256;
constexpr int NCH = S / CH;
constexpr u32 TBYTES = H * S * D * 2;
constexpr u32 CG_BYTES = 2304;
constexpr u32 PART_BYTES = 3 * CG_BYTES;
constexpr u32 FLAG_OFF = NCH * PART_BYTES;
constexpr u32 HINT_OFF = FLAG_OFF + NCH * 256;
constexpr u32 EPOCH_OFF = HINT_OFF + 256;
constexpr u32 HEAD_BYTES = 57 * 1024;

__device__ __forceinline__ u64 rt() { u64 t; __builtin_amdgcn_sched_barrier(0); asm volatile("s_memrealtime %0\n\ts_waitcnt lgkmcnt(0)" : "=s"(t) :: "memory"); __builtin_amdgcn_sched_barrier(0); return t; }
#define STAMP(i) if (i == SEL) tx = rt();

__device__ __forceinline__ float lo_f(u32 x) { return __uint_as_float(x << 16); }
__device__ __forceinline__ float hi_f(u32 x) { return __uint_as_float(x & 0xffff0000u); }
__device__ __forceinline__ u32 pk_bf16(float a, float b) {
  f32x2 v = {a, b};
  bf16x2 r = __builtin_convertvector(v, bf16x2);
  return __builtin_bit_cast(u32, r);
}
__device__ __forceinline__ float rbf(float a) { return lo_f(pk_bf16(a, 0.f)); }
__device__ __forceinline__ f32x4 mfma32(u32x4 a, u32x4 b, f32x4 c) {
  return __builtin_amdgcn_mfma_f32_16x16x32_bf16(__builtin_bit_cast(bf16x8, a), __builtin_bit_cast(bf16x8, b), c, 0, 0, 0);
}
__device__ __forceinline__ f32x4 mfma16(u32x2 a, u32x2 b, f32x4 c) {
  return __builtin_amdgcn_mfma_f32_16x16x16bf16_1k(__builtin_bit_cast(s16x4, a), __builtin_bit_cast(s16x4, b), c, 0, 0, 0);
}
template <bool E>
__device__ __forceinline__ u32x4 scale_frag(u32x4 x, float scale, float& ss) {
  u32x4 r;
#pragma unroll
  for (int i = 0; i < 4; ++i) {
    u32 p = E ? pk_bf16(lo_f(x[i]) * scale, hi_f(x[i]) * scale) : x[i];
    r[i] = p;
    ss = __builtin_amdgcn_fdot2_f32_bf16(__builtin_bit_cast(bf16x2, p), __builtin_bit_cast(bf16x2, p), ss, false);
  }
  return r;
}
template <bool E>
__device__ __forceinline__ u32 phi2(float p0, float s0, float p1, float s1, float scale) {
  if (E) {
  u32 pp = pk_bf16(p0, p1), sp = pk_bf16(s0, s1);
  u32 tp = pk_bf16(fmaf(lo_f(sp), -0.5f, lo_f(pp)), fmaf(hi_f(sp), -0.5f, hi_f(pp)));
  u32 ep = pk_bf16(__expf(lo_f(tp)), __expf(hi_f(tp)));
  return pk_bf16(fmaf(lo_f(ep), 0.25f, 1e-6f), fmaf(hi_f(ep), 0.25f, 1e-6f));
  } else {
  const float sl = scale * 1.44269504f, hs = -0.5f * scale * sl;
  return pk_bf16(fmaf(__builtin_amdgcn_exp2f(fmaf(p0, sl, s0 * hs)), 0.25f, 1e-6f), fmaf(__builtin_amdgcn_exp2f(fmaf(p1, sl, s1 * hs)), 0.25f, 1e-6f));
  }
}
__device__ __forceinline__ float swap1(float x) {
  return __builtin_bit_cast(float, __builtin_amdgcn_update_dpp(0, __builtin_bit_cast(int, x), 0xB1, 0xf, 0xf, true));
}
__device__ __forceinline__ void lds_signal(LAS u32* p) {
  asm volatile("s_waitcnt lgkmcnt(0)" ::: "memory");
  __hip_atomic_fetch_add(p, 1u, __ATOMIC_RELAXED, __HIP_MEMORY_SCOPE_WORKGROUP);
}
__device__ __forceinline__ void lds_wait(LAS u32* p, u32 want) {
  while (__builtin_amdgcn_readfirstlane(__hip_atomic_load(p, __ATOMIC_RELAXED, __HIP_MEMORY_SCOPE_WORKGROUP)) < want)
    __builtin_amdgcn_s_sleep(1);
  asm volatile("" ::: "memory");
}
__device__ __forceinline__ void lds_spin(LAS u32* p, u32 want) {
  while (__builtin_amdgcn_readfirstlane(__hip_atomic_load(p, __ATOMIC_RELAXED, __HIP_MEMORY_SCOPE_WORKGROUP)) < want) {}
  asm volatile("" ::: "memory");
}
__device__ __forceinline__ u32x4 tag_pack(f32x4 v, u32x4 tagv) {
  return ((__builtin_bit_cast(u32x4, v) + 0x80u) & 0xffffff00u) | tagv;
}

__device__ __forceinline__ u32 xor3(u32 a, u32 b, u32 c) { return __builtin_amdgcn_bitop3_b32(a, b, c, 0x96); }
__device__ __forceinline__ u32 xor9(u32x4 a, u32x4 b, u32 c) {
  return xor3(xor3(a[0], a[1], a[2]), xor3(a[3], b[0], b[1]), xor3(b[2], b[3], c));
}
struct __attribute__((aligned(16))) Smem {
  u32x4 om[6][64];
  u32x4 phik[8][64];
  u32x2 kv[48 * 17];
  u32x2 z[4];
  u32 tag;
  u32 pad[3];
  u32 cnt[8];
};
#define CNT(i) ((LAS u32*)&sm.cnt[i])
__device__ __forceinline__ void lds_add(LAS u32* p) {
  __hip_atomic_fetch_add(p, 1u, __ATOMIC_RELAXED, __HIP_MEMORY_SCOPE_WORKGROUP);
}

__global__ __launch_bounds__(768) void rfa_fused(const u32* __restrict__ Q, const u32* __restrict__ K, const u32* __restrict__ V,
                                                 const u32* __restrict__ Om, u32* __restrict__ O, u32* ws, float scale) {
  __shared__ Smem sm;
  const u64 st0 = rt();
  u64 tx = st0;
  STAMP(0)
  const int b = blockIdx.x;
  const int h = (b & 7) * 4 + (b >> 6), j = (b >> 3) & 7;
  const int w = __builtin_amdgcn_readfirstlane(threadIdx.x >> 6);
  const int l = threadIdx.x & 63, g = l >> 4, c = l & 15;
  const u32 rowb = h * S + j * CH;
  if (w >= 1 && w <= 8) {
    const int wa = w - 1;
    const u32 row0 = rowb + wa * 32;
    const u32 off_frag = (row0 + (c >> 1)) * 384 + (2 * g + (c & 1)) * 16;
    auto rk = __builtin_amdgcn_make_buffer_rsrc((void*)K, 0, TBYTES, 0x00020000);
    auto rq = __builtin_amdgcn_make_buffer_rsrc((void*)Q, 0, TBYTES, 0x00020000);
    auto ro = __builtin_amdgcn_make_buffer_rsrc((void*)O, 0, TBYTES, 0x00020000);
    u32x4 om[3][2], xf[12];
    if (wa >= 4) __builtin_amdgcn_s_barrier();
    __builtin_amdgcn_sched_barrier(0);
#pragma unroll
    for (int n = 0; n < 12; ++n) xf[n] = __builtin_amdgcn_raw_buffer_load_b128(rk, off_frag + (n % 3) * 128, (n / 3) * 3072, LDAUX);
    __builtin_amdgcn_sched_barrier(0);
    if (wa < 4) __builtin_amdgcn_s_barrier();
    else if (l == 0) lds_add(CNT(0));
    STAMP(1)
    lds_wait(CNT(1), 1);
#pragma unroll
    for (int i = 0; i < 3; ++i)
#pragma unroll
      for (int p = 0; p < 2; ++p) om[i][p] = sm.om[2 * i + p][l];
    {
      u32x4 phiT;
#pragma unroll
      for (int a = 0; a < 4; ++a) {
        float ss = 0.f;
        f32x4 d0 = {0, 0, 0, 0}, d1 = {0, 0, 0, 0};
#pragma unroll
        for (int i = 0; i < 3; ++i) {
          u32x4 ks = scale_frag<(EMU & 1) != 0>(xf[3 * a + i], scale, ss);
          d0 = mfma32(ks, om[i][0], d0);
          d1 = mfma32(ks, om[i][1], d1);
        }
        f32x4 sq = __builtin_amdgcn_mfma_f32_16x16x4f32(ss, 1.0f, (f32x4){0, 0, 0, 0}, 0, 0, 0);
        phiT[a] = phi2<(EMU & 1) != 0>(d0[0] + d1[1], sq[0] + sq[1], d0[2] + d1[3], sq[2] + sq[3], scale);
      }
      sm.phik[wa][l] = phiT;
    }
    if (l == 0) lds_signal(CNT(2));
    STAMP(2)
    if (!STAGGER || wa < 4) lds_wait(CNT(0), 7);
    else lds_wait(CNT(6), 4);
    __builtin_amdgcn_sched_barrier(0);
#pragma unroll
    for (int n = 0; n < WQ; ++n) xf[n] = __builtin_amdgcn_raw_buffer_load_b128(rq, off_frag + (n % 3) * 128, (n / 3) * 3072, LDAUX);
    __builtin_amdgcn_sched_barrier(0);
    if (WQ >= 12 && l == 0) lds_add(CNT(3));
    STAMP(3)
    const bool odd = c & 1;
    u32x2 phq[2];
#pragma unroll
    for (int u = 0; u < 2; ++u) {
      float ss[2];
      f32x4 d[2][2];
#pragma unroll
      for (int a = 0; a < 2; ++a) {
        ss[a] = 0.f;
        d[a][0] = (f32x4){0, 0, 0, 0};
        d[a][1] = (f32x4){0, 0, 0, 0};
#pragma unroll
        for (int i = 0; i < 3; ++i) {
          const int n = 6 * u + 3 * a + i;
          if (n + WQ < 12) {
            asm volatile("" : "+v"(xf[n]));
            __builtin_amdgcn_sched_barrier(0);
            xf[n + WQ] = __builtin_amdgcn_raw_buffer_load_b128(rq, off_frag + ((n + WQ) % 3) * 128, ((n + WQ) / 3) * 3072, LDAUX);
            __builtin_amdgcn_sched_barrier(0);
            if (n + WQ == 11 && l == 0) lds_add(CNT(3));
          }
          if (STAGGER && n == HANDOVER) {
            asm volatile("" : "+v"(xf[n]));
            if (l == 0) lds_add(CNT(6));
          }
          u32x4 qs = scale_frag<(EMU & 2) != 0>(xf[n], scale, ss[a]);
          d[a][0] = mfma32(om[i][0], qs, d[a][0]);
          d[a][1] = mfma32(om[i][1], qs, d[a][1]);
        }
      }
      const float st = (odd ? ss[1] : ss[0]) + swap1(odd ? ss[0] : ss[1]);
      f32x4 sq = __builtin_amdgcn_mfma_f32_16x16x4f32(1.0f, st, (f32x4){0, 0, 0, 0}, 0, 0, 0);
      float pr[4];
#pragma unroll
      for (int r = 0; r < 4; ++r) pr[r] = (odd ? d[1][1][r] : d[0][0][r]) + swap1(odd ? d[0][1][r] : d[1][0][r]);
      phq[u][0] = phi2<(EMU & 2) != 0>(pr[0], sq[0], pr[1], sq[0], scale);
      phq[u][1] = phi2<(EMU & 2) != 0>(pr[2], sq[0], pr[3], sq[0], scale);
    }
    asm volatile("" : "+v"(phq[0]), "+v"(phq[1]));
    STAMP(4)
    lds_wait(CNT(7), 1);
    f32x4 inv[2];
    {
      const u32x2 zf = sm.z[g];
#pragma unroll
      for (int u = 0; u < 2; ++u) {
        f32x4 den = mfma16(phq[u], zf, (f32x4){0, 0, 0, 0});
#pragma unroll
        for (int r = 0; r < 4; ++r) inv[u][r] = __builtin_amdgcn_rcpf(rbf(rbf(den[r]) + 1e-6f));
      }
    }
    asm volatile("" : "+v"(inv[0]), "+v"(inv[1]));
    const u32 off_row = (row0 + 2 * g) * 384;
    lds_spin(CNT(6), 1);
    STAMP(5)
    {
      u32x2 kvf[4];
#pragma unroll
      for (int t = 0; t < 4; ++t) kvf[t] = sm.kv[4 * (128 + 4 * c + t) + g + 32 + c];
#pragma unroll
      for (int u = 0; u < 2; ++u) {
        f32x4 num[4];
#pragma unroll
        for (int t = 0; t < 4; ++t) num[t] = mfma16(phq[u], kvf[t], (f32x4){0, 0, 0, 0}) * inv[u];
#pragma unroll
        for (int r = 0; r < 4; ++r)
          __builtin_amdgcn_raw_buffer_store_b64((u32x2){pk_bf16(num[0][r], num[1][r]), pk_bf16(num[2][r], num[3][r])}, ro,
                                                off_row + 8 * c + 256 + (r >> 1) * 384, (16 * u + 8 * (r & 1)) * 384, STAUX);
      }
    }
    lds_spin(CNT(5), 2);
    {
      u32x2 kvf[8];
#pragma unroll
      for (int t = 0; t < 8; ++t) kvf[t] = sm.kv[4 * (8 * c + t) + g + 2 * c + (t >> 2)];
#pragma unroll
      for (int u = 0; u < 2; ++u) {
        f32x4 num[8];
#pragma unroll
        for (int t = 0; t < 8; ++t) num[t] = mfma16(phq[u], kvf[t], (f32x4){0, 0, 0, 0}) * inv[u];
#pragma unroll
        for (int r = 0; r < 4; ++r)
          __builtin_amdgcn_raw_buffer_store_b128((u32x4){pk_bf16(num[0][r], num[1][r]), pk_bf16(num[2][r], num[3][r]),
                                                         pk_bf16(num[4][r], num[5][r]), pk_bf16(num[6][r], num[7][r])}, ro,
                                                 off_row + 16 * c + (r >> 1) * 384, (16 * u + 8 * (r & 1)) * 384, STAUX);
      }
    }
    STAMP(6)
    asm volatile("s_waitcnt vmcnt(0)" ::: "memory");
    STAMP(7)
  } else {
    auto rw = __builtin_amdgcn_make_buffer_rsrc((char*)ws + h * HEAD_BYTES, 0, HEAD_BYTES, 0x00020000);
    if (w > 8) {
      const int cg = w - 9;
      auto rv = __builtin_amdgcn_make_buffer_rsrc((void*)V, 0, TBYTES, 0x00020000);
      const u32 voff = (rowb + 2 * g) * 384 + cg * 128 + 8 * c;
      __builtin_amdgcn_s_barrier();
      lds_wait(CNT(0), 4);
      __builtin_amdgcn_sched_barrier(0);
      STAMP(1)
      u32x2 vf[8][4][2];
#pragma unroll
      for (int s = 0; s < 8; ++s)
#pragma unroll
        for (int a = 0; a < 4; ++a)
#pragma unroll
          for (int bb = 0; bb < 2; ++bb)
            vf[s][a][bb] = __builtin_amdgcn_raw_buffer_load_b64(rv, voff + bb * 384, (s * 4 + a) * 3072, LDAUX);
      __builtin_amdgcn_sched_barrier(0);
      if (l == 0) lds_add(CNT(0));
      __builtin_amdgcn_s_setprio(3);
      STAMP(2)
      lds_wait(CNT(2), 9);
      f32x4 acc[4];
#pragma unroll
      for (int t = 0; t < 4; ++t) acc[t] = (f32x4){0, 0, 0, 0};
#pragma unroll
      for (int s = 0; s < 8; ++s) {
        const u32x4 phiT = sm.phik[s][l];
#pragma unroll
        for (int t = 0; t < 4; ++t) {
          u32x4 bf;
          const u32 sel = (t & 1) ? 0x07060302u : 0x05040100u;
#pragma unroll
          for (int a = 0; a < 4; ++a) bf[a] = __builtin_amdgcn_perm(vf[s][a][1][t >> 1], vf[s][a][0][t >> 1], sel);
          acc[t] = mfma32(phiT, bf, acc[t]);
        }
      }
      STAMP(3)
      const u32 tag = sm.tag;
      u32x4 p0, p1;
      {
        p0 = (u32x4){pk_bf16(acc[0][0], acc[0][1]), pk_bf16(acc[0][2], acc[0][3]), pk_bf16(acc[1][0], acc[1][1]), pk_bf16(acc[1][2], acc[1][3])};
        p1 = (u32x4){pk_bf16(acc[2][0], acc[2][1]), pk_bf16(acc[2][2], acc[2][3]), pk_bf16(acc[3][0], acc[3][1]), pk_bf16(acc[3][2], acc[3][3])};
        const u32 chk = xor9(p0, p1, tag);
        const u32 so = j * PART_BYTES + cg * CG_BYTES;
        __builtin_amdgcn_raw_buffer_store_b128(p0, rw, l * 16, so, 16);
        __builtin_amdgcn_raw_buffer_store_b128(p1, rw, l * 16 + 1024, so, 16);
        u32x4 cw;
#pragma unroll
        for (int e = 0; e < 4; ++e) cw[e] = __builtin_amdgcn_ds_bpermute(((4 * l + e) & 63) * 4, chk);
        if (l < 16) __builtin_amdgcn_raw_buffer_store_b128(cw, rw, l * 16 + 2048, so, 16);
      }
      STAMP(4)
      u32x4 sel[2];
      {
        const u32 one = (c & 1) ? 0x3f800000u : 0x00003f80u;
#pragma unroll
        for (int hf = 0; hf < 2; ++hf)
#pragma unroll
          for (int e = 0; e < 4; ++e) sel[hf][e] = (g == 2 * hf + (c >> 3) && ((c & 7) >> 1) == e) ? one : 0u;
      }
      f32x4 own[2][2];
#pragma unroll
      for (int hf = 0; hf < 2; ++hf) {
        own[0][hf] = mfma32(sel[hf], p0, (f32x4){0, 0, 0, 0});
        own[1][hf] = mfma32(sel[hf], p1, (f32x4){0, 0, 0, 0});
      }
      lds_wait(CNT(4), 1);
      lds_wait(CNT(3), cg == 2 ? QG2 : QG01);
      STAMP(5)
      int spin = 0;
      f32x4 d[2][2];
      for (; spin < (1 << 18); ++spin) {
        u32x4 x[7][2];
        u32 xc[7];
#pragma unroll
        for (int i = 0; i < 7; ++i) {
          const u32 pb = ((j + 1 + i) & 7) * PART_BYTES + cg * CG_BYTES;
          x[i][0] = __builtin_amdgcn_raw_buffer_load_b128(rw, l * 16, pb, 16);
          x[i][1] = __builtin_amdgcn_raw_buffer_load_b128(rw, l * 16 + 1024, pb, 16);
          xc[i] = __builtin_amdgcn_raw_buffer_load_b32(rw, l * 4 + 2048, pb, 16);
        }
        __builtin_amdgcn_sched_barrier(0);
        u32 bad = 0;
#pragma unroll
        for (int s2 = 0; s2 < 2; ++s2)
#pragma unroll
          for (int hf = 0; hf < 2; ++hf) d[s2][hf] = own[s2][hf];
#pragma unroll
        for (int i = 0; i < 7; ++i) {
#pragma unroll
          for (int s2 = 0; s2 < 2; ++s2)
#pragma unroll
            for (int hf = 0; hf < 2; ++hf) d[s2][hf] = mfma32(sel[hf], x[i][s2], d[s2][hf]);
          bad |= xor9(x[i][0], x[i][1], xc[i]) ^ tag;
        }
        if (__all(bad == 0)) break;
        __builtin_amdgcn_s_sleep(POLLSLEEP);
        asm volatile("" ::: "memory");
      }
#pragma unroll
      for (int s2 = 0; s2 < 2; ++s2)
#pragma unroll
        for (int hf = 0; hf < 2; ++hf)
          sm.kv[4 * (64 * cg + 4 * c + 2 * s2 + (g & 1)) + 2 * hf + (g >> 1) + 16 * cg + c] =
              (u32x2){pk_bf16(d[s2][hf][0], d[s2][hf][1]), pk_bf16(d[s2][hf][2], d[s2][hf][3])};
      if (l == 0) lds_signal(cg == 2 ? CNT(6) : CNT(5));
      STAMP(6)
    } else {
      __builtin_amdgcn_s_setprio(3);
      auto rom = __builtin_amdgcn_make_buffer_rsrc((void*)Om, 0, M * D * 2, 0x00020000);
      u32x4 om[6];
#pragma unroll
      for (int i = 0; i < 6; ++i) om[i] = __builtin_amdgcn_raw_buffer_load_b128(rom, c * 384 + 2 * g * 16, (i >> 1) * 128 + (i & 1) * 16, 0);
      u32 vz;
      asm volatile("v_mov_b32 %0, 0" : "=v"(vz));
      const u32 epoch = __builtin_amdgcn_raw_buffer_load_b32(rw, vz, EPOCH_OFF, 16);
      u32 pf = 0;
      {
        const char* base = g == 0 ? (const char*)Q : g == 1 ? (const char*)O : (const char*)V;
        const char* p = base + rowb * 384 + (c < 2 ? c * 98300 : 49152);
        if (g == 3) p = (const char*)ws + h * HEAD_BYTES;
        pf = __builtin_nontemporal_load((const u32*)p);
      }
      if (l < 8) sm.cnt[l] = 0;
      asm volatile("s_waitcnt lgkmcnt(0)" ::: "memory");
      __builtin_amdgcn_s_barrier();
      asm volatile("" ::: "memory");
#pragma unroll
      for (int i = 0; i < 6; ++i) sm.om[i][l] = om[i];
      if (l == 0) lds_signal(CNT(1));
      STAMP(1)
      const u32 tag = epoch + 1u;
      if (l == 0) sm.tag = tag;
      if (l == 0) lds_signal(CNT(2));
      asm volatile("" :: "v"(pf));
      STAMP(2)
      lds_wait(CNT(2), 9);
      f32x4 acc = {0, 0, 0, 0};
      const u32x4 ones = {0x3f803f80u, 0x3f803f80u, 0x3f803f80u, 0x3f803f80u};
#pragma unroll
      for (int s = 0; s < 8; ++s) acc = mfma32(ones, sm.phik[s][l], acc);
      asm volatile("" : "+v"(acc));
      lds_wait(CNT(0), 7);
      __builtin_amdgcn_s_setprio(ZPRIO);
      STAMP(3)
      if (g == 0) __builtin_amdgcn_raw_buffer_store_b128((u32x4){tag, __float_as_uint(acc[0]), tag, __float_as_uint(acc[0])}, rw, c * 16, FLAG_OFF + j * 256, 16);
      u32* hint = (u32*)((char*)ws + h * HEAD_BYTES + HINT_OFF);
      if (l == 0) asm volatile("global_atomic_swap %0, %1, off" :: "v"(hint + j), "v"(tag) : "memory");
      int spin;
      u32x2 xa, xb;
      {
        u32 r0, r1, r2, r3;
        u64 sv;
        const u32* hp = hint + (l & 7);
        const u32* zp = (const u32*)((char*)ws + h * HEAD_BYTES + FLAG_OFF) + l * 4;
        const u32 goaddr = (u32)(size_t)CNT(4), one = 1u;
#define STR2(x) #x
#define STR(x) STR2(x)
#define HP(r) "global_atomic_add " r ", %[hp], %[vz], off sc0\n\t"
#define HC(r) "s_waitcnt vmcnt(3)\n\tv_cmp_eq_u32 vcc, " r ", %[tag]\n\ts_cmp_eq_u64 vcc, exec\n\ts_cbranch_scc1 2f\n\t"
        asm volatile("s_mov_b64 %[sv], exec\n\t"
                     "s_mov_b64 exec, 0xff\n\t"
                     "s_mov_b32 %[spin], 0\n\t"
                     HP("%[r0]") "s_sleep " STR(HSLEEP) "\n\t"
                     HP("%[r1]") "s_sleep " STR(HSLEEP) "\n\t"
                     HP("%[r2]") "s_sleep " STR(HSLEEP) "\n"
                     "1:\n\t"
                     HP("%[r3]") HC("%[r0]")
                     HP("%[r0]") HC("%[r1]")
                     HP("%[r1]") HC("%[r2]")
                     HP("%[r2]") HC("%[r3]")
                     "s_add_u32 %[spin], %[spin], 1\n\t"
                     "s_cmp_lt_u32 %[spin], 0x4000\n\t"
                     "s_cbranch_scc1 1b\n"
                     "2:\n\t"
                     "s_sleep " STR(GODELAY) "\n\t"
                     "ds_add_u32 %[go], %[one]\n\t"
                     "s_mov_b64 exec, %[sv]\n\t"
                     "global_load_dwordx2 %[xa], %[zp], off sc1\n\t"
                     "global_load_dwordx2 %[xb], %[zp], off offset:1024 sc1\n\t"
                     "s_waitcnt vmcnt(0) lgkmcnt(0)"
                     : [r0] "=&v"(r0), [r1] "=&v"(r1), [r2] "=&v"(r2), [r3] "=&v"(r3), [spin] "=&s"(spin), [sv] "=&s"(sv),
                       [xa] "=&v"(xa), [xb] "=&v"(xb)
                     : [hp] "v"(hp), [vz] "v"(vz), [tag] "v"(tag), [go] "v"(goaddr), [one] "v"(one), [zp] "v"(zp)
                     : "vcc", "scc", "memory");
      }
      STAMP(4)
      for (int sp2 = 0; sp2 < (1 << 18); ++sp2) {
        if (__all(xa[0] == tag && xb[0] == tag)) break;
        __builtin_amdgcn_s_sleep(POLLSLEEP);
        asm volatile("" ::: "memory");
        xa = __builtin_amdgcn_raw_buffer_load_b64(rw, l * 16, FLAG_OFF, 16);
        xb = __builtin_amdgcn_raw_buffer_load_b64(rw, l * 16 + 1024, FLAG_OFF, 16);
      }
      STAMP(5)
      float z0 = __uint_as_float(xa[1]) + __uint_as_float(xb[1]);
      z0 += __shfl_xor(z0, 16);
      z0 += __shfl_xor(z0, 32);
      if (l < 16) ((unsigned short*)sm.z)[l] = (unsigned short)pk_bf16(z0, 0.f);
      if (l == 0) lds_signal(CNT(7));
      STAMP(6)
      if (j == 0) {
        lds_wait(CNT(5), 2);
        if (l == 0) __builtin_amdgcn_raw_buffer_store_b32(tag, rw, 0, EPOCH_OFF, 16);
      }
    }
  }
  {
    const int role = w == 0 ? 2 : w <= 8 ? 0 : 1;
    if (role == SELROLE && (SELB < 0 || b == SELB)) {
      const u64 target = st0 + 6000 + (tx - st0);
      while (rt() < target) __builtin_amdgcn_s_sleep(1);
    }
  }
}

extern "C" void kernel_launch(void* const* d_in, const int* in_sizes, int n_in, void* d_out, int out_size,
                              void* d_ws, size_t ws_size, hipStream_t stream) {
  const float scale = 0.26864248f;
  rfa_fused<<<H * NCH, 768, 0, stream>>>((const u32*)d_in[0], (const u32*)d_in[1], (const u32*)d_in[2], (const u32*)d_in[3],
                                        (u32*)d_out, (u32*)d_ws, scale);
}
```

```cpp
#define EVDLYQ 28
#include <hip/hip_runtime.h>
#include <stdint.h>

#define LDAUX 2
#define STAUX 2
#define STA2 STAUX
#define STA0 STAUX
#define STA1 STAUX
#define EVDLY 0
#define EVDLYS 0
#define AFIRST 0
#define EMU 1
#define TLBPF 1
#define WQ 4
#define QMID 2
#define QLATE (11 - WQ)
#define EARLY0 0
#define XMAP 0
#define ABL 0
#define POLLSLEEP 8

typedef unsigned u32;
typedef unsigned long long u64;
typedef u32 u32x4 __attribute__((ext_vector_type(4)));
typedef u32 u32x2 __attribute__((ext_vector_type(2)));
typedef float f32x4 __attribute__((ext_vector_type(4)));
typedef float f32x2 __attribute__((ext_vector_type(2)));
typedef __bf16 bf16x8 __attribute__((ext_vector_type(8)));
typedef __bf16 bf16x2 __attribute__((ext_vector_type(2)));
typedef short s16x4 __attribute__((ext_vector_type(4)));
#define LAS __attribute__((address_space(3)))

constexpr int H = 32, S = 2048, D = 192, M = 16;
constexpr int CH = 256;
constexpr int NCH = S / CH;
constexpr u32 TBYTES = H * S * D * 2;
constexpr u32 CG_BYTES = 2304;
constexpr u32 PART_BYTES = 3 * CG_BYTES + 256;
constexpr u32 EPOCH_OFF = NCH * PART_BYTES;
constexpr u32 HEAD_BYTES = EPOCH_OFF + 256;

#define STAMP(i)

__device__ __forceinline__ float lo_f(u32 x) { return __uint_as_float(x << 16); }
__device__ __forceinline__ float hi_f(u32 x) { return __uint_as_float(x & 0xffff0000u); }
__device__ __forceinline__ u32 pk_bf16(float a, float b) {
  f32x2 v = {a, b};
  bf16x2 r = __builtin_convertvector(v, bf16x2);
  return __builtin_bit_cast(u32, r);
}
__device__ __forceinline__ float rbf(float a) { return lo_f(pk_bf16(a, 0.f)); }
__device__ __forceinline__ f32x4 mfma32(u32x4 a, u32x4 b, f32x4 c) {
  return __builtin_amdgcn_mfma_f32_16x16x32_bf16(__builtin_bit_cast(bf16x8, a), __builtin_bit_cast(bf16x8, b), c, 0, 0, 0);
}
__device__ __forceinline__ f32x4 mfma16(u32x2 a, u32x2 b, f32x4 c) {
  return __builtin_amdgcn_mfma_f32_16x16x16bf16_1k(__builtin_bit_cast(s16x4, a), __builtin_bit_cast(s16x4, b), c, 0, 0, 0);
}
template <bool E>
__device__ __forceinline__ u32x4 scale_frag(u32x4 x, float scale, float& ss) {
  u32x4 r;
#pragma unroll
  for (int i = 0; i < 4; ++i) {
    u32 p = E ? pk_bf16(lo_f(x[i]) * scale, hi_f(x[i]) * scale) : x[i];
    r[i] = p;
    ss = __builtin_amdgcn_fdot2_f32_bf16(__builtin_bit_cast(bf16x2, p), __builtin_bit_cast(bf16x2, p), ss, false);
  }
  return r;
}
template <bool E>
__device__ __forceinline__ u32 phi2(float p0, float s0, float p1, float s1, float scale) {
  if (E) {
    u32 pp = pk_bf16(p0, p1), sp = pk_bf16(s0, s1);
    u32 tp = pk_bf16(fmaf(lo_f(sp), -0.5f, lo_f(pp)), fmaf(hi_f(sp), -0.5f, hi_f(pp)));
    u32 ep = pk_bf16(__expf(lo_f(tp)), __expf(hi_f(tp)));
    return pk_bf16(fmaf(lo_f(ep), 0.25f, 1e-6f), fmaf(hi_f(ep), 0.25f, 1e-6f));
  } else {
    const float sl = scale * 1.44269504f, hs = -0.5f * scale * sl;
    return pk_bf16(fmaf(__builtin_amdgcn_exp2f(fmaf(p0, sl, s0 * hs)), 0.25f, 1e-6f),
                   fmaf(__builtin_amdgcn_exp2f(fmaf(p1, sl, s1 * hs)), 0.25f, 1e-6f));
  }
}
__device__ __forceinline__ float swap1(float x) {
  return __builtin_bit_cast(float, __builtin_amdgcn_update_dpp(0, __builtin_bit_cast(int, x), 0xB1, 0xf, 0xf, true));
}
__device__ __forceinline__ void lds_add(LAS u32* p) {
  __hip_atomic_fetch_add(p, 1u, __ATOMIC_RELAXED, __HIP_MEMORY_SCOPE_WORKGROUP);
}
__device__ __forceinline__ void lds_signal(LAS u32* p) {
  asm volatile("s_waitcnt lgkmcnt(0)" ::: "memory");
  lds_add(p);
}
__device__ __forceinline__ void lds_wait(LAS u32* p, u32 want) {
  while (__builtin_amdgcn_readfirstlane(__hip_atomic_load(p, __ATOMIC_RELAXED, __HIP_MEMORY_SCOPE_WORKGROUP)) < want)
    __builtin_amdgcn_s_sleep(1);
  asm volatile("" ::: "memory");
}
__device__ __forceinline__ void lds_spin(LAS u32* p, u32 want) {
  while (__builtin_amdgcn_readfirstlane(__hip_atomic_load(p, __ATOMIC_RELAXED, __HIP_MEMORY_SCOPE_WORKGROUP)) < want) {}
  asm volatile("" ::: "memory");
}
__device__ __forceinline__ u32 xor3(u32 a, u32 b, u32 c) { return __builtin_amdgcn_bitop3_b32(a, b, c, 0x96); }
__device__ __forceinline__ u32 xor9(u32x4 a, u32x4 b, u32 c) {
  return xor3(xor3(a[0], a[1], a[2]), xor3(a[3], b[0], b[1]), xor3(b[2], b[3], c));
}

struct __attribute__((aligned(16))) Smem {
  u32x4 om[6][64];
  u32x4 phik[8][64];
  u32x2 kv[48 * 17];
  u32x2 z[4];
  u32 tag;
  u32 pad[3];
  u32 cnt[8];
};
#define CNT(i) ((LAS u32*)&sm.cnt[i])

__global__ __launch_bounds__(704) void rfa_fused(const u32* __restrict__ Q, const u32* __restrict__ K, const u32* __restrict__ V,
                                                 const u32* __restrict__ Om, u32* __restrict__ O, u32* ws, float scale) {
  __shared__ Smem sm;
  STAMP(0)
  const int b = blockIdx.x;
  const int h = (b & 7) * 4 + (b >> 6), j = (b >> 3) & 7;
  const int w = __builtin_amdgcn_readfirstlane(threadIdx.x >> 6);
  const int l = threadIdx.x & 63, g = l >> 4, c = l & 15;
  const u32 rowb = h * S + j * CH;
  if (w >= 3) {
    const int wa = w - 3;
    const u32 row0 = rowb + wa * 32;
    const u32 off_frag = (row0 + (c >> 1)) * 384 + (2 * g + (c & 1)) * 16;
    auto rk = __builtin_amdgcn_make_buffer_rsrc((void*)K, 0, TBYTES, 0x00020000);
    auto rq = __builtin_amdgcn_make_buffer_rsrc((void*)Q, 0, TBYTES, 0x00020000);
    auto ro = __builtin_amdgcn_make_buffer_rsrc((void*)O, 0, TBYTES, 0x00020000);
    u32x4 om[3][2], xf[12];
    if (wa >= 4) __builtin_amdgcn_s_barrier();
    if (EVDLY && wa < 4 && !(b & 1)) __builtin_amdgcn_s_sleep(EVDLY);
    __builtin_amdgcn_sched_barrier(0);
#pragma unroll
    for (int n = 0; n < 12; ++n) xf[n] = __builtin_amdgcn_raw_buffer_load_b128(rk, off_frag + (n % 3) * 128, (n / 3) * 3072, LDAUX);
    __builtin_amdgcn_sched_barrier(0);
    if (wa < 4) __builtin_amdgcn_s_barrier();
    else if (l == 0) lds_add(CNT(0));
    STAMP(1)
    lds_wait(CNT(1), 1);
#pragma unroll
    for (int i = 0; i < 3; ++i)
#pragma unroll
      for (int p = 0; p < 2; ++p) om[i][p] = sm.om[2 * i + p][l];
    {
      u32x4 phiT;
#pragma unroll
      for (int a = 0; a < 4; ++a) {
        float ss = 0.f;
        f32x4 d0 = {0, 0, 0, 0}, d1 = {0, 0, 0, 0};
#pragma unroll
        for (int i = 0; i < 3; ++i) {
          u32x4 ks = scale_frag<(EMU & 1) != 0>(xf[3 * a + i], scale, ss);
          d0 = mfma32(ks, om[i][0], d0);
          d1 = mfma32(ks, om[i][1], d1);
        }
        f32x4 sq = __builtin_amdgcn_mfma_f32_16x16x4f32(ss, 1.0f, (f32x4){0, 0, 0, 0}, 0, 0, 0);
        phiT[a] = phi2<(EMU & 1) != 0>(d0[0] + d1[1], sq[0] + sq[1], d0[2] + d1[3], sq[2] + sq[3], scale);
      }
      sm.phik[wa][l] = phiT;
    }
    if (l == 0) lds_signal(CNT(2));
    STAMP(2)
    lds_wait(CNT(0), 7);
    if (EVDLYQ && !(__builtin_amdgcn_s_getreg(20) & 1)) __builtin_amdgcn_s_sleep(EVDLYQ);
    __builtin_amdgcn_sched_barrier(0);
#pragma unroll
    for (int n = 0; n < WQ; ++n) {
      xf[n] = __builtin_amdgcn_raw_buffer_load_b128(rq, off_frag + (n % 3) * 128, (n / 3) * 3072, LDAUX);
      __builtin_amdgcn_sched_barrier(0);
    }
    STAMP(3)
    const bool odd = c & 1;
    const u32 off_row = (row0 + 2 * g) * 384;
    u32x2 phq[2], kv2[4], zf;
    f32x4 inv[2];
    auto part2 = [&](int u) {
      const f32x4 den = mfma16(phq[u], zf, (f32x4){0, 0, 0, 0});
      f32x4 num[4];
#pragma unroll
      for (int t = 0; t < 4; ++t) num[t] = mfma16(phq[u], kv2[t], (f32x4){0, 0, 0, 0});
#pragma unroll
      for (int r = 0; r < 4; ++r) inv[u][r] = __builtin_amdgcn_rcpf(rbf(rbf(den[r]) + 1e-6f));
#pragma unroll
      for (int t = 0; t < 4; ++t) num[t] *= inv[u];
#pragma unroll
      for (int r = 0; r < 4; ++r)
        __builtin_amdgcn_raw_buffer_store_b64((u32x2){pk_bf16(num[0][r], num[1][r]), pk_bf16(num[2][r], num[3][r])}, ro,
                                              off_row + 8 * c + 256 + (r >> 1) * 384, (16 * u + 8 * (r & 1)) * 384, STA2);
    };
    auto load_kv2 = [&]() {
      lds_spin(CNT(6), 1);
      zf = sm.z[g];
#pragma unroll
      for (int t = 0; t < 4; ++t) kv2[t] = sm.kv[4 * (128 + 4 * c + t) + g + 32 + c];
    };
#pragma unroll
    for (int u = 0; u < 2; ++u) {
      float ss[2];
      f32x4 d[2][2];
#pragma unroll
      for (int a = 0; a < 2; ++a) {
        ss[a] = 0.f;
        d[a][0] = (f32x4){0, 0, 0, 0};
        d[a][1] = (f32x4){0, 0, 0, 0};
#pragma unroll
        for (int i = 0; i < 3; ++i) {
          const int n = 6 * u + 3 * a + i;
          if (n + WQ < 12) {
            asm volatile("" : "+v"(xf[n]));
            __builtin_amdgcn_sched_barrier(0);
            xf[n + WQ] = __builtin_amdgcn_raw_buffer_load_b128(rq, off_frag + ((n + WQ) % 3) * 128, ((n + WQ) / 3) * 3072, LDAUX);
            __builtin_amdgcn_sched_barrier(0);
          }
          if (n == QMID || n == QLATE) {
            asm volatile("" : "+v"(xf[n]));
            if (n == QMID && l == 0) lds_add(CNT(4));
            if (n == QLATE && l == 0) lds_add(CNT(3));
          }
          if (EARLY0 && n == EARLY0) {
            load_kv2();
            if (EARLY0 < 9) lds_spin(CNT(3), 8);
            part2(0);
          }
          u32x4 qs = scale_frag<(EMU & 2) != 0>(xf[n], scale, ss[a]);
          d[a][0] = mfma32(om[i][0], qs, d[a][0]);
          d[a][1] = mfma32(om[i][1], qs, d[a][1]);
        }
      }
      const float st = (odd ? ss[1] : ss[0]) + swap1(odd ? ss[0] : ss[1]);
      f32x4 sq = __builtin_amdgcn_mfma_f32_16x16x4f32(1.0f, st, (f32x4){0, 0, 0, 0}, 0, 0, 0);
      float pr[4];
#pragma unroll
      for (int r = 0; r < 4; ++r) pr[r] = (odd ? d[1][1][r] : d[0][0][r]) + swap1(odd ? d[0][1][r] : d[1][0][r]);
      phq[u][0] = phi2<(EMU & 2) != 0>(pr[0], sq[0], pr[1], sq[0], scale);
      phq[u][1] = phi2<(EMU & 2) != 0>(pr[2], sq[0], pr[3], sq[0], scale);
    }
    asm volatile("" : "+v"(phq[0]), "+v"(phq[1]));
    STAMP(4)
    if (EVDLYS && !(__builtin_amdgcn_s_getreg(20) & 1)) __builtin_amdgcn_s_sleep(EVDLYS);
    if (!EARLY0) {
      load_kv2();
      part2(0);
    }
    part2(1);
    STAMP(5)
    lds_spin(CNT(5), 2);
    {
      u32x2 kvf[8];
#pragma unroll
      for (int t = 0; t < 8; ++t) kvf[t] = sm.kv[4 * (8 * c + t) + g + 2 * c + (t >> 2)];
#pragma unroll
      for (int u = 0; u < 2; ++u) {
        f32x4 num[8];
#pragma unroll
        for (int t = 0; t < 8; ++t) num[t] = mfma16(phq[u], kvf[t], (f32x4){0, 0, 0, 0}) * inv[u];
#pragma unroll
        for (int r = 0; r < 4; ++r) {
          const u32x4 o = {pk_bf16(num[0][r], num[1][r]), pk_bf16(num[2][r], num[3][r]), pk_bf16(num[4][r], num[5][r]), pk_bf16(num[6][r], num[7][r])};
          if (u == 0) __builtin_amdgcn_raw_buffer_store_b128(o, ro, off_row + 16 * c + (r >> 1) * 384, (16 * u + 8 * (r & 1)) * 384, STA0);
          else __builtin_amdgcn_raw_buffer_store_b128(o, ro, off_row + 16 * c + (r >> 1) * 384, (16 * u + 8 * (r & 1)) * 384, STA1);
        }
      }
    }
    STAMP(6)
    STAMP(7)
  } else {
    const int cg = w;
    auto rw = __builtin_amdgcn_make_buffer_rsrc((char*)ws + h * HEAD_BYTES, 0, HEAD_BYTES, 0x00020000);
    if (cg == 0) {
      auto rom = __builtin_amdgcn_make_buffer_rsrc((void*)Om, 0, M * D * 2, 0x00020000);
      u32x4 om[6];
#pragma unroll
      for (int i = 0; i < 6; ++i) om[i] = __builtin_amdgcn_raw_buffer_load_b128(rom, c * 384 + 2 * g * 16, (i >> 1) * 128 + (i & 1) * 16, 0);
      __builtin_amdgcn_sched_barrier(0);
      __builtin_amdgcn_s_barrier();
      asm volatile("" ::: "memory");
#pragma unroll
      for (int i = 0; i < 6; ++i) sm.om[i][l] = om[i];
      if (l == 0) lds_signal(CNT(1));
    } else if (cg == 1) {
      u32 pf = 0;
      const char* base = g == 0 ? (const char*)Q : g == 1 ? (const char*)O : (const char*)V;
      const char* p = base + rowb * 384 + (c < 2 ? c * 98300 : 49152);
      if (g == 3) p = (const char*)ws + h * HEAD_BYTES;
      pf = __builtin_nontemporal_load((const u32*)p);
      if (l < 8) sm.cnt[l] = 0;
      asm volatile("s_waitcnt lgkmcnt(0)" ::: "memory");
      __builtin_amdgcn_s_barrier();
      asm volatile("" :: "v"(pf));
    } else {
      u32 vz;
      asm volatile("v_mov_b32 %0, 0" : "=v"(vz));
      const u32 epoch = __builtin_amdgcn_raw_buffer_load_b32(rw, vz, EPOCH_OFF, 16);
      __builtin_amdgcn_sched_barrier(0);
      __builtin_amdgcn_s_barrier();
      asm volatile("" ::: "memory");
      if (l == 0) {
        sm.tag = epoch + 1u;
        lds_signal(CNT(2));
      }
    }
    auto rv = __builtin_amdgcn_make_buffer_rsrc((void*)V, 0, TBYTES, 0x00020000);
    const u32 voff = (rowb + 2 * g) * 384 + cg * 128 + 8 * c;
    lds_wait(CNT(0), 4);
    __builtin_amdgcn_sched_barrier(0);
    STAMP(1)
    u32x2 vf[8][4][2];
#pragma unroll
    for (int s = 0; s < 8; ++s)
#pragma unroll
      for (int a = 0; a < 4; ++a)
#pragma unroll
        for (int bb = 0; bb < 2; ++bb)
          vf[s][a][bb] = __builtin_amdgcn_raw_buffer_load_b64(rv, voff + bb * 384, (s * 4 + a) * 3072, LDAUX);
    __builtin_amdgcn_sched_barrier(0);
    if (l == 0) lds_add(CNT(0));
    STAMP(2)
    lds_wait(CNT(2), 9);
    const u32 tag = sm.tag;
    f32x4 acc[4], zacc = {0, 0, 0, 0};
#pragma unroll
    for (int t = 0; t < 4; ++t) acc[t] = (f32x4){0, 0, 0, 0};
    const u32x4 ones = {0x3f803f80u, 0x3f803f80u, 0x3f803f80u, 0x3f803f80u};
#pragma unroll
    for (int s = 0; s < 8; ++s) {
      const u32x4 phiT = sm.phik[s][l];
      zacc = mfma32(ones, phiT, zacc);
#pragma unroll
      for (int t = 0; t < 4; ++t) {
        u32x4 bf;
        const u32 sel = (t & 1) ? 0x07060302u : 0x05040100u;
#pragma unroll
        for (int a = 0; a < 4; ++a) bf[a] = __builtin_amdgcn_perm(vf[s][a][1][t >> 1], vf[s][a][0][t >> 1], sel);
        acc[t] = mfma32(phiT, bf, acc[t]);
      }
    }
    STAMP(3)
    u32x4 p0, p1;
    {
      p0 = (u32x4){pk_bf16(acc[0][0], acc[0][1]), pk_bf16(acc[0][2], acc[0][3]), pk_bf16(acc[1][0], acc[1][1]), pk_bf16(acc[1][2], acc[1][3])};
      p1 = (u32x4){pk_bf16(acc[2][0], acc[2][1]), pk_bf16(acc[2][2], acc[2][3]), pk_bf16(acc[3][0], acc[3][1]), pk_bf16(acc[3][2], acc[3][3])};
      const u32 chk = xor9(p0, p1, tag);
      const u32 so = j * PART_BYTES + cg * CG_BYTES;
      if (!(ABL & 1)) __builtin_amdgcn_raw_buffer_store_b128(p0, rw, l * 16, so, 16);
      if (!(ABL & 1)) __builtin_amdgcn_raw_buffer_store_b128(p1, rw, l * 16 + 1024, so, 16);
      u32x4 cw;
#pragma unroll
      for (int e = 0; e < 4; ++e) cw[e] = __builtin_amdgcn_ds_bpermute(((4 * l + e) & 63) * 4, chk);
      if (g == 1) cw = (u32x4){tag, __float_as_uint(zacc[0]), tag, __float_as_uint(zacc[0])};
      if (!(ABL & 1) && l < (cg == 2 ? 32 : 16)) __builtin_amdgcn_raw_buffer_store_b128(cw, rw, l * 16 + 2048, so, 16);
    }
    STAMP(4)
    u32x4 sel[2];
    {
      const u32 one = (c & 1) ? 0x3f800000u : 0x00003f80u;
#pragma unroll
      for (int hf = 0; hf < 2; ++hf)
#pragma unroll
        for (int e = 0; e < 4; ++e) sel[hf][e] = (g == 2 * hf + (c >> 3) && ((c & 7) >> 1) == e) ? one : 0u;
    }
    f32x4 own[2][2];
#pragma unroll
    for (int hf = 0; hf < 2; ++hf) {
      own[0][hf] = mfma32(sel[hf], p0, (f32x4){0, 0, 0, 0});
      own[1][hf] = mfma32(sel[hf], p1, (f32x4){0, 0, 0, 0});
    }
    lds_wait(cg == 2 ? CNT(4) : CNT(3), 8);
    STAMP(5)
    int spin = 0;
    f32x4 d[2][2];
    u32x2 za = {tag, 0u}, zb = {tag, 0u};
    if (ABL & 2) { d[0][0] = own[0][0]; d[0][1] = own[0][1]; d[1][0] = own[1][0]; d[1][1] = own[1][1]; za[1] = __float_as_uint(zacc[0]); }
    for (; !(ABL & 2) && spin < (1 << 18); ++spin) {
      u32x4 x[7][2];
      u32 xc[7];
#pragma unroll
      for (int i = 0; i < 7; ++i) {
        const u32 pb = ((j + 1 + i) & 7) * PART_BYTES + cg * CG_BYTES;
        x[i][0] = __builtin_amdgcn_raw_buffer_load_b128(rw, l * 16, pb, 16);
        x[i][1] = __builtin_amdgcn_raw_buffer_load_b128(rw, l * 16 + 1024, pb, 16);
        xc[i] = __builtin_amdgcn_raw_buffer_load_b32(rw, l * 4 + 2048, pb, 16);
      }
      const u32 zo = cg == 2 ? g * PART_BYTES + c * 16 : 0x80000000u;
      za = __builtin_amdgcn_raw_buffer_load_b64(rw, zo, 3 * CG_BYTES, 16);
      zb = __builtin_amdgcn_raw_buffer_load_b64(rw, zo, 3 * CG_BYTES + 4 * PART_BYTES, 16);
      __builtin_amdgcn_sched_barrier(0);
      u32 bad = 0;
#pragma unroll
      for (int s2 = 0; s2 < 2; ++s2)
#pragma unroll
        for (int hf = 0; hf < 2; ++hf) d[s2][hf] = own[s2][hf];
#pragma unroll
      for (int i = 0; i < 7; ++i) {
#pragma unroll
        for (int s2 = 0; s2 < 2; ++s2)
#pragma unroll
          for (int hf = 0; hf < 2; ++hf) d[s2][hf] = mfma32(sel[hf], x[i][s2], d[s2][hf]);
        bad |= xor9(x[i][0], x[i][1], xc[i]) ^ tag;
      }
      if (cg == 2) bad |= (za[0] ^ tag) | (zb[0] ^ tag);
      if (__all(bad == 0) | (ABL & 1)) break;
      __builtin_amdgcn_s_sleep(POLLSLEEP);
      asm volatile("" ::: "memory");
    }
#pragma unroll
    for (int s2 = 0; s2 < 2; ++s2)
#pragma unroll
      for (int hf = 0; hf < 2; ++hf)
        sm.kv[4 * (64 * cg + 4 * c + 2 * s2 + (g & 1)) + 2 * hf + (g >> 1) + 16 * cg + c] =
            (u32x2){pk_bf16(d[s2][hf][0], d[s2][hf][1]), pk_bf16(d[s2][hf][2], d[s2][hf][3])};
    if (cg == 2) {
      const f32x4 zs = __builtin_amdgcn_mfma_f32_16x16x4f32(1.0f, __uint_as_float(za[1]) + __uint_as_float(zb[1]), (f32x4){0, 0, 0, 0}, 0, 0, 0);
      if (l < 16) ((unsigned short*)sm.z)[l] = (unsigned short)pk_bf16(zs[0], 0.f);
    }
    if (l == 0) lds_signal(cg == 2 ? CNT(6) : CNT(5));
    STAMP(6)
    if (cg == 2 && j == 0) {
      lds_wait(CNT(5), 2);
      if (l == 0) __builtin_amdgcn_raw_buffer_store_b32(tag, rw, 0, EPOCH_OFF, 16);
    }
  }
}

extern "C" void kernel_launch(void* const* d_in, const int* in_sizes, int n_in, void* d_out, int out_size,
                              void* d_ws, size_t ws_size, hipStream_t stream) {
  const float scale = 0.26864248f;
  rfa_fused<<<H * NCH, 704, 0, stream>>>((const u32*)d_in[0], (const u32*)d_in[1], (const u32*)d_in[2], (const u32*)d_in[3],
                                        (u32*)d_out, (u32*)d_ws, scale);
}
```

```cpp
#define EVDLYQ 28
#define STA2 0
#include <hip/hip_runtime.h>
#include <stdint.h>

#define LDAUX 2
#define STAUX 2
#define STA0 STAUX
#define STA1 STAUX
#define EVDLY 0
#define EVDLYS 0
#define AFIRST 0
#define PACE2 0
#define PACE0 0
#define PACE1 0
#define ALLDLYS 0
#define STA2B STA2
#define EMU 1
#define TLBPF 1
#define WQ 4
#define QMID 2
#define QLATE (11 - WQ)
#define EARLY0 0
#define XMAP 0
#define ABL 0
#define POLLSLEEP 8

typedef unsigned u32;
typedef unsigned long long u64;
typedef u32 u32x4 __attribute__((ext_vector_type(4)));
typedef u32 u32x2 __attribute__((ext_vector_type(2)));
typedef float f32x4 __attribute__((ext_vector_type(4)));
typedef float f32x2 __attribute__((ext_vector_type(2)));
typedef __bf16 bf16x8 __attribute__((ext_vector_type(8)));
typedef __bf16 bf16x2 __attribute__((ext_vector_type(2)));
typedef short s16x4 __attribute__((ext_vector_type(4)));
#define LAS __attribute__((address_space(3)))

constexpr int H = 32, S = 2048, D = 192, M = 16;
constexpr int CH = 256;
constexpr int NCH = S / CH;
constexpr u32 TBYTES = H * S * D * 2;
constexpr u32 CG_BYTES = 2304;
constexpr u32 PART_BYTES = 3 * CG_BYTES + 256;
constexpr u32 EPOCH_OFF = NCH * PART_BYTES;
constexpr u32 HEAD_BYTES = EPOCH_OFF + 256;

#define STAMP(i)

__device__ __forceinline__ float lo_f(u32 x) { return __uint_as_float(x << 16); }
__device__ __forceinline__ float hi_f(u32 x) { return __uint_as_float(x & 0xffff0000u); }
__device__ __forceinline__ u32 pk_bf16(float a, float b) {
  f32x2 v = {a, b};
  bf16x2 r = __builtin_convertvector(v, bf16x2);
  return __builtin_bit_cast(u32, r);
}
__device__ __forceinline__ float rbf(float a) { return lo_f(pk_bf16(a, 0.f)); }
__device__ __forceinline__ f32x4 mfma32(u32x4 a, u32x4 b, f32x4 c) {
  return __builtin_amdgcn_mfma_f32_16x16x32_bf16(__builtin_bit_cast(bf16x8, a), __builtin_bit_cast(bf16x8, b), c, 0, 0, 0);
}
__device__ __forceinline__ f32x4 mfma16(u32x2 a, u32x2 b, f32x4 c) {
  return __builtin_amdgcn_mfma_f32_16x16x16bf16_1k(__builtin_bit_cast(s16x4, a), __builtin_bit_cast(s16x4, b), c, 0, 0, 0);
}
template <bool E>
__device__ __forceinline__ u32x4 scale_frag(u32x4 x, float scale, float& ss) {
  u32x4 r;
#pragma unroll
  for (int i = 0; i < 4; ++i) {
    u32 p = E ? pk_bf16(lo_f(x[i]) * scale, hi_f(x[i]) * scale) : x[i];
    r[i] = p;
    ss = __builtin_amdgcn_fdot2_f32_bf16(__builtin_bit_cast(bf16x2, p), __builtin_bit_cast(bf16x2, p), ss, false);
  }
  return r;
}
template <bool E>
__device__ __forceinline__ u32 phi2(float p0, float s0, float p1, float s1, float scale) {
  if (E) {
    u32 pp = pk_bf16(p0, p1), sp = pk_bf16(s0, s1);
    u32 tp = pk_bf16(fmaf(lo_f(sp), -0.5f, lo_f(pp)), fmaf(hi_f(sp), -0.5f, hi_f(pp)));
    u32 ep = pk_bf16(__expf(lo_f(tp)), __expf(hi_f(tp)));
    return pk_bf16(fmaf(lo_f(ep), 0.25f, 1e-6f), fmaf(hi_f(ep), 0.25f, 1e-6f));
  } else {
    const float sl = scale * 1.44269504f, hs = -0.5f * scale * sl;
    return pk_bf16(fmaf(__builtin_amdgcn_exp2f(fmaf(p0, sl, s0 * hs)), 0.25f, 1e-6f),
                   fmaf(__builtin_amdgcn_exp2f(fmaf(p1, sl, s1 * hs)), 0.25f, 1e-6f));
  }
}
__device__ __forceinline__ float swap1(float x) {
  return __builtin_bit_cast(float, __builtin_amdgcn_update_dpp(0, __builtin_bit_cast(int, x), 0xB1, 0xf, 0xf, true));
}
__device__ __forceinline__ void lds_add(LAS u32* p) {
  __hip_atomic_fetch_add(p, 1u, __ATOMIC_RELAXED, __HIP_MEMORY_SCOPE_WORKGROUP);
}
__device__ __forceinline__ void lds_signal(LAS u32* p) {
  asm volatile("s_waitcnt lgkmcnt(0)" ::: "memory");
  lds_add(p);
}
__device__ __forceinline__ void lds_wait(LAS u32* p, u32 want) {
  while (__builtin_amdgcn_readfirstlane(__hip_atomic_load(p, __ATOMIC_RELAXED, __HIP_MEMORY_SCOPE_WORKGROUP)) < want)
    __builtin_amdgcn_s_sleep(1);
  asm volatile("" ::: "memory");
}
__device__ __forceinline__ void lds_spin(LAS u32* p, u32 want) {
  while (__builtin_amdgcn_readfirstlane(__hip_atomic_load(p, __ATOMIC_RELAXED, __HIP_MEMORY_SCOPE_WORKGROUP)) < want) {}
  asm volatile("" ::: "memory");
}
__device__ __forceinline__ u32 xor3(u32 a, u32 b, u32 c) { return __builtin_amdgcn_bitop3_b32(a, b, c, 0x96); }
__device__ __forceinline__ u32 xor9(u32x4 a, u32x4 b, u32 c) {
  return xor3(xor3(a[0], a[1], a[2]), xor3(a[3], b[0], b[1]), xor3(b[2], b[3], c));
}

struct __attribute__((aligned(16))) Smem {
  u32x4 om[6][64];
  u32x4 phik[8][64];
  u32x2 kv[48 * 17];
  u32x2 z[4];
  u32 tag;
  u32 pad[3];
  u32 cnt[8];
};
#define CNT(i) ((LAS u32*)&sm.cnt[i])

__global__ __launch_bounds__(704) void rfa_fused(const u32* __restrict__ Q, const u32* __restrict__ K, const u32* __restrict__ V,
                                                 const u32* __restrict__ Om, u32* __restrict__ O, u32* ws, float scale) {
  __shared__ Smem sm;
  STAMP(0)
  const int b = blockIdx.x;
  const int h = (b & 7) * 4 + (b >> 6), j = (b >> 3) & 7;
  const int w = __builtin_amdgcn_readfirstlane(threadIdx.x >> 6);
  const int l = threadIdx.x & 63, g = l >> 4, c = l & 15;
  const u32 rowb = h * S + j * CH;
  if (w >= 3) {
    const int wa = w - 3;
    const u32 row0 = rowb + wa * 32;
    const u32 off_frag = (row0 + (c >> 1)) * 384 + (2 * g + (c & 1)) * 16;
    auto rk = __builtin_amdgcn_make_buffer_rsrc((void*)K, 0, TBYTES, 0x00020000);
    auto rq = __builtin_amdgcn_make_buffer_rsrc((void*)Q, 0, TBYTES, 0x00020000);
    auto ro = __builtin_amdgcn_make_buffer_rsrc((void*)O, 0, TBYTES, 0x00020000);
    u32x4 om[3][2], xf[12];
    if (wa >= 4) __builtin_amdgcn_s_barrier();
    if (EVDLY && wa < 4 && !(b & 1)) __builtin_amdgcn_s_sleep(EVDLY);
    __builtin_amdgcn_sched_barrier(0);
#pragma unroll
    for (int n = 0; n < 12; ++n) xf[n] = __builtin_amdgcn_raw_buffer_load_b128(rk, off_frag + (n % 3) * 128, (n / 3) * 3072, LDAUX);
    __builtin_amdgcn_sched_barrier(0);
    if (wa < 4) __builtin_amdgcn_s_barrier();
    else if (l == 0) lds_add(CNT(0));
    STAMP(1)
    lds_wait(CNT(1), 1);
#pragma unroll
    for (int i = 0; i < 3; ++i)
#pragma unroll
      for (int p = 0; p < 2; ++p) om[i][p] = sm.om[2 * i + p][l];
    {
      u32x4 phiT;
#pragma unroll
      for (int a = 0; a < 4; ++a) {
        float ss = 0.f;
        f32x4 d0 = {0, 0, 0, 0}, d1 = {0, 0, 0, 0};
#pragma unroll
        for (int i = 0; i < 3; ++i) {
          u32x4 ks = scale_frag<(EMU & 1) != 0>(xf[3 * a + i], scale, ss);
          d0 = mfma32(ks, om[i][0], d0);
          d1 = mfma32(ks, om[i][1], d1);
        }
        f32x4 sq = __builtin_amdgcn_mfma_f32_16x16x4f32(ss, 1.0f, (f32x4){0, 0, 0, 0}, 0, 0, 0);
        phiT[a] = phi2<(EMU & 1) != 0>(d0[0] + d1[1], sq[0] + sq[1], d0[2] + d1[3], sq[2] + sq[3], scale);
      }
      sm.phik[wa][l] = phiT;
    }
    if (l == 0) lds_signal(CNT(2));
    STAMP(2)
    lds_wait(CNT(0), 7);
    if (EVDLYQ && !(__builtin_amdgcn_s_getreg(20) & 1)) __builtin_amdgcn_s_sleep(EVDLYQ);
    __builtin_amdgcn_sched_barrier(0);
#pragma unroll
    for (int n = 0; n < WQ; ++n) {
      xf[n] = __builtin_amdgcn_raw_buffer_load_b128(rq, off_frag + (n % 3) * 128, (n / 3) * 3072, LDAUX);
      __builtin_amdgcn_sched_barrier(0);
    }
    STAMP(3)
    const bool odd = c & 1;
    const u32 off_row = (row0 + 2 * g) * 384;
    u32x2 phq[2], kv2[4], zf;
    f32x4 inv[2];
    auto part2 = [&](int u) {
      const f32x4 den = mfma16(phq[u], zf, (f32x4){0, 0, 0, 0});
      f32x4 num[4];
#pragma unroll
      for (int t = 0; t < 4; ++t) num[t] = mfma16(phq[u], kv2[t], (f32x4){0, 0, 0, 0});
#pragma unroll
      for (int r = 0; r < 4; ++r) inv[u][r] = __builtin_amdgcn_rcpf(rbf(rbf(den[r]) + 1e-6f));
#pragma unroll
      for (int t = 0; t < 4; ++t) num[t] *= inv[u];
#pragma unroll
      for (int r = 0; r < 4; ++r) {
        const u32x2 o = {pk_bf16(num[0][r], num[1][r]), pk_bf16(num[2][r], num[3][r])};
        if (u == 0) __builtin_amdgcn_raw_buffer_store_b64(o, ro, off_row + 8 * c + 256 + (r >> 1) * 384, (16 * u + 8 * (r & 1)) * 384, STA2);
        else __builtin_amdgcn_raw_buffer_store_b64(o, ro, off_row + 8 * c + 256 + (r >> 1) * 384, (16 * u + 8 * (r & 1)) * 384, STA2B);
        if (PACE2) { __builtin_amdgcn_sched_barrier(0); __builtin_amdgcn_s_sleep(PACE2); __builtin_amdgcn_sched_barrier(0); }
      }
    };
    auto load_kv2 = [&]() {
      lds_spin(CNT(6), 1);
      zf = sm.z[g];
#pragma unroll
      for (int t = 0; t < 4; ++t) kv2[t] = sm.kv[4 * (128 + 4 * c + t) + g + 32 + c];
    };
#pragma unroll
    for (int u = 0; u < 2; ++u) {
      float ss[2];
      f32x4 d[2][2];
#pragma unroll
      for (int a = 0; a < 2; ++a) {
        ss[a] = 0.f;
        d[a][0] = (f32x4){0, 0, 0, 0};
        d[a][1] = (f32x4){0, 0, 0, 0};
#pragma unroll
        for (int i = 0; i < 3; ++i) {
          const int n = 6 * u + 3 * a + i;
          if (n + WQ < 12) {
            asm volatile("" : "+v"(xf[n]));
            __builtin_amdgcn_sched_barrier(0);
            xf[n + WQ] = __builtin_amdgcn_raw_buffer_load_b128(rq, off_frag + ((n + WQ) % 3) * 128, ((n + WQ) / 3) * 3072, LDAUX);
            __builtin_amdgcn_sched_barrier(0);
          }
          if (n == QMID || n == QLATE) {
            asm volatile("" : "+v"(xf[n]));
            if (n == QMID && l == 0) lds_add(CNT(4));
            if (n == QLATE && l == 0) lds_add(CNT(3));
          }
          if (EARLY0 && n == EARLY0) {
            load_kv2();
            if (EARLY0 < 9) lds_spin(CNT(3), 8);
            part2(0);
          }
          u32x4 qs = scale_frag<(EMU & 2) != 0>(xf[n], scale, ss[a]);
          d[a][0] = mfma32(om[i][0], qs, d[a][0]);
          d[a][1] = mfma32(om[i][1], qs, d[a][1]);
        }
      }
      const float st = (odd ? ss[1] : ss[0]) + swap1(odd ? ss[0] : ss[1]);
      f32x4 sq = __builtin_amdgcn_mfma_f32_16x16x4f32(1.0f, st, (f32x4){0, 0, 0, 0}, 0, 0, 0);
      float pr[4];
#pragma unroll
      for (int r = 0; r < 4; ++r) pr[r] = (odd ? d[1][1][r] : d[0][0][r]) + swap1(odd ? d[0][1][r] : d[1][0][r]);
      phq[u][0] = phi2<(EMU & 2) != 0>(pr[0], sq[0], pr[1], sq[0], scale);
      phq[u][1] = phi2<(EMU & 2) != 0>(pr[2], sq[0], pr[3], sq[0], scale);
    }
    asm volatile("" : "+v"(phq[0]), "+v"(phq[1]));
    STAMP(4)
    if (EVDLYS && !(__builtin_amdgcn_s_getreg(20) & 1)) __builtin_amdgcn_s_sleep(EVDLYS);
    if (ALLDLYS) __builtin_amdgcn_s_sleep(ALLDLYS);
    if (!EARLY0) {
      load_kv2();
      part2(0);
    }
    part2(1);
    STAMP(5)
    lds_spin(CNT(5), 2);
    {
      u32x2 kvf[8];
#pragma unroll
      for (int t = 0; t < 8; ++t) kvf[t] = sm.kv[4 * (8 * c + t) + g + 2 * c + (t >> 2)];
#pragma unroll
      for (int u = 0; u < 2; ++u) {
        f32x4 num[8];
#pragma unroll
        for (int t = 0; t < 8; ++t) num[t] = mfma16(phq[u], kvf[t], (f32x4){0, 0, 0, 0}) * inv[u];
#pragma unroll
        for (int r = 0; r < 4; ++r) {
          const u32x4 o = {pk_bf16(num[0][r], num[1][r]), pk_bf16(num[2][r], num[3][r]), pk_bf16(num[4][r], num[5][r]), pk_bf16(num[6][r], num[7][r])};
          if (u == 0) __builtin_amdgcn_raw_buffer_store_b128(o, ro, off_row + 16 * c + (r >> 1) * 384, (16 * u + 8 * (r & 1)) * 384, STA0);
          else __builtin_amdgcn_raw_buffer_store_b128(o, ro, off_row + 16 * c + (r >> 1) * 384, (16 * u + 8 * (r & 1)) * 384, STA1);
          if (u == 0 && PACE0) { __builtin_amdgcn_sched_barrier(0); __builtin_amdgcn_s_sleep(PACE0); __builtin_amdgcn_sched_barrier(0); }
          if (u == 1 && PACE1 && r < 3) { __builtin_amdgcn_sched_barrier(0); __builtin_amdgcn_s_sleep(PACE1); __builtin_amdgcn_sched_barrier(0); }
        }
      }
    }
    STAMP(6)
    STAMP(7)
  } else {
    const int cg = w;
    auto rw = __builtin_amdgcn_make_buffer_rsrc((char*)ws + h * HEAD_BYTES, 0, HEAD_BYTES, 0x00020000);
    if (cg == 0) {
      auto rom = __builtin_amdgcn_make_buffer_rsrc((void*)Om, 0, M * D * 2, 0x00020000);
      u32x4 om[6];
#pragma unroll
      for (int i = 0; i < 6; ++i) om[i] = __builtin_amdgcn_raw_buffer_load_b128(rom, c * 384 + 2 * g * 16, (i >> 1) * 128 + (i & 1) * 16, 0);
      __builtin_amdgcn_sched_barrier(0);
      __builtin_amdgcn_s_barrier();
      asm volatile("" ::: "memory");
#pragma unroll
      for (int i = 0; i < 6; ++i) sm.om[i][l] = om[i];
      if (l == 0) lds_signal(CNT(1));
    } else if (cg == 1) {
      u32 pf = 0;
      const char* base = g == 0 ? (const char*)Q : g == 1 ? (const char*)O : (const char*)V;
      const char* p = base + rowb * 384 + (c < 2 ? c * 98300 : 49152);
      if (g == 3) p = (const char*)ws + h * HEAD_BYTES;
      pf = __builtin_nontemporal_load((const u32*)p);
      if (l < 8) sm.cnt[l] = 0;
      asm volatile("s_waitcnt lgkmcnt(0)" ::: "memory");
      __builtin_amdgcn_s_barrier();
      asm volatile("" :: "v"(pf));
    } else {
      u32 vz;
      asm volatile("v_mov_b32 %0, 0" : "=v"(vz));
      const u32 epoch = __builtin_amdgcn_raw_buffer_load_b32(rw, vz, EPOCH_OFF, 16);
      __builtin_amdgcn_sched_barrier(0);
      __builtin_amdgcn_s_barrier();
      asm volatile("" ::: "memory");
      if (l == 0) {
        sm.tag = epoch + 1u;
        lds_signal(CNT(2));
      }
    }
    auto rv = __builtin_amdgcn_make_buffer_rsrc((void*)V, 0, TBYTES, 0x00020000);
    const u32 voff = (rowb + 2 * g) * 384 + cg * 128 + 8 * c;
    lds_wait(CNT(0), 4);
    __builtin_amdgcn_sched_barrier(0);
    STAMP(1)
    u32x2 vf[8][4][2];
#pragma unroll
    for (int s = 0; s < 8; ++s)
#pragma unroll
      for (int a = 0; a < 4; ++a)
#pragma unroll
        for (int bb = 0; bb < 2; ++bb)
          vf[s][a][bb] = __builtin_amdgcn_raw_buffer_load_b64(rv, voff + bb * 384, (s * 4 + a) * 3072, LDAUX);
    __builtin_amdgcn_sched_barrier(0);
    if (l == 0) lds_add(CNT(0));
    STAMP(2)
    lds_wait(CNT(2), 9);
    const u32 tag = sm.tag;
    f32x4 acc[4], zacc = {0, 0, 0, 0};
#pragma unroll
    for (int t = 0; t < 4; ++t) acc[t] = (f32x4){0, 0, 0, 0};
    const u32x4 ones = {0x3f803f80u, 0x3f803f80u, 0x3f803f80u, 0x3f803f80u};
#pragma unroll
    for (int s = 0; s < 8; ++s) {
      const u32x4 phiT = sm.phik[s][l];
      zacc = mfma32(ones, phiT, zacc);
#pragma unroll
      for (int t = 0; t < 4; ++t) {
        u32x4 bf;
        const u32 sel = (t & 1) ? 0x07060302u : 0x05040100u;
#pragma unroll
        for (int a = 0; a < 4; ++a) bf[a] = __builtin_amdgcn_perm(vf[s][a][1][t >> 1], vf[s][a][0][t >> 1], sel);
        acc[t] = mfma32(phiT, bf, acc[t]);
      }
    }
    STAMP(3)
    u32x4 p0, p1;
    {
      p0 = (u32x4){pk_bf16(acc[0][0], acc[0][1]), pk_bf16(acc[0][2], acc[0][3]), pk_bf16(acc[1][0], acc[1][1]), pk_bf16(acc[1][2], acc[1][3])};
      p1 = (u32x4){pk_bf16(acc[2][0], acc[2][1]), pk_bf16(acc[2][2], acc[2][3]), pk_bf16(acc[3][0], acc[3][1]), pk_bf16(acc[3][2], acc[3][3])};
      const u32 chk = xor9(p0, p1, tag);
      const u32 so = j * PART_BYTES + cg * CG_BYTES;
      if (!(ABL & 1)) __builtin_amdgcn_raw_buffer_store_b128(p0, rw, l * 16, so, 16);
      if (!(ABL & 1)) __builtin_amdgcn_raw_buffer_store_b128(p1, rw, l * 16 + 1024, so, 16);
      u32x4 cw;
#pragma unroll
      for (int e = 0; e < 4; ++e) cw[e] = __builtin_amdgcn_ds_bpermute(((4 * l + e) & 63) * 4, chk);
      if (g == 1) cw = (u32x4){tag, __float_as_uint(zacc[0]), tag, __float_as_uint(zacc[0])};
      if (!(ABL & 1) && l < (cg == 2 ? 32 : 16)) __builtin_amdgcn_raw_buffer_store_b128(cw, rw, l * 16 + 2048, so, 16);
    }
    STAMP(4)
    u32x4 sel[2];
    {
      const u32 one = (c & 1) ? 0x3f800000u : 0x00003f80u;
#pragma unroll
      for (int hf = 0; hf < 2; ++hf)
#pragma unroll
        for (int e = 0; e < 4; ++e) sel[hf][e] = (g == 2 * hf + (c >> 3) && ((c & 7) >> 1) == e) ? one : 0u;
    }
    f32x4 own[2][2];
#pragma unroll
    for (int hf = 0; hf < 2; ++hf) {
      own[0][hf] = mfma32(sel[hf], p0, (f32x4){0, 0, 0, 0});
      own[1][hf] = mfma32(sel[hf], p1, (f32x4){0, 0, 0, 0});
    }
    lds_wait(cg == 2 ? CNT(4) : CNT(3), 8);
    STAMP(5)
    int spin = 0;
    f32x4 d[2][2];
    u32x2 za = {tag, 0u}, zb = {tag, 0u};
    if (ABL & 2) { d[0][0] = own[0][0]; d[0][1] = own[0][1]; d[1][0] = own[1][0]; d[1][1] = own[1][1]; za[1] = __float_as_uint(zacc[0]); }
    for (; !(ABL & 2) && spin < (1 << 18); ++spin) {
      u32x4 x[7][2];
      u32 xc[7];
#pragma unroll
      for (int i = 0; i < 7; ++i) {
        const u32 pb = ((j + 1 + i) & 7) * PART_BYTES + cg * CG_BYTES;
        x[i][0] = __builtin_amdgcn_raw_buffer_load_b128(rw, l * 16, pb, 16);
        x[i][1] = __builtin_amdgcn_raw_buffer_load_b128(rw, l * 16 + 1024, pb, 16);
        xc[i] = __builtin_amdgcn_raw_buffer_load_b32(rw, l * 4 + 2048, pb, 16);
      }
      const u32 zo = cg == 2 ? g * PART_BYTES + c * 16 : 0x80000000u;
      za = __builtin_amdgcn_raw_buffer_load_b64(rw, zo, 3 * CG_BYTES, 16);
      zb = __builtin_amdgcn_raw_buffer_load_b64(rw, zo, 3 * CG_BYTES + 4 * PART_BYTES, 16);
      __builtin_amdgcn_sched_barrier(0);
      u32 bad = 0;
#pragma unroll
      for (int s2 = 0; s2 < 2; ++s2)
#pragma unroll
        for (int hf = 0; hf < 2; ++hf) d[s2][hf] = own[s2][hf];
#pragma unroll
      for (int i = 0; i < 7; ++i) {
#pragma unroll
        for (int s2 = 0; s2 < 2; ++s2)
#pragma unroll
          for (int hf = 0; hf < 2; ++hf) d[s2][hf] = mfma32(sel[hf], x[i][s2], d[s2][hf]);
        bad |= xor9(x[i][0], x[i][1], xc[i]) ^ tag;
      }
      if (cg == 2) bad |= (za[0] ^ tag) | (zb[0] ^ tag);
      if (__all(bad == 0) | (ABL & 1)) break;
      __builtin_amdgcn_s_sleep(POLLSLEEP);
      asm volatile("" ::: "memory");
    }
#pragma unroll
    for (int s2 = 0; s2 < 2; ++s2)
#pragma unroll
      for (int hf = 0; hf < 2; ++hf)
        sm.kv[4 * (64 * cg + 4 * c + 2 * s2 + (g & 1)) + 2 * hf + (g >> 1) + 16 * cg + c] =
            (u32x2){pk_bf16(d[s2][hf][0], d[s2][hf][1]), pk_bf16(d[s2][hf][2], d[s2][hf][3])};
    if (cg == 2) {
      const f32x4 zs = __builtin_amdgcn_mfma_f32_16x16x4f32(1.0f, __uint_as_float(za[1]) + __uint_as_float(zb[1]), (f32x4){0, 0, 0, 0}, 0, 0, 0);
      if (l < 16) ((unsigned short*)sm.z)[l] = (unsigned short)pk_bf16(zs[0], 0.f);
    }
    if (l == 0) lds_signal(cg == 2 ? CNT(6) : CNT(5));
    STAMP(6)
    if (cg == 2 && j == 0) {
      lds_wait(CNT(5), 2);
      if (l == 0) __builtin_amdgcn_raw_buffer_store_b32(tag, rw, 0, EPOCH_OFF, 16);
    }
  }
}

extern "C" void kernel_launch(void* const* d_in, const int* in_sizes, int n_in, void* d_out, int out_size,
                              void* d_ws, size_t ws_size, hipStream_t stream) {
  const float scale = 0.26864248f;
  rfa_fused<<<H * NCH, 704, 0, stream>>>((const u32*)d_in[0], (const u32*)d_in[1], (const u32*)d_in[2], (const u32*)d_in[3],
                                        (u32*)d_out, (u32*)d_ws, scale);
}
```

```cpp
#include <hip/hip_runtime.h>
#include <stdint.h>

typedef unsigned u32;
typedef unsigned long long u64;
typedef u32 u32x4 __attribute__((ext_vector_type(4)));
typedef u32 u32x2 __attribute__((ext_vector_type(2)));
typedef float f32x4 __attribute__((ext_vector_type(4)));
typedef float f32x2 __attribute__((ext_vector_type(2)));
typedef __bf16 bf16x8 __attribute__((ext_vector_type(8)));
typedef __bf16 bf16x2 __attribute__((ext_vector_type(2)));
typedef short s16x4 __attribute__((ext_vector_type(4)));
#define LAS __attribute__((address_space(3)))
#define GAS __attribute__((address_space(1)))

constexpr int H = 32, S = 2048, D = 192, M = 16;
constexpr int CH = 256;
constexpr int NCH = S / CH;
constexpr u32 TBYTES = H * S * D * 2;
constexpr u32 CG_BYTES = 2304;
constexpr u32 PART_BYTES = 3 * CG_BYTES + 256;
constexpr u32 EPOCH_OFF = NCH * PART_BYTES;
constexpr u32 HEAD_BYTES = EPOCH_OFF + 256;
constexpr int AUX_NT = 2, AUX_DEV = 16, AUX_PLAIN = 0;
constexpr int WQ = 4;
constexpr int QMID = 2, QLATE = 11 - WQ, QEARLY = 9;
#define CODELEN 10880
#define XMAP 0

__device__ __forceinline__ float lo_f(u32 x) { return __uint_as_float(x << 16); }
__device__ __forceinline__ float hi_f(u32 x) { return __uint_as_float(x & 0xffff0000u); }
__device__ __forceinline__ u32 pk_bf16(float a, float b) {
  f32x2 v = {a, b};
  bf16x2 r = __builtin_convertvector(v, bf16x2);
  return __builtin_bit_cast(u32, r);
}
__device__ __forceinline__ f32x4 mfma32(u32x4 a, u32x4 b, f32x4 c) {
  return __builtin_amdgcn_mfma_f32_16x16x32_bf16(__builtin_bit_cast(bf16x8, a), __builtin_bit_cast(bf16x8, b), c, 0, 0, 0);
}
__device__ __forceinline__ f32x4 mfma16(u32x2 a, u32x2 b, f32x4 c) {
  return __builtin_amdgcn_mfma_f32_16x16x16bf16_1k(__builtin_bit_cast(s16x4, a), __builtin_bit_cast(s16x4, b), c, 0, 0, 0);
}
__device__ __forceinline__ float sumsq2(u32 p, float acc) {
  return __builtin_amdgcn_fdot2_f32_bf16(__builtin_bit_cast(bf16x2, p), __builtin_bit_cast(bf16x2, p), acc, false);
}
__device__ __forceinline__ u32 phi2_bf16(float p0, float s0, float p1, float s1) {
  const u32 pp = pk_bf16(p0, p1), sp = pk_bf16(s0, s1);
  const u32 tp = pk_bf16(fmaf(lo_f(sp), -0.5f, lo_f(pp)), fmaf(hi_f(sp), -0.5f, hi_f(pp)));
  const u32 ep = pk_bf16(__expf(lo_f(tp)), __expf(hi_f(tp)));
  return pk_bf16(fmaf(lo_f(ep), 0.25f, 1e-6f), fmaf(hi_f(ep), 0.25f, 1e-6f));
}
__device__ __forceinline__ float swap1(float x) {
  return __builtin_bit_cast(float, __builtin_amdgcn_update_dpp(0, __builtin_bit_cast(int, x), 0xB1, 0xf, 0xf, true));
}
__device__ __forceinline__ void lds_add(LAS u32* p) {
  u64 sv;
  asm volatile("s_mov_b64 %0, exec\n\ts_mov_b64 exec, 1\n\tds_add_u32 %1, %2\n\ts_mov_b64 exec, %0"
               : "=&s"(sv) : "v"((u32)(size_t)p), "v"(1u) : "memory");
}
__device__ __forceinline__ void lds_signal(LAS u32* p) {
  asm volatile("s_waitcnt lgkmcnt(0)" ::: "memory");
  lds_add(p);
}
template <int SLEEP>
__device__ __forceinline__ void lds_wait(LAS u32* p, u32 want) {
  while (__builtin_amdgcn_readfirstlane(__hip_atomic_load(p, __ATOMIC_RELAXED, __HIP_MEMORY_SCOPE_WORKGROUP)) < want)
    if (SLEEP) __builtin_amdgcn_s_sleep(SLEEP);
  asm volatile("" ::: "memory");
}
__device__ __forceinline__ u32 xor3(u32 a, u32 b, u32 c) { return __builtin_amdgcn_bitop3_b32(a, b, c, 0x96); }
__device__ __forceinline__ u32 xor9(u32x4 a, u32x4 b, u32 c) {
  return xor3(xor3(a[0], a[1], a[2]), xor3(a[3], b[0], b[1]), xor3(b[2], b[3], c));
}

struct __attribute__((aligned(16))) Smem {
  u32x4 om[6][64];
  u32x4 phik[8][64];
  u32x2 kv[48 * 17];
  f32x4 z[4];
  u32 tag;
  u32 pad[3];
  u32 cnt[8];
};
enum { C_GATE, C_OMEGA, C_PHIK, C_QLATE, C_QMID, C_KV01, C_KV2 };
#define CNT(i) ((LAS u32*)&sm.cnt[i])

__global__ __launch_bounds__(704, 3) void rfa_fused(const u32* __restrict__ Q, const u32* __restrict__ K,
                                                    const u32* __restrict__ V, const u32* __restrict__ Om,
                                                    u32* __restrict__ O, u32* ws, float scale) {
  __shared__ Smem sm;
  const int b = blockIdx.x;
  const int h = (b & 7) * 4 + (b >> 6), j = (b >> 3) & 7;
  const int w = __builtin_amdgcn_readfirstlane(threadIdx.x >> 6);
  const int l = threadIdx.x & 63, g = l >> 4, c = l & 15;
  const u32 rowb = h * S + j * CH;
  if (w < 3) {
    const int cg = w;
    auto rw = __builtin_amdgcn_make_buffer_rsrc((char*)ws + h * HEAD_BYTES, 0, HEAD_BYTES, 0x00020000);
    if (cg == 0) {
      auto rom = __builtin_amdgcn_make_buffer_rsrc((void*)Om, 0, M * D * 2, 0x00020000);
      u32x4 om[6];
#pragma unroll
      for (int i = 0; i < 6; ++i)
        om[i] = __builtin_amdgcn_raw_buffer_load_b128(rom, c * 384 + 2 * g * 16, (i >> 1) * 128 + (i & 1) * 16, 0);
      __builtin_amdgcn_sched_barrier(0);
      __builtin_amdgcn_s_barrier();
      asm volatile("" ::: "memory");
#pragma unroll
      for (int i = 0; i < 6; ++i) sm.om[i][l] = om[i];
      lds_signal(CNT(C_OMEGA));
    } else if (cg == 1) {
      const char* base = g == 0 ? (const char*)Q : g == 1 ? (const char*)O : (const char*)V;
      const char* p = base + rowb * 384 + (c < 2 ? c * 98300 : 49152);
      if (g == 3) p = (const char*)ws + h * HEAD_BYTES;
      const u32 pf = __builtin_nontemporal_load((const u32*)p);
      if (l < 8) sm.cnt[l] = 0;
      asm volatile("s_waitcnt lgkmcnt(0)" ::: "memory");
      __builtin_amdgcn_s_barrier();
      asm volatile("" ::"v"(pf));
    } else {
      u32 vz;
      asm volatile("v_mov_b32 %0, 0" : "=v"(vz));
      const u32 epoch = __builtin_amdgcn_raw_buffer_load_b32(rw, vz, EPOCH_OFF, AUX_DEV);
      __builtin_amdgcn_sched_barrier(0);
      __builtin_amdgcn_s_barrier();
      asm volatile("" ::: "memory");
      sm.tag = epoch + 1u;
      lds_signal(CNT(C_PHIK));
    }
    auto rv = __builtin_amdgcn_make_buffer_rsrc((void*)V, 0, TBYTES, 0x00020000);
    const u32 voff = (rowb + 2 * g) * 384 + cg * 128 + 8 * c;
    lds_wait<1>(CNT(C_GATE), 4);
    __builtin_amdgcn_sched_barrier(0);
    u32x2 vf[8][4][2];
#pragma unroll
    for (int s = 0; s < 8; ++s)
#pragma unroll
      for (int a = 0; a < 4; ++a)
#pragma unroll
        for (int bb = 0; bb < 2; ++bb)
          vf[s][a][bb] = __builtin_amdgcn_raw_buffer_load_b64(rv, voff + bb * 384, (s * 4 + a) * 3072, AUX_NT);
    __builtin_amdgcn_sched_barrier(0);
    lds_add(CNT(C_GATE));
    lds_wait<1>(CNT(C_PHIK), 9);
    const u32 tag = sm.tag;
    f32x4 acc[4], zacc = {0, 0, 0, 0};
#pragma unroll
    for (int t = 0; t < 4; ++t) acc[t] = (f32x4){0, 0, 0, 0};
    const u32x4 ones = {0x3f803f80u, 0x3f803f80u, 0x3f803f80u, 0x3f803f80u};
#pragma unroll
    for (int s = 0; s < 8; ++s) {
      const u32x4 phiT = sm.phik[s][l];
      zacc = mfma32(ones, phiT, zacc);
#pragma unroll
      for (int t = 0; t < 4; ++t) {
        u32x4 bf;
        const u32 sel = (t & 1) ? 0x07060302u : 0x05040100u;
#pragma unroll
        for (int a = 0; a < 4; ++a) bf[a] = __builtin_amdgcn_perm(vf[s][a][1][t >> 1], vf[s][a][0][t >> 1], sel);
        acc[t] = mfma32(phiT, bf, acc[t]);
      }
    }
    const u32x4 p0 = {pk_bf16(acc[0][0], acc[0][1]), pk_bf16(acc[0][2], acc[0][3]), pk_bf16(acc[1][0], acc[1][1]),
                      pk_bf16(acc[1][2], acc[1][3])};
    const u32x4 p1 = {pk_bf16(acc[2][0], acc[2][1]), pk_bf16(acc[2][2], acc[2][3]), pk_bf16(acc[3][0], acc[3][1]),
                      pk_bf16(acc[3][2], acc[3][3])};
    {
      const u32 chk = xor9(p0, p1, tag);
      const u32 so = j * PART_BYTES + cg * CG_BYTES;
      __builtin_amdgcn_raw_buffer_store_b128(p0, rw, l * 16, so, AUX_DEV);
      __builtin_amdgcn_raw_buffer_store_b128(p1, rw, l * 16 + 1024, so, AUX_DEV);
      u32x4 cw;
#pragma unroll
      for (int e = 0; e < 4; ++e) cw[e] = __builtin_amdgcn_ds_bpermute(((4 * l + e) & 63) * 4, chk);
      if (g == 1) cw = (u32x4){tag, __float_as_uint(zacc[0]), tag, __float_as_uint(zacc[0])};
      if (l < (cg == 2 ? 32 : 16)) __builtin_amdgcn_raw_buffer_store_b128(cw, rw, l * 16 + 2048, so, AUX_DEV);
    }
    u32x4 sel[2];
    {
      const u32 one = (c & 1) ? 0x3f800000u : 0x00003f80u;
#pragma unroll
      for (int hf = 0; hf < 2; ++hf)
#pragma unroll
        for (int e = 0; e < 4; ++e) sel[hf][e] = (g == 2 * hf + (c >> 3) && ((c & 7) >> 1) == e) ? one : 0u;
    }
    f32x4 own[2][2];
#pragma unroll
    for (int hf = 0; hf < 2; ++hf) {
      own[0][hf] = mfma32(sel[hf], p0, (f32x4){0, 0, 0, 0});
      own[1][hf] = mfma32(sel[hf], p1, (f32x4){0, 0, 0, 0});
    }
    lds_wait<1>(cg == 2 ? CNT(C_QMID) : CNT(C_QLATE), 8);
    f32x4 d[2][2];
    u32x2 za, zb;
    for (int spin = 0; spin < (1 << 18); ++spin) {
      u32x4 x[7][2];
      u32 xc[7];
#pragma unroll
      for (int i = 0; i < 7; ++i) {
        const u32 pb = ((j + 1 + i) & 7) * PART_BYTES + cg * CG_BYTES;
        x[i][0] = __builtin_amdgcn_raw_buffer_load_b128(rw, l * 16, pb, AUX_DEV);
        x[i][1] = __builtin_amdgcn_raw_buffer_load_b128(rw, l * 16 + 1024, pb, AUX_DEV);
        xc[i] = __builtin_amdgcn_raw_buffer_load_b32(rw, l * 4 + 2048, pb, AUX_DEV);
      }
      const u32 zo = cg == 2 ? g * PART_BYTES + c * 16 : 0x80000000u;
      za = __builtin_amdgcn_raw_buffer_load_b64(rw, zo, 3 * CG_BYTES, AUX_DEV);
      zb = __builtin_amdgcn_raw_buffer_load_b64(rw, zo, 3 * CG_BYTES + 4 * PART_BYTES, AUX_DEV);
      __builtin_amdgcn_sched_barrier(0);
      u32 bad = 0;
#pragma unroll
      for (int s2 = 0; s2 < 2; ++s2)
#pragma unroll
        for (int hf = 0; hf < 2; ++hf) d[s2][hf] = own[s2][hf];
#pragma unroll
      for (int i = 0; i < 7; ++i) {
#pragma unroll
        for (int s2 = 0; s2 < 2; ++s2)
#pragma unroll
          for (int hf = 0; hf < 2; ++hf) d[s2][hf] = mfma32(sel[hf], x[i][s2], d[s2][hf]);
        bad |= xor9(x[i][0], x[i][1], xc[i]) ^ tag;
      }
      if (cg == 2) bad |= (za[0] ^ tag) | (zb[0] ^ tag);
      if (__all(bad == 0)) break;
      __builtin_amdgcn_s_sleep(8);
      asm volatile("" ::: "memory");
    }
#pragma unroll
    for (int s2 = 0; s2 < 2; ++s2)
#pragma unroll
      for (int hf = 0; hf < 2; ++hf)
        sm.kv[4 * (64 * cg + 4 * c + 2 * s2 + (g & 1)) + 2 * hf + (g >> 1) + 16 * cg + c] =
            (u32x2){pk_bf16(d[s2][hf][0], d[s2][hf][1]), pk_bf16(d[s2][hf][2], d[s2][hf][3])};
    if (cg == 2) {
      const f32x4 zs = __builtin_amdgcn_mfma_f32_16x16x4f32(1.0f, __uint_as_float(za[1]) + __uint_as_float(zb[1]),
                                                            (f32x4){0, 0, 0, 0}, 0, 0, 0);
      if (l < 16) ((float*)sm.z)[l] = lo_f(pk_bf16(zs[0], 0.f));
    }
    lds_signal(cg == 2 ? CNT(C_KV2) : CNT(C_KV01));
    if (cg == 2 && j == 0) {
      lds_wait<1>(CNT(C_KV01), 2);
      if (l == 0) __builtin_amdgcn_raw_buffer_store_b32(tag, rw, 0, EPOCH_OFF, AUX_DEV);
    }
  } else {
    const int wa = w - 3;
    const u32 row0 = rowb + wa * 32;
    const u32 off_frag = (row0 + (c >> 1)) * 384 + (2 * g + (c & 1)) * 16;
    auto rk = __builtin_amdgcn_make_buffer_rsrc((void*)K, 0, TBYTES, 0x00020000);
    auto rq = __builtin_amdgcn_make_buffer_rsrc((void*)Q, 0, TBYTES, 0x00020000);
    auto ro = __builtin_amdgcn_make_buffer_rsrc((void*)O, 0, TBYTES, 0x00020000);
    u32x4 om[3][2], xf[12];
    u32 cpf = 0;
    if (wa == 4) {
      u64 pc;
      asm volatile("s_getpc_b64 %0" : "=s"(pc));
      const u32 off = (((b >> 3) & 3) * 64 + l) * 64;
      cpf = *(const GAS u32*)(pc + (off < CODELEN ? off : 0));
    }
    if (wa >= 4) __builtin_amdgcn_s_barrier();
    __builtin_amdgcn_sched_barrier(0);
#pragma unroll
    for (int n = 0; n < 12; ++n)
      xf[n] = __builtin_amdgcn_raw_buffer_load_b128(rk, off_frag + (n % 3) * 128, (n / 3) * 3072, AUX_NT);
    __builtin_amdgcn_sched_barrier(0);
    if (wa < 4) __builtin_amdgcn_s_barrier();
    else lds_add(CNT(C_GATE));
    asm volatile("" ::"v"(cpf));
    lds_wait<1>(CNT(C_OMEGA), 1);
#pragma unroll
    for (int i = 0; i < 3; ++i)
#pragma unroll
      for (int p = 0; p < 2; ++p) om[i][p] = sm.om[2 * i + p][l];
    {
      u32x4 phiT;
#pragma unroll
      for (int a = 0; a < 4; ++a) {
        float ss = 0.f;
        f32x4 d0 = {0, 0, 0, 0}, d1 = {0, 0, 0, 0};
#pragma unroll
        for (int i = 0; i < 3; ++i) {
          u32x4 ks;
#pragma unroll
          for (int e = 0; e < 4; ++e) {
            ks[e] = pk_bf16(lo_f(xf[3 * a + i][e]) * scale, hi_f(xf[3 * a + i][e]) * scale);
            ss = sumsq2(ks[e], ss);
          }
          d0 = mfma32(ks, om[i][0], d0);
          d1 = mfma32(ks, om[i][1], d1);
        }
        const f32x4 sq = __builtin_amdgcn_mfma_f32_16x16x4f32(ss, 1.0f, (f32x4){0, 0, 0, 0}, 0, 0, 0);
        phiT[a] = phi2_bf16(d0[0] + d1[1], sq[0] + sq[1], d0[2] + d1[3], sq[2] + sq[3]);
      }
      sm.phik[wa][l] = phiT;
    }
    lds_signal(CNT(C_PHIK));
    lds_wait<1>(CNT(C_GATE), 7);
    if (!(__builtin_amdgcn_s_getreg(20) & 1)) __builtin_amdgcn_s_sleep(28);
    __builtin_amdgcn_sched_barrier(0);
#pragma unroll
    for (int n = 0; n < WQ; ++n) {
      xf[n] = __builtin_amdgcn_raw_buffer_load_b128(rq, off_frag + (n % 3) * 128, (n / 3) * 3072, AUX_NT);
      __builtin_amdgcn_sched_barrier(0);
    }
    const bool odd = c & 1;
    const u32 off_row = (row0 + 2 * g) * 384;
    u32x2 phq[2], kv2[4];
    f32x4 zf, ph[2];
    auto part2 = [&](int u) {
      const float dp = fmaf(ph[u][0], zf[0], ph[u][1] * zf[1]) + fmaf(ph[u][2], zf[2], ph[u][3] * zf[3]);
      const f32x4 den = __builtin_amdgcn_mfma_f32_16x16x4f32(1.0f, dp, (f32x4){1e-6f, 1e-6f, 1e-6f, 1e-6f}, 0, 0, 0);
      const float inv = __builtin_amdgcn_rcpf(den[0]);
      phq[u] = (u32x2){pk_bf16(ph[u][0] * inv, ph[u][1] * inv), pk_bf16(ph[u][2] * inv, ph[u][3] * inv)};
      f32x4 num[4];
#pragma unroll
      for (int t = 0; t < 4; ++t) num[t] = mfma16(phq[u], kv2[t], (f32x4){0, 0, 0, 0});
#pragma unroll
      for (int r = 0; r < 4; ++r)
        __builtin_amdgcn_raw_buffer_store_b64((u32x2){pk_bf16(num[0][r], num[1][r]), pk_bf16(num[2][r], num[3][r])}, ro,
                                              off_row + 8 * c + 256 + (r >> 1) * 384, (16 * u + 8 * (r & 1)) * 384,
                                              AUX_PLAIN);
    };
#pragma unroll
    for (int u = 0; u < 2; ++u) {
      float ss[2];
      f32x4 d[2][2];
#pragma unroll
      for (int a = 0; a < 2; ++a) {
        ss[a] = 0.f;
        d[a][0] = (f32x4){0, 0, 0, 0};
        d[a][1] = (f32x4){0, 0, 0, 0};
#pragma unroll
        for (int i = 0; i < 3; ++i) {
          const int n = 6 * u + 3 * a + i;
          if (n + WQ < 12) {
            asm volatile("" : "+v"(xf[n]));
            __builtin_amdgcn_sched_barrier(0);
            xf[n + WQ] = __builtin_amdgcn_raw_buffer_load_b128(rq, off_frag + ((n + WQ) % 3) * 128,
                                                               ((n + WQ) / 3) * 3072, AUX_NT);
            __builtin_amdgcn_sched_barrier(0);
          }
          if (n == QMID) lds_add(CNT(C_QMID));
          if (n == QLATE) lds_add(CNT(C_QLATE));
          if (n == QEARLY) {
            lds_wait<0>(CNT(C_KV2), 1);
            zf = sm.z[g];
#pragma unroll
            for (int t = 0; t < 4; ++t) kv2[t] = sm.kv[4 * (128 + 4 * c + t) + g + 32 + c];
            part2(0);
          }
#pragma unroll
          for (int e = 0; e < 4; ++e) ss[a] = sumsq2(xf[n][e], ss[a]);
          d[a][0] = mfma32(om[i][0], xf[n], d[a][0]);
          d[a][1] = mfma32(om[i][1], xf[n], d[a][1]);
        }
      }
      const float st = (odd ? ss[1] : ss[0]) + swap1(odd ? ss[0] : ss[1]);
      const f32x4 sq = __builtin_amdgcn_mfma_f32_16x16x4f32(1.0f, st, (f32x4){0, 0, 0, 0}, 0, 0, 0);
      const float sl = scale * 1.44269504f, hq = sq[0] * (-0.5f * scale * sl);
#pragma unroll
      for (int r = 0; r < 4; ++r) {
        const float pr = (odd ? d[1][1][r] : d[0][0][r]) + swap1(odd ? d[0][1][r] : d[1][0][r]);
        ph[u][r] = fmaf(__builtin_amdgcn_exp2f(fmaf(pr, sl, hq)), 0.25f, 1e-6f);
      }
    }
    part2(1);
    lds_wait<0>(CNT(C_KV01), 2);
    u32x2 kvf[8];
#pragma unroll
    for (int t = 0; t < 8; ++t) kvf[t] = sm.kv[4 * (8 * c + t) + g + 2 * c + (t >> 2)];
#pragma unroll
    for (int u = 0; u < 2; ++u) {
      f32x4 num[8];
#pragma unroll
      for (int t = 0; t < 8; ++t) num[t] = mfma16(phq[u], kvf[t], (f32x4){0, 0, 0, 0});
#pragma unroll
      for (int r = 0; r < 4; ++r)
        __builtin_amdgcn_raw_buffer_store_b128((u32x4){pk_bf16(num[0][r], num[1][r]), pk_bf16(num[2][r], num[3][r]),
                                                       pk_bf16(num[4][r], num[5][r]), pk_bf16(num[6][r], num[7][r])},
                                               ro, off_row + 16 * c + (r >> 1) * 384, (16 * u + 8 * (r & 1)) * 384, AUX_NT);
    }
  }
}

extern "C" void kernel_launch(void* const* d_in, const int* in_sizes, int n_in, void* d_out, int out_size,
                              void* d_ws, size_t ws_size, hipStream_t stream) {
  const float scale = 0.26864248f;
  rfa_fused<<<H * NCH, 704, 0, stream>>>((const u32*)d_in[0], (const u32*)d_in[1], (const u32*)d_in[2],
                                         (const u32*)d_in[3], (u32*)d_out, (u32*)d_ws, scale);
}
```

```cpp
#define PMAP 1
#define PWR 2
#define PLIN 0
#include <hip/hip_runtime.h>
#include <stdint.h>

typedef unsigned u32;
typedef unsigned long long u64;
typedef u32 u32x4 __attribute__((ext_vector_type(4)));
typedef u32 u32x2 __attribute__((ext_vector_type(2)));
typedef float f32x4 __attribute__((ext_vector_type(4)));
typedef float f32x2 __attribute__((ext_vector_type(2)));
typedef __bf16 bf16x8 __attribute__((ext_vector_type(8)));
typedef __bf16 bf16x2 __attribute__((ext_vector_type(2)));
typedef short s16x4 __attribute__((ext_vector_type(4)));
#define LAS __attribute__((address_space(3)))
#define GAS __attribute__((address_space(1)))

constexpr int H = 32, S = 2048, D = 192, M = 16;
constexpr int CH = 256;
constexpr int NCH = S / CH;
constexpr u32 TBYTES = H * S * D * 2;
constexpr u32 CG_BYTES = 2304;
constexpr u32 PART_BYTES = 3 * CG_BYTES + 256;
constexpr u32 EPOCH_OFF = NCH * PART_BYTES;
constexpr u32 HEAD_BYTES = EPOCH_OFF + 256;
constexpr int AUX_NT = 2, AUX_DEV = 16, AUX_PLAIN = 0;
constexpr int WQ = 4;
constexpr int QMID = 2, QLATE = 11 - WQ, QEARLY = 9;
#define CODELEN 10880
#define XMAP 0

__device__ __forceinline__ float lo_f(u32 x) { return __uint_as_float(x << 16); }
__device__ __forceinline__ float hi_f(u32 x) { return __uint_as_float(x & 0xffff0000u); }
__device__ __forceinline__ u32 pk_bf16(float a, float b) {
  f32x2 v = {a, b};
  bf16x2 r = __builtin_convertvector(v, bf16x2);
  return __builtin_bit_cast(u32, r);
}
__device__ __forceinline__ f32x4 mfma32(u32x4 a, u32x4 b, f32x4 c) {
  return __builtin_amdgcn_mfma_f32_16x16x32_bf16(__builtin_bit_cast(bf16x8, a), __builtin_bit_cast(bf16x8, b), c, 0, 0, 0);
}
__device__ __forceinline__ f32x4 mfma16(u32x2 a, u32x2 b, f32x4 c) {
  return __builtin_amdgcn_mfma_f32_16x16x16bf16_1k(__builtin_bit_cast(s16x4, a), __builtin_bit_cast(s16x4, b), c, 0, 0, 0);
}
__device__ __forceinline__ float sumsq2(u32 p, float acc) {
  return __builtin_amdgcn_fdot2_f32_bf16(__builtin_bit_cast(bf16x2, p), __builtin_bit_cast(bf16x2, p), acc, false);
}
__device__ __forceinline__ u32 phi2_bf16(float p0, float s0, float p1, float s1) {
  const u32 pp = pk_bf16(p0, p1), sp = pk_bf16(s0, s1);
  const u32 tp = pk_bf16(fmaf(lo_f(sp), -0.5f, lo_f(pp)), fmaf(hi_f(sp), -0.5f, hi_f(pp)));
  const u32 ep = pk_bf16(__expf(lo_f(tp)), __expf(hi_f(tp)));
  return pk_bf16(fmaf(lo_f(ep), 0.25f, 1e-6f), fmaf(hi_f(ep), 0.25f, 1e-6f));
}
__device__ __forceinline__ float swap1(float x) {
  return __builtin_bit_cast(float, __builtin_amdgcn_update_dpp(0, __builtin_bit_cast(int, x), 0xB1, 0xf, 0xf, true));
}
__device__ __forceinline__ void lds_add(LAS u32* p) {
  u64 sv;
  asm volatile("s_mov_b64 %0, exec\n\ts_mov_b64 exec, 1\n\tds_add_u32 %1, %2\n\ts_mov_b64 exec, %0"
               : "=&s"(sv) : "v"((u32)(size_t)p), "v"(1u) : "memory");
}
__device__ __forceinline__ void lds_signal(LAS u32* p) {
  asm volatile("s_waitcnt lgkmcnt(0)" ::: "memory");
  lds_add(p);
}
template <int SLEEP>
__device__ __forceinline__ void lds_wait(LAS u32* p, u32 want) {
  while (__builtin_amdgcn_readfirstlane(__hip_atomic_load(p, __ATOMIC_RELAXED, __HIP_MEMORY_SCOPE_WORKGROUP)) < want)
    if (SLEEP) __builtin_amdgcn_s_sleep(SLEEP);
  asm volatile("" ::: "memory");
}
__device__ __forceinline__ u32 xor3(u32 a, u32 b, u32 c) { return __builtin_amdgcn_bitop3_b32(a, b, c, 0x96); }
__device__ __forceinline__ u32 xor9(u32x4 a, u32x4 b, u32 c) {
  return xor3(xor3(a[0], a[1], a[2]), xor3(a[3], b[0], b[1]), xor3(b[2], b[3], c));
}

struct __attribute__((aligned(16))) Smem {
  u32x4 om[6][64];
  u32x4 phik[8][64];
  u32x2 kv[48 * 17];
  f32x4 z[4];
  u32 tag;
  u32 pad[3];
  u32 cnt[8];
};
enum { C_GATE, C_OMEGA, C_PHIK, C_QLATE, C_QMID, C_KV01, C_KV2 };
#define CNT(i) ((LAS u32*)&sm.cnt[i])

__global__ __launch_bounds__(704, 3) void rfa_fused(const u32* __restrict__ Q, const u32* __restrict__ K,
                                                    const u32* __restrict__ V, const u32* __restrict__ Om,
                                                    u32* __restrict__ O, u32* ws, float scale) {
  __shared__ Smem sm;
  const int b = blockIdx.x;
  const int h = (b & 7) * 4 + (b >> 6), j = (b >> 3) & 7;
  const int w = __builtin_amdgcn_readfirstlane(threadIdx.x >> 6);
  const int l = threadIdx.x & 63, g = l >> 4, c = l & 15;
  const u32 rowb = h * S + j * CH;
  if (w < 3) {
    const int cg = w;
    auto rw = __builtin_amdgcn_make_buffer_rsrc((char*)ws + h * HEAD_BYTES, 0, HEAD_BYTES, 0x00020000);
    if (cg == 0) {
      auto rom = __builtin_amdgcn_make_buffer_rsrc((void*)Om, 0, M * D * 2, 0x00020000);
      u32x4 om[6];
#pragma unroll
      for (int i = 0; i < 6; ++i)
        om[i] = __builtin_amdgcn_raw_buffer_load_b128(rom, c * 384 + 2 * g * 16, (i >> 1) * 128 + (i & 1) * 16, 0);
      __builtin_amdgcn_sched_barrier(0);
      __builtin_amdgcn_s_barrier();
      asm volatile("" ::: "memory");
#pragma unroll
      for (int i = 0; i < 6; ++i) sm.om[i][l] = om[i];
      lds_signal(CNT(C_OMEGA));
    } else if (cg == 1) {
      const char* base = g == 0 ? (const char*)Q : g == 1 ? (const char*)O : (const char*)V;
      const char* p = base + rowb * 384 + (c < 2 ? c * 98300 : 49152);
      if (g == 3) p = (const char*)ws + h * HEAD_BYTES;
      const u32 pf = __builtin_nontemporal_load((const u32*)p);
      if (l < 8) sm.cnt[l] = 0;
      asm volatile("s_waitcnt lgkmcnt(0)" ::: "memory");
      __builtin_amdgcn_s_barrier();
      asm volatile("" ::"v"(pf));
    } else {
      u32 vz;
      asm volatile("v_mov_b32 %0, 0" : "=v"(vz));
      const u32 epoch = __builtin_amdgcn_raw_buffer_load_b32(rw, vz, EPOCH_OFF, AUX_DEV);
      __builtin_amdgcn_sched_barrier(0);
      __builtin_amdgcn_s_barrier();
      asm volatile("" ::: "memory");
      sm.tag = epoch + 1u;
      lds_signal(CNT(C_PHIK));
    }
    auto rv = __builtin_amdgcn_make_buffer_rsrc((void*)V, 0, TBYTES, 0x00020000);
    const u32 voff = (rowb + 2 * g) * 384 + cg * 128 + 8 * c;
    lds_wait<1>(CNT(C_GATE), 4);
    __builtin_amdgcn_sched_barrier(0);
    u32x2 vf[8][4][2];
#pragma unroll
    for (int s = 0; s < 8; ++s)
#pragma unroll
      for (int a = 0; a < 4; ++a)
#pragma unroll
        for (int bb = 0; bb < 2; ++bb)
          vf[s][a][bb] = __builtin_amdgcn_raw_buffer_load_b64(rv, voff + bb * 384, (s * 4 + a) * 3072, AUX_NT);
    __builtin_amdgcn_sched_barrier(0);
    lds_add(CNT(C_GATE));
    lds_wait<1>(CNT(C_PHIK), 9);
    const u32 tag = sm.tag;
    f32x4 acc[4], zacc = {0, 0, 0, 0};
#pragma unroll
    for (int t = 0; t < 4; ++t) acc[t] = (f32x4){0, 0, 0, 0};
    const u32x4 ones = {0x3f803f80u, 0x3f803f80u, 0x3f803f80u, 0x3f803f80u};
#pragma unroll
    for (int s = 0; s < 8; ++s) {
      const u32x4 phiT = sm.phik[s][l];
      zacc = mfma32(ones, phiT, zacc);
#pragma unroll
      for (int t = 0; t < 4; ++t) {
        u32x4 bf;
        const u32 sel = (t & 1) ? 0x07060302u : 0x05040100u;
#pragma unroll
        for (int a = 0; a < 4; ++a) bf[a] = __builtin_amdgcn_perm(vf[s][a][1][t >> 1], vf[s][a][0][t >> 1], sel);
        acc[t] = mfma32(phiT, bf, acc[t]);
      }
    }
    const u32x4 p0 = {pk_bf16(acc[0][0], acc[0][1]), pk_bf16(acc[0][2], acc[0][3]), pk_bf16(acc[1][0], acc[1][1]),
                      pk_bf16(acc[1][2], acc[1][3])};
    const u32x4 p1 = {pk_bf16(acc[2][0], acc[2][1]), pk_bf16(acc[2][2], acc[2][3]), pk_bf16(acc[3][0], acc[3][1]),
                      pk_bf16(acc[3][2], acc[3][3])};
    {
      const u32 chk = xor9(p0, p1, tag);
      const u32 so = j * PART_BYTES + cg * CG_BYTES;
      __builtin_amdgcn_raw_buffer_store_b128(p0, rw, l * 16, so, AUX_DEV);
      __builtin_amdgcn_raw_buffer_store_b128(p1, rw, l * 16 + 1024, so, AUX_DEV);
      u32x4 cw;
#pragma unroll
      for (int e = 0; e < 4; ++e) cw[e] = __builtin_amdgcn_ds_bpermute(((4 * l + e) & 63) * 4, chk);
      if (g == 1) cw = (u32x4){tag, __float_as_uint(zacc[0]), tag, __float_as_uint(zacc[0])};
      if (l < (cg == 2 ? 32 : 16)) __builtin_amdgcn_raw_buffer_store_b128(cw, rw, l * 16 + 2048, so, AUX_DEV);
    }
    u32x4 sel[2];
    {
      const u32 one = (c & 1) ? 0x3f800000u : 0x00003f80u;
#pragma unroll
      for (int hf = 0; hf < 2; ++hf)
#pragma unroll
        for (int e = 0; e < 4; ++e) sel[hf][e] = (g == 2 * hf + (c >> 3) && ((c & 7) >> 1) == e) ? one : 0u;
    }
    f32x4 own[2][2];
#pragma unroll
    for (int hf = 0; hf < 2; ++hf) {
      own[0][hf] = mfma32(sel[hf], p0, (f32x4){0, 0, 0, 0});
      own[1][hf] = mfma32(sel[hf], p1, (f32x4){0, 0, 0, 0});
    }
    lds_wait<1>(cg == 2 ? CNT(C_QMID) : CNT(C_QLATE), 8);
    f32x4 d[2][2];
    u32x2 za, zb;
    for (int spin = 0; spin < (1 << 18); ++spin) {
      u32x4 x[7][2];
      u32 xc[7];
#pragma unroll
      for (int i = 0; i < 7; ++i) {
        const u32 pb = ((j + 1 + i) & 7) * PART_BYTES + cg * CG_BYTES;
        x[i][0] = __builtin_amdgcn_raw_buffer_load_b128(rw, l * 16, pb, AUX_DEV);
        x[i][1] = __builtin_amdgcn_raw_buffer_load_b128(rw, l * 16 + 1024, pb, AUX_DEV);
        xc[i] = __builtin_amdgcn_raw_buffer_load_b32(rw, l * 4 + 2048, pb, AUX_DEV);
      }
      const u32 zo = cg == 2 ? g * PART_BYTES + c * 16 : 0x80000000u;
      za = __builtin_amdgcn_raw_buffer_load_b64(rw, zo, 3 * CG_BYTES, AUX_DEV);
      zb = __builtin_amdgcn_raw_buffer_load_b64(rw, zo, 3 * CG_BYTES + 4 * PART_BYTES, AUX_DEV);
      __builtin_amdgcn_sched_barrier(0);
      u32 bad = 0;
#pragma unroll
      for (int s2 = 0; s2 < 2; ++s2)
#pragma unroll
        for (int hf = 0; hf < 2; ++hf) d[s2][hf] = own[s2][hf];
#pragma unroll
      for (int i = 0; i < 7; ++i) {
#pragma unroll
        for (int s2 = 0; s2 < 2; ++s2)
#pragma unroll
          for (int hf = 0; hf < 2; ++hf) d[s2][hf] = mfma32(sel[hf], x[i][s2], d[s2][hf]);
        bad |= xor9(x[i][0], x[i][1], xc[i]) ^ tag;
      }
      if (cg == 2) bad |= (za[0] ^ tag) | (zb[0] ^ tag);
      if (__all(bad == 0)) break;
      __builtin_amdgcn_s_sleep(8);
      asm volatile("" ::: "memory");
    }
#pragma unroll
    for (int s2 = 0; s2 < 2; ++s2)
#pragma unroll
      for (int hf = 0; hf < 2; ++hf)
        sm.kv[4 * (64 * cg + 4 * c + 2 * s2 + (g & 1)) + 2 * hf + (g >> 1) + 16 * cg + c] =
            (u32x2){pk_bf16(d[s2][hf][0], d[s2][hf][1]), pk_bf16(d[s2][hf][2], d[s2][hf][3])};
    if (cg == 2) {
      const f32x4 zs = __builtin_amdgcn_mfma_f32_16x16x4f32(1.0f, __uint_as_float(za[1]) + __uint_as_float(zb[1]),
                                                            (f32x4){0, 0, 0, 0}, 0, 0, 0);
      if (l < 16) ((float*)sm.z)[l] = lo_f(pk_bf16(zs[0], 0.f));
    }
    lds_signal(cg == 2 ? CNT(C_KV2) : CNT(C_KV01));
    if (cg == 2 && j == 0) {
      lds_wait<1>(CNT(C_KV01), 2);
      if (l == 0) __builtin_amdgcn_raw_buffer_store_b32(tag, rw, 0, EPOCH_OFF, AUX_DEV);
    }
  } else {
    const int wa = w - 3;
    const u32 row0 = rowb + wa * 32;
    const u32 off_frag = (row0 + (c >> 1)) * 384 + (2 * g + (c & 1)) * 16;
    auto rk = __builtin_amdgcn_make_buffer_rsrc((void*)K, 0, TBYTES, 0x00020000);
    auto rq = __builtin_amdgcn_make_buffer_rsrc((void*)Q, 0, TBYTES, 0x00020000);
    auto ro = __builtin_amdgcn_make_buffer_rsrc((void*)O, 0, TBYTES, 0x00020000);
    u32x4 om[3][2], xf[12];
    u32 cpf = 0;
    if (wa == 4) {
      u64 pc;
      asm volatile("s_getpc_b64 %0" : "=s"(pc));
      const u32 off = (((b >> 3) & 3) * 64 + l) * 64;
      cpf = *(const GAS u32*)(pc + (off < CODELEN ? off : 0));
    }
    if (wa >= 4) __builtin_amdgcn_s_barrier();
    __builtin_amdgcn_sched_barrier(0);
#pragma unroll
    for (int n = 0; n < 12; ++n)
      xf[n] = __builtin_amdgcn_raw_buffer_load_b128(rk, off_frag + (n % 3) * 128, (n / 3) * 3072, AUX_NT);
    __builtin_amdgcn_sched_barrier(0);
    if (wa < 4) __builtin_amdgcn_s_barrier();
    else lds_add(CNT(C_GATE));
    asm volatile("" ::"v"(cpf));
    lds_wait<1>(CNT(C_OMEGA), 1);
#pragma unroll
    for (int i = 0; i < 3; ++i)
#pragma unroll
      for (int p = 0; p < 2; ++p) om[i][p] = sm.om[2 * i + p][l];
    {
      u32x4 phiT;
#pragma unroll
      for (int a = 0; a < 4; ++a) {
        float ss = 0.f;
        f32x4 d0 = {0, 0, 0, 0}, d1 = {0, 0, 0, 0};
#pragma unroll
        for (int i = 0; i < 3; ++i) {
          u32x4 ks;
#pragma unroll
          for (int e = 0; e < 4; ++e) {
            ks[e] = pk_bf16(lo_f(xf[3 * a + i][e]) * scale, hi_f(xf[3 * a + i][e]) * scale);
            ss = sumsq2(ks[e], ss);
          }
          d0 = mfma32(ks, om[i][0], d0);
          d1 = mfma32(ks, om[i][1], d1);
        }
        const f32x4 sq = __builtin_amdgcn_mfma_f32_16x16x4f32(ss, 1.0f, (f32x4){0, 0, 0, 0}, 0, 0, 0);
        phiT[a] = phi2_bf16(d0[0] + d1[1], sq[0] + sq[1], d0[2] + d1[3], sq[2] + sq[3]);
      }
      sm.phik[wa][l] = phiT;
    }
    lds_signal(CNT(C_PHIK));
    lds_wait<1>(CNT(C_GATE), 7);
    if (!(__builtin_amdgcn_s_getreg(20) & 1)) __builtin_amdgcn_s_sleep(28);
    __builtin_amdgcn_sched_barrier(0);
#pragma unroll
    for (int n = 0; n < WQ; ++n) {
      xf[n] = __builtin_amdgcn_raw_buffer_load_b128(rq, off_frag + (n % 3) * 128, (n / 3) * 3072, AUX_NT);
      __builtin_amdgcn_sched_barrier(0);
    }
    const bool odd = c & 1;
    const u32 off_row = (row0 + 2 * g) * 384;
    u32x2 phq[2], kv2[4];
    f32x4 zf, ph[2];
    auto part2 = [&](int u) {
      const float dp = fmaf(ph[u][0], zf[0], ph[u][1] * zf[1]) + fmaf(ph[u][2], zf[2], ph[u][3] * zf[3]);
      const f32x4 den = __builtin_amdgcn_mfma_f32_16x16x4f32(1.0f, dp, (f32x4){1e-6f, 1e-6f, 1e-6f, 1e-6f}, 0, 0, 0);
      const float inv = __builtin_amdgcn_rcpf(den[0]);
      phq[u] = (u32x2){pk_bf16(ph[u][0] * inv, ph[u][1] * inv), pk_bf16(ph[u][2] * inv, ph[u][3] * inv)};
      f32x4 num[4];
#pragma unroll
      for (int t = 0; t < 4; ++t) num[t] = mfma16(phq[u], kv2[t], (f32x4){0, 0, 0, 0});
#pragma unroll
      for (int r = 0; r < 4; ++r)
        __builtin_amdgcn_raw_buffer_store_b64((u32x2){pk_bf16(num[0][r], num[1][r]), pk_bf16(num[2][r], num[3][r])}, ro,
                                              off_row + 8 * c + 256 + (r >> 1) * 384, (16 * u + 8 * (r & 1)) * 384,
                                              AUX_PLAIN);
    };
#pragma unroll
    for (int u = 0; u < 2; ++u) {
      float ss[2];
      f32x4 d[2][2];
#pragma unroll
      for (int a = 0; a < 2; ++a) {
        ss[a] = 0.f;
        d[a][0] = (f32x4){0, 0, 0, 0};
        d[a][1] = (f32x4){0, 0, 0, 0};
#pragma unroll
        for (int i = 0; i < 3; ++i) {
          const int n = 6 * u + 3 * a + i;
          if (n + WQ < 12) {
            asm volatile("" : "+v"(xf[n]));
            __builtin_amdgcn_sched_barrier(0);
            xf[n + WQ] = __builtin_amdgcn_raw_buffer_load_b128(rq, off_frag + ((n + WQ) % 3) * 128,
                                                               ((n + WQ) / 3) * 3072, AUX_NT);
            __builtin_amdgcn_sched_barrier(0);
          }
          if (n == QMID) lds_add(CNT(C_QMID));
          if (n == QLATE) lds_add(CNT(C_QLATE));
          if (n == QEARLY) {
            lds_wait<0>(CNT(C_KV2), 1);
            zf = sm.z[g];
#pragma unroll
            for (int t = 0; t < 4; ++t) kv2[t] = sm.kv[4 * (128 + 4 * c + t) + g + 32 + c];
            part2(0);
          }
#pragma unroll
          for (int e = 0; e < 4; ++e) ss[a] = sumsq2(xf[n][e], ss[a]);
          d[a][0] = mfma32(om[i][0], xf[n], d[a][0]);
          d[a][1] = mfma32(om[i][1], xf[n], d[a][1]);
        }
      }
      const float st = (odd ? ss[1] : ss[0]) + swap1(odd ? ss[0] : ss[1]);
      const f32x4 sq = __builtin_amdgcn_mfma_f32_16x16x4f32(1.0f, st, (f32x4){0, 0, 0, 0}, 0, 0, 0);
      const float sl = scale * 1.44269504f, hq = sq[0] * (-0.5f * scale * sl);
#pragma unroll
      for (int r = 0; r < 4; ++r) {
        const float pr = (odd ? d[1][1][r] : d[0][0][r]) + swap1(odd ? d[0][1][r] : d[1][0][r]);
        ph[u][r] = fmaf(__builtin_amdgcn_exp2f(fmaf(pr, sl, hq)), 0.25f, 1e-6f);
      }
    }
    part2(1);
    lds_wait<0>(CNT(C_KV01), 2);
    u32x2 kvf[8];
#pragma unroll
    for (int t = 0; t < 8; ++t) kvf[t] = sm.kv[4 * (8 * c + t) + g + 2 * c + (t >> 2)];
#pragma unroll
    for (int u = 0; u < 2; ++u) {
      f32x4 num[8];
#pragma unroll
      for (int t = 0; t < 8; ++t) num[t] = mfma16(phq[u], kvf[t], (f32x4){0, 0, 0, 0});
#pragma unroll
      for (int r = 0; r < 4; ++r)
        __builtin_amdgcn_raw_buffer_store_b128((u32x4){pk_bf16(num[0][r], num[1][r]), pk_bf16(num[2][r], num[3][r]),
                                                       pk_bf16(num[4][r], num[5][r]), pk_bf16(num[6][r], num[7][r])},
                                               ro, off_row + 16 * c + (r >> 1) * 384, (16 * u + 8 * (r & 1)) * 384, AUX_NT);
    }
  }
}

__device__ __forceinline__ u64 rt() { u64 t; __builtin_amdgcn_sched_barrier(0); asm volatile("s_memrealtime %0\n\ts_waitcnt lgkmcnt(0)" : "=s"(t) :: "memory"); __builtin_amdgcn_sched_barrier(0); return t; }
__global__ void next_k(u64* ts) { if (threadIdx.x == 0) ts[256] = rt(); }
__global__ void spin_k(u64* ts) {
  u64 tb = rt();
  u64 mn = ~0ull;
  for (int i = 0; i < 256; ++i) { u64 x = ts[i]; mn = x < mn ? x : mn; }
  u64 span = ts[256] - mn;
  u64 target = mn + 10000 + span;
  while (rt() < target && rt() < tb + 30000) __builtin_amdgcn_s_sleep(1);
}

__global__ __launch_bounds__(512) void probe_k(const u32* K, const u32* V, const u32* Q, u32* O, u64* ts) {
  const u64 t0 = __builtin_amdgcn_s_memrealtime();
  const int b0 = blockIdx.x;
  const int b = PMAP ? (b0 & 7) * 32 + (b0 >> 3) : b0;
  const int w = __builtin_amdgcn_readfirstlane(threadIdx.x >> 6);
  const int l = threadIdx.x & 63, g = l >> 4, c = l & 15;
  const u32 base = (b * 256 + w * 32 + (l >> 3)) * 384 + (l & 7) * 16;
  __amdgpu_buffer_rsrc_t r[3] = {__builtin_amdgcn_make_buffer_rsrc((void*)K, 0, 25165824, 0x00020000),
    __builtin_amdgcn_make_buffer_rsrc((void*)V, 0, 25165824, 0x00020000),
    __builtin_amdgcn_make_buffer_rsrc((void*)Q, 0, 25165824, 0x00020000)};
  auto ro = __builtin_amdgcn_make_buffer_rsrc((void*)O, 0, 25165824, 0x00020000);
  u32x4 a[3][12];
#pragma unroll
  for (int p = 0; p < 3; ++p)
#pragma unroll
    for (int k = 0; k < 3; ++k)
#pragma unroll
      for (int i = 0; i < 4; ++i) a[p][k * 4 + i] = PLIN ? __builtin_amdgcn_raw_buffer_load_b128(r[p], (b * 256 + w * 32) * 384 + l * 16, (k * 4 + i) * 1024, 2) : __builtin_amdgcn_raw_buffer_load_b128(r[p], base + k * 128, i * 3072, 2);
  u32x4 s = {0, 0, 0, 0};
#pragma unroll
  for (int t = 0; t < 3; ++t)
#pragma unroll
    for (int i = 0; i < 12; ++i) s ^= a[t][i];
  asm volatile("" : "+v"(s));
  __builtin_amdgcn_s_barrier();
  const u32 off_row = (b * 256 + w * 32 + 2 * g) * 384;
  if (PWR == 2) {
#pragma unroll
    for (int i = 0; i < 12; ++i) __builtin_amdgcn_raw_buffer_store_b128(s, ro, (b * 256 + w * 32) * 384 + l * 16, i * 1024, 2);
  }
  if (PWR == 1) {
#pragma unroll
  for (int i = 0; i < 8; ++i) {
    __builtin_amdgcn_raw_buffer_store_b128(s, ro, off_row + 16 * c + (i & 1) * 384, (i >> 1) * 3072, 2);
    __builtin_amdgcn_raw_buffer_store_b64((u32x2){s[0], s[1]}, ro, off_row + 8 * c + 256 + (i & 1) * 384, (i >> 1) * 3072, 2);
  }
  }
  if (threadIdx.x == 0) ts[b0] = t0;
}
extern "C" void kernel_launch(void* const* d_in, const int* in_sizes, int n_in, void* d_out, int out_size,
                              void* d_ws, size_t ws_size, hipStream_t stream) {
  const float scale = 0.26864248f;
  u64* ts = (u64*)((char*)d_ws + (64 << 20));
  probe_k<<<256, 512, 0, stream>>>((const u32*)d_in[1], (const u32*)d_in[2], (const u32*)d_in[0], (u32*)d_out, ts);
  next_k<<<1, 64, 0, stream>>>(ts);
  rfa_fused<<<H * NCH, 704, 0, stream>>>((const u32*)d_in[0], (const u32*)d_in[1], (const u32*)d_in[2], (const u32*)d_in[3], (u32*)d_out, (u32*)d_ws, scale);
  spin_k<<<1, 64, 0, stream>>>(ts);
}
```

```cpp
#include <hip/hip_runtime.h>
#include <stdint.h>

typedef unsigned u32;
typedef unsigned long long u64;
typedef u32 u32x4 __attribute__((ext_vector_type(4)));
typedef u32 u32x2 __attribute__((ext_vector_type(2)));
typedef float f32x4 __attribute__((ext_vector_type(4)));
typedef float f32x2 __attribute__((ext_vector_type(2)));
typedef __bf16 bf16x8 __attribute__((ext_vector_type(8)));
typedef __bf16 bf16x2 __attribute__((ext_vector_type(2)));
typedef short s16x4 __attribute__((ext_vector_type(4)));
#define LAS __attribute__((address_space(3)))
#define GAS __attribute__((address_space(1)))

constexpr int H = 32, S = 2048, D = 192, M = 16;
constexpr int CH = 256;
constexpr int NCH = S / CH;
constexpr u32 TBYTES = H * S * D * 2;
constexpr u32 CG_BYTES = 2304;
constexpr u32 PART_BYTES = 3 * CG_BYTES + 256;
constexpr u32 EPOCH_OFF = NCH * PART_BYTES;
constexpr u32 HEAD_BYTES = EPOCH_OFF + 256;
constexpr int AUX_NT = 2, AUX_DEV = 16, AUX_PLAIN = 0;
constexpr int WQ = 4;
constexpr int QMID = 2, QLATE = 11 - WQ, QEARLY = 9;
#define CODELEN 11264
#define KS 3
#define XMAP 0

__device__ __forceinline__ float lo_f(u32 x) { return __uint_as_float(x << 16); }
__device__ __forceinline__ float hi_f(u32 x) { return __uint_as_float(x & 0xffff0000u); }
__device__ __forceinline__ u32 pk_bf16(float a, float b) {
  f32x2 v = {a, b};
  bf16x2 r = __builtin_convertvector(v, bf16x2);
  return __builtin_bit_cast(u32, r);
}
__device__ __forceinline__ f32x4 mfma32(u32x4 a, u32x4 b, f32x4 c) {
  return __builtin_amdgcn_mfma_f32_16x16x32_bf16(__builtin_bit_cast(bf16x8, a), __builtin_bit_cast(bf16x8, b), c, 0, 0, 0);
}
__device__ __forceinline__ f32x4 mfma16(u32x2 a, u32x2 b, f32x4 c) {
  return __builtin_amdgcn_mfma_f32_16x16x16bf16_1k(__builtin_bit_cast(s16x4, a), __builtin_bit_cast(s16x4, b), c, 0, 0, 0);
}
__device__ __forceinline__ float sumsq2(u32 p, float acc) {
  return __builtin_amdgcn_fdot2_f32_bf16(__builtin_bit_cast(bf16x2, p), __builtin_bit_cast(bf16x2, p), acc, false);
}
__device__ __forceinline__ u32 phi2_bf16(float p0, float s0, float p1, float s1) {
  const u32 pp = pk_bf16(p0, p1), sp = pk_bf16(s0, s1);
  const u32 tp = pk_bf16(fmaf(lo_f(sp), -0.5f, lo_f(pp)), fmaf(hi_f(sp), -0.5f, hi_f(pp)));
  const u32 ep = pk_bf16(__expf(lo_f(tp)), __expf(hi_f(tp)));
  return pk_bf16(fmaf(lo_f(ep), 0.25f, 1e-6f), fmaf(hi_f(ep), 0.25f, 1e-6f));
}
__device__ __forceinline__ float swap1(float x) {
  return __builtin_bit_cast(float, __builtin_amdgcn_update_dpp(0, __builtin_bit_cast(int, x), 0xB1, 0xf, 0xf, true));
}
__device__ __forceinline__ void lds_add(LAS u32* p) {
  u64 sv;
  asm volatile("s_mov_b64 %0, exec\n\ts_mov_b64 exec, 1\n\tds_add_u32 %1, %2\n\ts_mov_b64 exec, %0"
               : "=&s"(sv) : "v"((u32)(size_t)p), "v"(1u) : "memory");
}
__device__ __forceinline__ void lds_signal(LAS u32* p) {
  asm volatile("s_waitcnt lgkmcnt(0)" ::: "memory");
  lds_add(p);
}
template <int SLEEP>
__device__ __forceinline__ void lds_wait(LAS u32* p, u32 want) {
  while (__builtin_amdgcn_readfirstlane(__hip_atomic_load(p, __ATOMIC_RELAXED, __HIP_MEMORY_SCOPE_WORKGROUP)) < want)
    if (SLEEP) __builtin_amdgcn_s_sleep(SLEEP);
  asm volatile("" ::: "memory");
}
__device__ __forceinline__ u32 xor3(u32 a, u32 b, u32 c) { return __builtin_amdgcn_bitop3_b32(a, b, c, 0x96); }
__device__ __forceinline__ u32 xor9(u32x4 a, u32x4 b, u32 c) {
  return xor3(xor3(a[0], a[1], a[2]), xor3(a[3], b[0], b[1]), xor3(b[2], b[3], c));
}

struct __attribute__((aligned(16))) Smem {
  u32x4 om[6][64];
  u32x4 phik[8][64];
  u32x2 kv[48 * 17];
  f32x4 z[4];
  u32 tag;
  u32 pad[3];
  u32 cnt[8];
};
enum { C_GATE, C_OMEGA, C_PHIK, C_QLATE, C_QMID, C_KV01, C_KV2 };
#define CNT(i) ((LAS u32*)&sm.cnt[i])

__global__ __launch_bounds__(704, 3) void rfa_fused(const u32* __restrict__ Q, const u32* __restrict__ K,
                                                    const u32* __restrict__ V, const u32* __restrict__ Om,
                                                    u32* __restrict__ O, u32* ws, float scale) {
  __shared__ Smem sm;
  const int b = blockIdx.x;
  const int h = (b & 7) * 4 + (b >> 6), j = (b >> 3) & 7;
  const int w = __builtin_amdgcn_readfirstlane(threadIdx.x >> 6);
  const int l = threadIdx.x & 63, g = l >> 4, c = l & 15;
  const u32 rowb = h * S + j * CH;
  if (w < 3) {
    const int cg = w;
    auto rw = __builtin_amdgcn_make_buffer_rsrc((char*)ws + h * HEAD_BYTES, 0, HEAD_BYTES, 0x00020000);
    if (cg == 0) {
      __builtin_amdgcn_s_barrier();
    } else if (cg == 1) {
      const char* base = g == 0 ? (const char*)Q : g == 1 ? (const char*)O : (const char*)V;
      const char* p = base + rowb * 384 + (c < 2 ? c * 98300 : 49152);
      if (g == 3) p = (const char*)ws + h * HEAD_BYTES;
      const u32 pf = __builtin_nontemporal_load((const u32*)p);
      if (l < 8) sm.cnt[l] = 0;
      asm volatile("s_waitcnt lgkmcnt(0)" ::: "memory");
      __builtin_amdgcn_s_barrier();
      asm volatile("" ::"v"(pf));
    } else {
      u32 vz;
      asm volatile("v_mov_b32 %0, 0" : "=v"(vz));
      const u32 epoch = __builtin_amdgcn_raw_buffer_load_b32(rw, vz, EPOCH_OFF, AUX_DEV);
      __builtin_amdgcn_sched_barrier(0);
      __builtin_amdgcn_s_barrier();
      asm volatile("" ::: "memory");
      sm.tag = epoch + 1u;
      lds_signal(CNT(C_PHIK));
    }
    auto rv = __builtin_amdgcn_make_buffer_rsrc((void*)V, 0, TBYTES, 0x00020000);
    const u32 voff = (rowb + 2 * g) * 384 + cg * 128 + 8 * c;
    lds_wait<1>(CNT(C_GATE), 4);
    __builtin_amdgcn_sched_barrier(0);
    u32x2 vf[8][4][2];
#pragma unroll
    for (int s = 0; s < 8; ++s)
#pragma unroll
      for (int a = 0; a < 4; ++a)
#pragma unroll
        for (int bb = 0; bb < 2; ++bb)
          vf[s][a][bb] = __builtin_amdgcn_raw_buffer_load_b64(rv, voff + bb * 384, (s * 4 + a) * 3072, AUX_NT);
    __builtin_amdgcn_sched_barrier(0);
    lds_add(CNT(C_GATE));
    lds_wait<1>(CNT(C_PHIK), 9);
    const u32 tag = sm.tag;
    f32x4 acc[4], zacc = {0, 0, 0, 0};
#pragma unroll
    for (int t = 0; t < 4; ++t) acc[t] = (f32x4){0, 0, 0, 0};
    const u32x4 ones = {0x3f803f80u, 0x3f803f80u, 0x3f803f80u, 0x3f803f80u};
#pragma unroll
    for (int s = 0; s < 8; ++s) {
      const u32x4 phiT = sm.phik[s][l];
      zacc = mfma32(ones, phiT, zacc);
#pragma unroll
      for (int t = 0; t < 4; ++t) {
        u32x4 bf;
        const u32 sel = (t & 1) ? 0x07060302u : 0x05040100u;
#pragma unroll
        for (int a = 0; a < 4; ++a) bf[a] = __builtin_amdgcn_perm(vf[s][a][1][t >> 1], vf[s][a][0][t >> 1], sel);
        acc[t] = mfma32(phiT, bf, acc[t]);
      }
    }
    const u32x4 p0 = {pk_bf16(acc[0][0], acc[0][1]), pk_bf16(acc[0][2], acc[0][3]), pk_bf16(acc[1][0], acc[1][1]),
                      pk_bf16(acc[1][2], acc[1][3])};
    const u32x4 p1 = {pk_bf16(acc[2][0], acc[2][1]), pk_bf16(acc[2][2], acc[2][3]), pk_bf16(acc[3][0], acc[3][1]),
                      pk_bf16(acc[3][2], acc[3][3])};
    {
      const u32 chk = xor9(p0, p1, tag);
      const u32 so = j * PART_BYTES + cg * CG_BYTES;
      __builtin_amdgcn_raw_buffer_store_b128(p0, rw, l * 16, so, AUX_DEV);
      __builtin_amdgcn_raw_buffer_store_b128(p1, rw, l * 16 + 1024, so, AUX_DEV);
      u32x4 cw;
#pragma unroll
      for (int e = 0; e < 4; ++e) cw[e] = __builtin_amdgcn_ds_bpermute(((4 * l + e) & 63) * 4, chk);
      if (g == 1) cw = (u32x4){tag, __float_as_uint(zacc[0]), tag, __float_as_uint(zacc[0])};
      if (l < (cg == 2 ? 32 : 16)) __builtin_amdgcn_raw_buffer_store_b128(cw, rw, l * 16 + 2048, so, AUX_DEV);
    }
    u32x4 sel[2];
    {
      const u32 one = (c & 1) ? 0x3f800000u : 0x00003f80u;
#pragma unroll
      for (int hf = 0; hf < 2; ++hf)
#pragma unroll
        for (int e = 0; e < 4; ++e) sel[hf][e] = (g == 2 * hf + (c >> 3) && ((c & 7) >> 1) == e) ? one : 0u;
    }
    f32x4 own[2][2];
#pragma unroll
    for (int hf = 0; hf < 2; ++hf) {
      own[0][hf] = mfma32(sel[hf], p0, (f32x4){0, 0, 0, 0});
      own[1][hf] = mfma32(sel[hf], p1, (f32x4){0, 0, 0, 0});
    }
    lds_wait<1>(cg == 2 ? CNT(C_QMID) : CNT(C_QLATE), 8);
    f32x4 d[2][2];
    u32x2 za, zb;
    for (int spin = 0; spin < (1 << 18); ++spin) {
      u32x4 x[7][2];
      u32 xc[7];
#pragma unroll
      for (int i = 0; i < 7; ++i) {
        const u32 pb = ((j + 1 + i) & 7) * PART_BYTES + cg * CG_BYTES;
        x[i][0] = __builtin_amdgcn_raw_buffer_load_b128(rw, l * 16, pb, AUX_DEV);
        x[i][1] = __builtin_amdgcn_raw_buffer_load_b128(rw, l * 16 + 1024, pb, AUX_DEV);
        xc[i] = __builtin_amdgcn_raw_buffer_load_b32(rw, l * 4 + 2048, pb, AUX_DEV);
      }
      const u32 zo = cg == 2 ? g * PART_BYTES + c * 16 : 0x80000000u;
      za = __builtin_amdgcn_raw_buffer_load_b64(rw, zo, 3 * CG_BYTES, AUX_DEV);
      zb = __builtin_amdgcn_raw_buffer_load_b64(rw, zo, 3 * CG_BYTES + 4 * PART_BYTES, AUX_DEV);
      __builtin_amdgcn_sched_barrier(0);
      u32 bad = 0;
#pragma unroll
      for (int s2 = 0; s2 < 2; ++s2)
#pragma unroll
        for (int hf = 0; hf < 2; ++hf) d[s2][hf] = own[s2][hf];
#pragma unroll
      for (int i = 0; i < 7; ++i) {
#pragma unroll
        for (int s2 = 0; s2 < 2; ++s2)
#pragma unroll
          for (int hf = 0; hf < 2; ++hf) d[s2][hf] = mfma32(sel[hf], x[i][s2], d[s2][hf]);
        bad |= xor9(x[i][0], x[i][1], xc[i]) ^ tag;
      }
      if (cg == 2) bad |= (za[0] ^ tag) | (zb[0] ^ tag);
      if (__all(bad == 0)) break;
      __builtin_amdgcn_s_sleep(8);
      asm volatile("" ::: "memory");
    }
#pragma unroll
    for (int s2 = 0; s2 < 2; ++s2)
#pragma unroll
      for (int hf = 0; hf < 2; ++hf)
        sm.kv[4 * (64 * cg + 4 * c + 2 * s2 + (g & 1)) + 2 * hf + (g >> 1) + 16 * cg + c] =
            (u32x2){pk_bf16(d[s2][hf][0], d[s2][hf][1]), pk_bf16(d[s2][hf][2], d[s2][hf][3])};
    if (cg == 2) {
      const f32x4 zs = __builtin_amdgcn_mfma_f32_16x16x4f32(1.0f, __uint_as_float(za[1]) + __uint_as_float(zb[1]),
                                                            (f32x4){0, 0, 0, 0}, 0, 0, 0);
      if (l < 16) ((float*)sm.z)[l] = lo_f(pk_bf16(zs[0], 0.f));
    }
    lds_signal(cg == 2 ? CNT(C_KV2) : CNT(C_KV01));
    if (cg == 2 && j == 0) {
      lds_wait<1>(CNT(C_KV01), 2);
      if (l == 0) __builtin_amdgcn_raw_buffer_store_b32(tag, rw, 0, EPOCH_OFF, AUX_DEV);
    }
  } else {
    const int wa = w - 3;
    const u32 row0 = rowb + wa * 32;
    const u32 off_frag = (row0 + (c >> 1)) * 384 + (2 * g + (c & 1)) * 16;
    auto rk = __builtin_amdgcn_make_buffer_rsrc((void*)K, 0, TBYTES, 0x00020000);
    auto rq = __builtin_amdgcn_make_buffer_rsrc((void*)Q, 0, TBYTES, 0x00020000);
    auto ro = __builtin_amdgcn_make_buffer_rsrc((void*)O, 0, TBYTES, 0x00020000);
    u32x4 om[3][2], xf[12];
    u32 cpf = 0;
    if (wa == 4) {
      u64 pc;
      asm volatile("s_getpc_b64 %0" : "=s"(pc));
      const u32 off = (((b >> 3) & 3) * 64 + l) * 64;
      cpf = *(const GAS u32*)(pc + (off < CODELEN ? off : 0));
    }
    if (wa >= 4) __builtin_amdgcn_s_barrier();
    __builtin_amdgcn_sched_barrier(0);
#pragma unroll
    for (int n = 0; n < 12; ++n)
      xf[n] = __builtin_amdgcn_raw_buffer_load_b128(rk, off_frag + (n % 3) * 128, (n / 3) * 3072, AUX_NT);
    __builtin_amdgcn_sched_barrier(0);
    {
      auto rom = __builtin_amdgcn_make_buffer_rsrc((void*)Om, 0, wa == 0 ? M * D * 2 : 0, 0x00020000);
#pragma unroll
      for (int i = 0; i < 6; ++i) om[i >> 1][i & 1] = __builtin_amdgcn_raw_buffer_load_b128(rom, l * 16, i * 1024, 0);
    }
    __builtin_amdgcn_sched_barrier(0);
    if (wa < 4) __builtin_amdgcn_s_barrier();
    else lds_add(CNT(C_GATE));
    asm volatile("" ::"v"(cpf));
    if (wa == 0) {
#pragma unroll
      for (int i = 0; i < 6; ++i) {
        const u32 n = 64 * i + l, m = n / 24, cc = n - 24 * m;
        sm.om[(cc >> 3) * 2 + (cc & 1)][16 * ((cc & 7) >> 1) + m] = om[i >> 1][i & 1];
      }
      lds_signal(CNT(C_OMEGA));
    }
    lds_wait<1>(CNT(C_OMEGA), 1);
#pragma unroll
    for (int i = 0; i < 3; ++i)
#pragma unroll
      for (int p = 0; p < 2; ++p) om[i][p] = sm.om[2 * i + p][l];
    {
      u32x4 phiT;
#pragma unroll
      for (int a = 0; a < 4; ++a) {
        float ss = 0.f;
        f32x4 d0 = {0, 0, 0, 0}, d1 = {0, 0, 0, 0};
#pragma unroll
        for (int i = 0; i < 3; ++i) {
          u32x4 ks;
#pragma unroll
          for (int e = 0; e < 4; ++e) {
            ks[e] = pk_bf16(lo_f(xf[3 * a + i][e]) * scale, hi_f(xf[3 * a + i][e]) * scale);
            ss = sumsq2(ks[e], ss);
          }
          d0 = mfma32(ks, om[i][0], d0);
          d1 = mfma32(ks, om[i][1], d1);
          __builtin_amdgcn_sched_barrier(0);
          __builtin_amdgcn_s_sleep(KS);
          __builtin_amdgcn_sched_barrier(0);
        }
        const f32x4 sq = __builtin_amdgcn_mfma_f32_16x16x4f32(ss, 1.0f, (f32x4){0, 0, 0, 0}, 0, 0, 0);
        phiT[a] = phi2_bf16(d0[0] + d1[1], sq[0] + sq[1], d0[2] + d1[3], sq[2] + sq[3]);
      }
      sm.phik[wa][l] = phiT;
    }
    lds_signal(CNT(C_PHIK));
    lds_wait<1>(CNT(C_GATE), 7);
    if (!(__builtin_amdgcn_s_getreg(20) & 1)) __builtin_amdgcn_s_sleep(28);
    __builtin_amdgcn_sched_barrier(0);
#pragma unroll
    for (int n = 0; n < WQ; ++n) {
      xf[n] = __builtin_amdgcn_raw_buffer_load_b128(rq, off_frag + (n % 3) * 128, (n / 3) * 3072, AUX_NT);
      __builtin_amdgcn_sched_barrier(0);
    }
    const bool odd = c & 1;
    const u32 off_row = (row0 + 2 * g) * 384;
    u32x2 phq[2], kv2[4];
    f32x4 zf, ph[2];
    auto part2 = [&](int u) {
      const float dp = fmaf(ph[u][0], zf[0], ph[u][1] * zf[1]) + fmaf(ph[u][2], zf[2], ph[u][3] * zf[3]);
      const f32x4 den = __builtin_amdgcn_mfma_f32_16x16x4f32(1.0f, dp, (f32x4){1e-6f, 1e-6f, 1e-6f, 1e-6f}, 0, 0, 0);
      const float inv = __builtin_amdgcn_rcpf(den[0]);
      phq[u] = (u32x2){pk_bf16(ph[u][0] * inv, ph[u][1] * inv), pk_bf16(ph[u][2] * inv, ph[u][3] * inv)};
      f32x4 num[4];
#pragma unroll
      for (int t = 0; t < 4; ++t) num[t] = mfma16(phq[u], kv2[t], (f32x4){0, 0, 0, 0});
#pragma unroll
      for (int r = 0; r < 4; ++r)
        __builtin_amdgcn_raw_buffer_store_b64((u32x2){pk_bf16(num[0][r], num[1][r]), pk_bf16(num[2][r], num[3][r])}, ro,
                                              off_row + 8 * c + 256 + (r >> 1) * 384, (16 * u + 8 * (r & 1)) * 384,
                                              AUX_PLAIN);
    };
#pragma unroll
    for (int u = 0; u < 2; ++u) {
      float ss[2];
      f32x4 d[2][2];
#pragma unroll
      for (int a = 0; a < 2; ++a) {
        ss[a] = 0.f;
        d[a][0] = (f32x4){0, 0, 0, 0};
        d[a][1] = (f32x4){0, 0, 0, 0};
#pragma unroll
        for (int i = 0; i < 3; ++i) {
          const int n = 6 * u + 3 * a + i;
          if (n + WQ < 12) {
            asm volatile("" : "+v"(xf[n]));
            __builtin_amdgcn_sched_barrier(0);
            xf[n + WQ] = __builtin_amdgcn_raw_buffer_load_b128(rq, off_frag + ((n + WQ) % 3) * 128,
                                                               ((n + WQ) / 3) * 3072, AUX_NT);
            __builtin_amdgcn_sched_barrier(0);
          }
          if (n == QMID) lds_add(CNT(C_QMID));
          if (n == QLATE) lds_add(CNT(C_QLATE));
          if (n == QEARLY) {
            lds_wait<0>(CNT(C_KV2), 1);
            zf = sm.z[g];
#pragma unroll
            for (int t = 0; t < 4; ++t) kv2[t] = sm.kv[4 * (128 + 4 * c + t) + g + 32 + c];
            part2(0);
          }
#pragma unroll
          for (int e = 0; e < 4; ++e) ss[a] = sumsq2(xf[n][e], ss[a]);
          d[a][0] = mfma32(om[i][0], xf[n], d[a][0]);
          d[a][1] = mfma32(om[i][1], xf[n], d[a][1]);
        }
      }
      const float st = (odd ? ss[1] : ss[0]) + swap1(odd ? ss[0] : ss[1]);
      const f32x4 sq = __builtin_amdgcn_mfma_f32_16x16x4f32(1.0f, st, (f32x4){0, 0, 0, 0}, 0, 0, 0);
      const float sl = scale * 1.44269504f, hq = sq[0] * (-0.5f * scale * sl);
#pragma unroll
      for (int r = 0; r < 4; ++r) {
        const float pr = (odd ? d[1][1][r] : d[0][0][r]) + swap1(odd ? d[0][1][r] : d[1][0][r]);
        ph[u][r] = fmaf(__builtin_amdgcn_exp2f(fmaf(pr, sl, hq)), 0.25f, 1e-6f);
      }
    }
    part2(1);
    lds_wait<0>(CNT(C_KV01), 2);
    u32x2 kvf[8];
#pragma unroll
    for (int t = 0; t < 8; ++t) kvf[t] = sm.kv[4 * (8 * c + t) + g + 2 * c + (t >> 2)];
#pragma unroll
    for (int u = 0; u < 2; ++u) {
      f32x4 num[8];
#pragma unroll
      for (int t = 0; t < 8; ++t) num[t] = mfma16(phq[u], kvf[t], (f32x4){0, 0, 0, 0});
#pragma unroll
      for (int r = 0; r < 4; ++r)
        __builtin_amdgcn_raw_buffer_store_b128((u32x4){pk_bf16(num[0][r], num[1][r]), pk_bf16(num[2][r], num[3][r]),
                                                       pk_bf16(num[4][r], num[5][r]), pk_bf16(num[6][r], num[7][r])},
                                               ro, off_row + 16 * c + (r >> 1) * 384, (16 * u + 8 * (r & 1)) * 384, AUX_NT);
    }
  }
}

extern "C" void kernel_launch(void* const* d_in, const int* in_sizes, int n_in, void* d_out, int out_size,
                              void* d_ws, size_t ws_size, hipStream_t stream) {
  const float scale = 0.26864248f;
  rfa_fused<<<H * NCH, 704, 0, stream>>>((const u32*)d_in[0], (const u32*)d_in[1], (const u32*)d_in[2],
                                         (const u32*)d_in[3], (u32*)d_out, (u32*)d_ws, scale);
}
```

```cpp
#include <hip/hip_runtime.h>
#include <stdint.h>

typedef unsigned u32;
typedef unsigned long long u64;
typedef u32 u32x4 __attribute__((ext_vector_type(4)));
typedef u32 u32x2 __attribute__((ext_vector_type(2)));
typedef float f32x4 __attribute__((ext_vector_type(4)));
typedef float f32x2 __attribute__((ext_vector_type(2)));
typedef __bf16 bf16x8 __attribute__((ext_vector_type(8)));
typedef __bf16 bf16x2 __attribute__((ext_vector_type(2)));
typedef short s16x4 __attribute__((ext_vector_type(4)));
#define LAS __attribute__((address_space(3)))
#define GAS __attribute__((address_space(1)))

constexpr int H = 32, S = 2048, D = 192, M = 16;
constexpr int CH = 256;
constexpr int NCH = S / CH;
constexpr u32 TBYTES = H * S * D * 2;
constexpr u32 CG_BYTES = 2304;
constexpr u32 PART_BYTES = 3 * CG_BYTES + 256;
constexpr u32 EPOCH_OFF = NCH * PART_BYTES;
constexpr u32 HEAD_BYTES = EPOCH_OFF + 256;
constexpr int AUX_NT = 2, AUX_DEV = 16, AUX_PLAIN = 0;
constexpr int WQ = 4;
constexpr int QMID = 2, QLATE = 11 - WQ, QEARLY = 9;
constexpr u32 CODELEN = 11200;

__device__ __forceinline__ float lo_f(u32 x) { return __uint_as_float(x << 16); }
__device__ __forceinline__ float hi_f(u32 x) { return __uint_as_float(x & 0xffff0000u); }
__device__ __forceinline__ u32 pk_bf16(float a, float b) {
  f32x2 v = {a, b};
  bf16x2 r = __builtin_convertvector(v, bf16x2);
  return __builtin_bit_cast(u32, r);
}
__device__ __forceinline__ f32x4 mfma32(u32x4 a, u32x4 b, f32x4 c) {
  return __builtin_amdgcn_mfma_f32_16x16x32_bf16(__builtin_bit_cast(bf16x8, a), __builtin_bit_cast(bf16x8, b), c, 0, 0, 0);
}
__device__ __forceinline__ f32x4 mfma16(u32x2 a, u32x2 b, f32x4 c) {
  return __builtin_amdgcn_mfma_f32_16x16x16bf16_1k(__builtin_bit_cast(s16x4, a), __builtin_bit_cast(s16x4, b), c, 0, 0, 0);
}
__device__ __forceinline__ float sumsq2(u32 p, float acc) {
  return __builtin_amdgcn_fdot2_f32_bf16(__builtin_bit_cast(bf16x2, p), __builtin_bit_cast(bf16x2, p), acc, false);
}
__device__ __forceinline__ u32 phi2_bf16(float p0, float s0, float p1, float s1) {
  const u32 pp = pk_bf16(p0, p1), sp = pk_bf16(s0, s1);
  const u32 tp = pk_bf16(fmaf(lo_f(sp), -0.5f, lo_f(pp)), fmaf(hi_f(sp), -0.5f, hi_f(pp)));
  const u32 ep = pk_bf16(__expf(lo_f(tp)), __expf(hi_f(tp)));
  return pk_bf16(fmaf(lo_f(ep), 0.25f, 1e-6f), fmaf(hi_f(ep), 0.25f, 1e-6f));
}
__device__ __forceinline__ float swap1(float x) {
  return __builtin_bit_cast(float, __builtin_amdgcn_update_dpp(0, __builtin_bit_cast(int, x), 0xB1, 0xf, 0xf, true));
}
__device__ __forceinline__ void lds_add(LAS u32* p) {
  u64 sv;
  asm volatile("s_mov_b64 %0, exec\n\ts_mov_b64 exec, 1\n\tds_add_u32 %1, %2\n\ts_mov_b64 exec, %0"
               : "=&s"(sv) : "v"((u32)(size_t)p), "v"(1u) : "memory");
}
__device__ __forceinline__ void lds_signal(LAS u32* p) {
  asm volatile("s_waitcnt lgkmcnt(0)" ::: "memory");
  lds_add(p);
}
template <int SLEEP>
__device__ __forceinline__ void lds_wait(LAS u32* p, u32 want) {
  while (__builtin_amdgcn_readfirstlane(__hip_atomic_load(p, __ATOMIC_RELAXED, __HIP_MEMORY_SCOPE_WORKGROUP)) < want)
    if (SLEEP) __builtin_amdgcn_s_sleep(SLEEP);
  asm volatile("" ::: "memory");
}
__device__ __forceinline__ u32 xor3(u32 a, u32 b, u32 c) { return __builtin_amdgcn_bitop3_b32(a, b, c, 0x96); }
__device__ __forceinline__ u32 xor9(u32x4 a, u32x4 b, u32 c) {
  return xor3(xor3(a[0], a[1], a[2]), xor3(a[3], b[0], b[1]), xor3(b[2], b[3], c));
}

struct __attribute__((aligned(16))) Smem {
  u32x4 om[6][64];
  u32x4 phik[8][64];
  u32x2 kv[48 * 17];
  f32x4 z[4];
  u32 tag;
  u32 pad[3];
  u32 cnt[8];
};
enum { C_GATE, C_OMEGA, C_PHIK, C_QLATE, C_QMID, C_KV01, C_KV2 };
#define CNT(i) ((LAS u32*)&sm.cnt[i])

__global__ __launch_bounds__(704, 3) void rfa_fused(const u32* __restrict__ Q, const u32* __restrict__ K,
                                                    const u32* __restrict__ V, const u32* __restrict__ Om,
                                                    u32* __restrict__ O, u32* ws, float scale) {
  __shared__ Smem sm;
  const int b = blockIdx.x;
  const int h = (b & 7) * 4 + (b >> 6), j = (b >> 3) & 7;
  const int w = __builtin_amdgcn_readfirstlane(threadIdx.x >> 6);
  const int l = threadIdx.x & 63, g = l >> 4, c = l & 15;
  const u32 rowb = h * S + j * CH;
  if (w < 3) {
    const int cg = w;
    auto rw = __builtin_amdgcn_make_buffer_rsrc((char*)ws + h * HEAD_BYTES, 0, HEAD_BYTES, 0x00020000);
    if (cg == 0) {
      __builtin_amdgcn_s_barrier();
      asm volatile("" ::: "memory");
    } else if (cg == 1) {
      const char* base = g == 0 ? (const char*)Q : g == 1 ? (const char*)O : (const char*)V;
      const char* p = base + rowb * 384 + (c < 2 ? c * 98300 : 49152);
      const u32 pf = __builtin_nontemporal_load((const u32*)p);
      if (l < 8) sm.cnt[l] = 0;
      asm volatile("s_waitcnt lgkmcnt(0)" ::: "memory");
      __builtin_amdgcn_s_barrier();
      asm volatile("" ::"v"(pf) : "memory");
    } else {
      u32 vz;
      asm volatile("v_mov_b32 %0, 0" : "=v"(vz));
      const u32 epoch = __builtin_amdgcn_raw_buffer_load_b32(rw, vz, EPOCH_OFF, AUX_DEV);
      __builtin_amdgcn_sched_barrier(0);
      __builtin_amdgcn_s_barrier();
      asm volatile("" ::: "memory");
      sm.tag = epoch + 1u;
      lds_signal(CNT(C_PHIK));
    }
    auto rv = __builtin_amdgcn_make_buffer_rsrc((void*)V, 0, TBYTES, 0x00020000);
    const u32 voff = (rowb + 2 * g) * 384 + cg * 128 + 8 * c;
    lds_wait<1>(CNT(C_GATE), 4);
    __builtin_amdgcn_sched_barrier(0);
    u32x2 vf[8][4][2];
#pragma unroll
    for (int s = 0; s < 8; ++s)
#pragma unroll
      for (int a = 0; a < 4; ++a)
#pragma unroll
        for (int bb = 0; bb < 2; ++bb)
          vf[s][a][bb] = __builtin_amdgcn_raw_buffer_load_b64(rv, voff + bb * 384, (s * 4 + a) * 3072, AUX_NT);
    __builtin_amdgcn_sched_barrier(0);
    lds_add(CNT(C_GATE));
    lds_wait<1>(CNT(C_PHIK), 9);
    const u32 tag = sm.tag;
    f32x4 acc[4], zacc = {0, 0, 0, 0};
#pragma unroll
    for (int t = 0; t < 4; ++t) acc[t] = (f32x4){0, 0, 0, 0};
    const u32x4 ones = {0x3f803f80u, 0x3f803f80u, 0x3f803f80u, 0x3f803f80u};
#pragma unroll
    for (int s = 0; s < 8; ++s) {
      const u32x4 phiT = sm.phik[s][l];
      zacc = mfma32(ones, phiT, zacc);
#pragma unroll
      for (int t = 0; t < 4; ++t) {
        u32x4 bf;
        const u32 sel = (t & 1) ? 0x07060302u : 0x05040100u;
#pragma unroll
        for (int a = 0; a < 4; ++a) bf[a] = __builtin_amdgcn_perm(vf[s][a][1][t >> 1], vf[s][a][0][t >> 1], sel);
        acc[t] = mfma32(phiT, bf, acc[t]);
      }
    }
    const u32x4 p0 = {pk_bf16(acc[0][0], acc[0][1]), pk_bf16(acc[0][2], acc[0][3]), pk_bf16(acc[1][0], acc[1][1]),
                      pk_bf16(acc[1][2], acc[1][3])};
    const u32x4 p1 = {pk_bf16(acc[2][0], acc[2][1]), pk_bf16(acc[2][2], acc[2][3]), pk_bf16(acc[3][0], acc[3][1]),
                      pk_bf16(acc[3][2], acc[3][3])};
    {
      const u32 chk = xor9(p0, p1, tag);
      const u32 so = j * PART_BYTES + cg * CG_BYTES;
      __builtin_amdgcn_raw_buffer_store_b128(p0, rw, l * 16, so, AUX_DEV);
      __builtin_amdgcn_raw_buffer_store_b128(p1, rw, l * 16 + 1024, so, AUX_DEV);
      u32x4 cw;
#pragma unroll
      for (int e = 0; e < 4; ++e) cw[e] = __builtin_amdgcn_ds_bpermute(((4 * l + e) & 63) * 4, chk);
      if (g == 1) cw = (u32x4){tag, __float_as_uint(zacc[0]), tag, __float_as_uint(zacc[0])};
      if (l < (cg == 2 ? 32 : 16)) __builtin_amdgcn_raw_buffer_store_b128(cw, rw, l * 16 + 2048, so, AUX_DEV);
    }
    u32x4 sel[2];
    {
      const u32 one = (c & 1) ? 0x3f800000u : 0x00003f80u;
#pragma unroll
      for (int hf = 0; hf < 2; ++hf)
#pragma unroll
        for (int e = 0; e < 4; ++e) sel[hf][e] = (g == 2 * hf + (c >> 3) && ((c & 7) >> 1) == e) ? one : 0u;
    }
    f32x4 own[2][2];
#pragma unroll
    for (int hf = 0; hf < 2; ++hf) {
      own[0][hf] = mfma32(sel[hf], p0, (f32x4){0, 0, 0, 0});
      own[1][hf] = mfma32(sel[hf], p1, (f32x4){0, 0, 0, 0});
    }
    lds_wait<1>(cg == 2 ? CNT(C_QMID) : CNT(C_QLATE), 8);
    f32x4 d[2][2];
    u32x2 za, zb;
    for (int spin = 0; spin < (1 << 18); ++spin) {
      u32x4 x[7][2];
      u32 xc[7];
#pragma unroll
      for (int i = 0; i < 7; ++i) {
        const u32 pb = ((j + 1 + i) & 7) * PART_BYTES + cg * CG_BYTES;
        x[i][0] = __builtin_amdgcn_raw_buffer_load_b128(rw, l * 16, pb, AUX_DEV);
        x[i][1] = __builtin_amdgcn_raw_buffer_load_b128(rw, l * 16 + 1024, pb, AUX_DEV);
        xc[i] = __builtin_amdgcn_raw_buffer_load_b32(rw, l * 4 + 2048, pb, AUX_DEV);
      }
      const u32 zo = cg == 2 ? g * PART_BYTES + c * 16 : 0x80000000u;
      za = __builtin_amdgcn_raw_buffer_load_b64(rw, zo, 3 * CG_BYTES, AUX_DEV);
      zb = __builtin_amdgcn_raw_buffer_load_b64(rw, zo, 3 * CG_BYTES + 4 * PART_BYTES, AUX_DEV);
      __builtin_amdgcn_sched_barrier(0);
      u32 bad = 0;
#pragma unroll
      for (int s2 = 0; s2 < 2; ++s2)
#pragma unroll
        for (int hf = 0; hf < 2; ++hf) d[s2][hf] = own[s2][hf];
#pragma unroll
      for (int i = 0; i < 7; ++i) {
#pragma unroll
        for (int s2 = 0; s2 < 2; ++s2)
#pragma unroll
          for (int hf = 0; hf < 2; ++hf) d[s2][hf] = mfma32(sel[hf], x[i][s2], d[s2][hf]);
        bad |= xor9(x[i][0], x[i][1], xc[i]) ^ tag;
      }
      if (cg == 2) bad |= (za[0] ^ tag) | (zb[0] ^ tag);
      if (__all(bad == 0)) break;
      __builtin_amdgcn_s_sleep(8);
      asm volatile("" ::: "memory");
    }
#pragma unroll
    for (int s2 = 0; s2 < 2; ++s2)
#pragma unroll
      for (int hf = 0; hf < 2; ++hf)
        sm.kv[4 * (64 * cg + 4 * c + 2 * s2 + (g & 1)) + 2 * hf + (g >> 1) + 16 * cg + c] =
            (u32x2){pk_bf16(d[s2][hf][0], d[s2][hf][1]), pk_bf16(d[s2][hf][2], d[s2][hf][3])};
    if (cg == 2) {
      const f32x4 zs = __builtin_amdgcn_mfma_f32_16x16x4f32(1.0f, __uint_as_float(za[1]) + __uint_as_float(zb[1]),
                                                            (f32x4){0, 0, 0, 0}, 0, 0, 0);
      if (l < 16) ((float*)sm.z)[l] = lo_f(pk_bf16(zs[0], 0.f));
    }
    lds_signal(cg == 2 ? CNT(C_KV2) : CNT(C_KV01));
    if (cg == 2 && j == 0) {
      lds_wait<1>(CNT(C_KV01), 2);
      if (l == 0) __builtin_amdgcn_raw_buffer_store_b32(tag, rw, 0, EPOCH_OFF, AUX_DEV);
    }
  } else {
    const int wa = w - 3;
    const u32 row0 = rowb + wa * 32;
    const u32 off_frag = (row0 + (c >> 1)) * 384 + (2 * g + (c & 1)) * 16;
    auto rk = __builtin_amdgcn_make_buffer_rsrc((void*)K, 0, TBYTES, 0x00020000);
    auto rq = __builtin_amdgcn_make_buffer_rsrc((void*)Q, 0, TBYTES, 0x00020000);
    auto ro = __builtin_amdgcn_make_buffer_rsrc((void*)O, 0, TBYTES, 0x00020000);
    u32x4 om[3][2], xf[12];
    u32 cpf = 0;
    if (wa == 4) {
      u64 pc;
      asm volatile("s_getpc_b64 %0" : "=s"(pc));
      const u32 off = (((b >> 3) & 3) * 64 + l) * 64;
      cpf = *(const GAS u32*)(pc + (off < CODELEN ? off : 0));
    }
    if (wa >= 4) __builtin_amdgcn_s_barrier();
    __builtin_amdgcn_sched_barrier(0);
#pragma unroll
    for (int n = 0; n < 12; ++n)
      xf[n] = __builtin_amdgcn_raw_buffer_load_b128(rk, off_frag + (n % 3) * 128, (n / 3) * 3072, AUX_NT);
    __builtin_amdgcn_sched_barrier(0);
    {
      auto rom = __builtin_amdgcn_make_buffer_rsrc((void*)Om, 0, wa == 0 ? M * D * 2 : 0, 0x00020000);
#pragma unroll
      for (int i = 0; i < 6; ++i) om[i >> 1][i & 1] = __builtin_amdgcn_raw_buffer_load_b128(rom, l * 16, i * 1024, 0);
    }
    __builtin_amdgcn_sched_barrier(0);
    if (wa < 4) __builtin_amdgcn_s_barrier();
    else lds_add(CNT(C_GATE));
    asm volatile("" ::"v"(cpf) : "memory");
    if (wa == 0) {
#pragma unroll
      for (int i = 0; i < 6; ++i) {
        const u32 n = 64 * i + l, m = n / 24, cc = n - 24 * m;
        sm.om[(cc >> 3) * 2 + (cc & 1)][16 * ((cc & 7) >> 1) + m] = om[i >> 1][i & 1];
      }
      lds_signal(CNT(C_OMEGA));
    }
    lds_wait<1>(CNT(C_OMEGA), 1);
#pragma unroll
    for (int i = 0; i < 3; ++i)
#pragma unroll
      for (int p = 0; p < 2; ++p) om[i][p] = sm.om[2 * i + p][l];
    {
      u32x4 phiT;
#pragma unroll
      for (int a = 0; a < 4; ++a) {
        float ss = 0.f;
        f32x4 d0 = {0, 0, 0, 0}, d1 = {0, 0, 0, 0};
#pragma unroll
        for (int i = 0; i < 3; ++i) {
          u32x4 ks;
#pragma unroll
          for (int e = 0; e < 4; ++e) {
            ks[e] = pk_bf16(lo_f(xf[3 * a + i][e]) * scale, hi_f(xf[3 * a + i][e]) * scale);
            ss = sumsq2(ks[e], ss);
          }
          d0 = mfma32(ks, om[i][0], d0);
          d1 = mfma32(ks, om[i][1], d1);
          __builtin_amdgcn_sched_barrier(0);
          __builtin_amdgcn_s_sleep(3);
          __builtin_amdgcn_sched_barrier(0);
        }
        const f32x4 sq = __builtin_amdgcn_mfma_f32_16x16x4f32(ss, 1.0f, (f32x4){0, 0, 0, 0}, 0, 0, 0);
        phiT[a] = phi2_bf16(d0[0] + d1[1], sq[0] + sq[1], d0[2] + d1[3], sq[2] + sq[3]);
      }
      sm.phik[wa][l] = phiT;
    }
    lds_signal(CNT(C_PHIK));
    lds_wait<1>(CNT(C_GATE), 7);
    if (!(__builtin_amdgcn_s_getreg(20) & 1)) __builtin_amdgcn_s_sleep(28);
    __builtin_amdgcn_sched_barrier(0);
#pragma unroll
    for (int n = 0; n < WQ; ++n) {
      xf[n] = __builtin_amdgcn_raw_buffer_load_b128(rq, off_frag + (n % 3) * 128, (n / 3) * 3072, AUX_NT);
      __builtin_amdgcn_sched_barrier(0);
    }
    const bool odd = c & 1;
    const u32 off_row = (row0 + 2 * g) * 384;
    u32x2 phq[2], kv2[4];
    f32x4 zf, ph[2];
    auto part2 = [&](int u) {
      const float dp = fmaf(ph[u][0], zf[0], ph[u][1] * zf[1]) + fmaf(ph[u][2], zf[2], ph[u][3] * zf[3]);
      const f32x4 den = __builtin_amdgcn_mfma_f32_16x16x4f32(1.0f, dp, (f32x4){1e-6f, 1e-6f, 1e-6f, 1e-6f}, 0, 0, 0);
      const float inv = __builtin_amdgcn_rcpf(den[0]);
      phq[u] = (u32x2){pk_bf16(ph[u][0] * inv, ph[u][1] * inv), pk_bf16(ph[u][2] * inv, ph[u][3] * inv)};
      f32x4 num[4];
#pragma unroll
      for (int t = 0; t < 4; ++t) num[t] = mfma16(phq[u], kv2[t], (f32x4){0, 0, 0, 0});
#pragma unroll
      for (int r = 0; r < 4; ++r)
        __builtin_amdgcn_raw_buffer_store_b64((u32x2){pk_bf16(num[0][r], num[1][r]), pk_bf16(num[2][r], num[3][r])}, ro,
                                              off_row + 8 * c + 256 + (r >> 1) * 384, (16 * u + 8 * (r & 1)) * 384,
                                              AUX_PLAIN);
    };
#pragma unroll
    for (int u = 0; u < 2; ++u) {
      float ss[2];
      f32x4 d[2][2];
#pragma unroll
      for (int a = 0; a < 2; ++a) {
        ss[a] = 0.f;
        d[a][0] = (f32x4){0, 0, 0, 0};
        d[a][1] = (f32x4){0, 0, 0, 0};
#pragma unroll
        for (int i = 0; i < 3; ++i) {
          const int n = 6 * u + 3 * a + i;
          if (n + WQ < 12) {
            asm volatile("" : "+v"(xf[n]));
            __builtin_amdgcn_sched_barrier(0);
            xf[n + WQ] = __builtin_amdgcn_raw_buffer_load_b128(rq, off_frag + ((n + WQ) % 3) * 128,
                                                               ((n + WQ) / 3) * 3072, AUX_NT);
            __builtin_amdgcn_sched_barrier(0);
          }
          if (n == QMID) lds_add(CNT(C_QMID));
          if (n == QLATE) lds_add(CNT(C_QLATE));
          if (n == QEARLY) {
            lds_wait<0>(CNT(C_KV2), 1);
            zf = sm.z[g];
#pragma unroll
            for (int t = 0; t < 4; ++t) kv2[t] = sm.kv[4 * (128 + 4 * c + t) + g + 32 + c];
            part2(0);
          }
#pragma unroll
          for (int e = 0; e < 4; ++e) ss[a] = sumsq2(xf[n][e], ss[a]);
          d[a][0] = mfma32(om[i][0], xf[n], d[a][0]);
          d[a][1] = mfma32(om[i][1], xf[n], d[a][1]);
        }
      }
      const float st = (odd ? ss[1] : ss[0]) + swap1(odd ? ss[0] : ss[1]);
      const f32x4 sq = __builtin_amdgcn_mfma_f32_16x16x4f32(1.0f, st, (f32x4){0, 0, 0, 0}, 0, 0, 0);
      const float sl = scale * 1.44269504f, hq = sq[0] * (-0.5f * scale * sl);
#pragma unroll
      for (int r = 0; r < 4; ++r) {
        const float pr = (odd ? d[1][1][r] : d[0][0][r]) + swap1(odd ? d[0][1][r] : d[1][0][r]);
        ph[u][r] = fmaf(__builtin_amdgcn_exp2f(fmaf(pr, sl, hq)), 0.25f, 1e-6f);
      }
    }
    part2(1);
    lds_wait<0>(CNT(C_KV01), 2);
    u32x2 kvf[8];
#pragma unroll
    for (int t = 0; t < 8; ++t) kvf[t] = sm.kv[4 * (8 * c + t) + g + 2 * c + (t >> 2)];
#pragma unroll
    for (int u = 0; u < 2; ++u) {
      f32x4 num[8];
#pragma unroll
      for (int t = 0; t < 8; ++t) num[t] = mfma16(phq[u], kvf[t], (f32x4){0, 0, 0, 0});
#pragma unroll
      for (int r = 0; r < 4; ++r)
        __builtin_amdgcn_raw_buffer_store_b128((u32x4){pk_bf16(num[0][r], num[1][r]), pk_bf16(num[2][r], num[3][r]),
                                                       pk_bf16(num[4][r], num[5][r]), pk_bf16(num[6][r], num[7][r])},
                                               ro, off_row + 16 * c + (r >> 1) * 384, (16 * u + 8 * (r & 1)) * 384, AUX_NT);
    }
  }
}

extern "C" void kernel_launch(void* const* d_in, const int* in_sizes, int n_in, void* d_out, int out_size,
                              void* d_ws, size_t ws_size, hipStream_t stream) {
  const float scale = 0.26864248f;
  rfa_fused<<<H * NCH, 704, 0, stream>>>((const u32*)d_in[0], (const u32*)d_in[1], (const u32*)d_in[2],
                                         (const u32*)d_in[3], (u32*)d_out, (u32*)d_ws, scale);
}
```

```cpp
#include <hip/hip_runtime.h>
#include <stdint.h>

typedef unsigned u32;
typedef unsigned long long u64;
typedef u32 u32x4 __attribute__((ext_vector_type(4)));
typedef u32 u32x2 __attribute__((ext_vector_type(2)));
typedef float f32x4 __attribute__((ext_vector_type(4)));
typedef float f32x2 __attribute__((ext_vector_type(2)));
typedef __bf16 bf16x8 __attribute__((ext_vector_type(8)));
typedef __bf16 bf16x2 __attribute__((ext_vector_type(2)));
typedef short s16x4 __attribute__((ext_vector_type(4)));
#define LAS __attribute__((address_space(3)))
#define GAS __attribute__((address_space(1)))

constexpr int H = 32, S = 2048, D = 192, M = 16;
constexpr int CH = 256;
constexpr int NCH = S / CH;
constexpr u32 TBYTES = H * S * D * 2;
constexpr u32 CG_BYTES = 2304;
constexpr u32 PART_BYTES = 3 * CG_BYTES + 256;
constexpr u32 EPOCH_OFF = NCH * PART_BYTES;
constexpr u32 HEAD_BYTES = EPOCH_OFF + 256;
constexpr int AUX_NT = 2, AUX_DEV = 16, AUX_PLAIN = 0;
constexpr int WQ = 4;
constexpr int QMID = 2, QLATE = 11 - WQ, QEARLY = 9;
#define CODELEN 10880
#define XMAP 0

__device__ __forceinline__ float lo_f(u32 x) { return __uint_as_float(x << 16); }
__device__ __forceinline__ float hi_f(u32 x) { return __uint_as_float(x & 0xffff0000u); }
__device__ __forceinline__ u32 pk_bf16(float a, float b) {
  f32x2 v = {a, b};
  bf16x2 r = __builtin_convertvector(v, bf16x2);
  return __builtin_bit_cast(u32, r);
}
__device__ __forceinline__ f32x4 mfma32(u32x4 a, u32x4 b, f32x4 c) {
  return __builtin_amdgcn_mfma_f32_16x16x32_bf16(__builtin_bit_cast(bf16x8, a), __builtin_bit_cast(bf16x8, b), c, 0, 0, 0);
}
__device__ __forceinline__ f32x4 mfma16(u32x2 a, u32x2 b, f32x4 c) {
  return __builtin_amdgcn_mfma_f32_16x16x16bf16_1k(__builtin_bit_cast(s16x4, a), __builtin_bit_cast(s16x4, b), c, 0, 0, 0);
}
__device__ __forceinline__ float sumsq2(u32 p, float acc) {
  return __builtin_amdgcn_fdot2_f32_bf16(__builtin_bit_cast(bf16x2, p), __builtin_bit_cast(bf16x2, p), acc, false);
}
__device__ __forceinline__ u32 phi2_bf16(float p0, float s0, float p1, float s1) {
  const u32 pp = pk_bf16(p0, p1), sp = pk_bf16(s0, s1);
  const u32 tp = pk_bf16(fmaf(lo_f(sp), -0.5f, lo_f(pp)), fmaf(hi_f(sp), -0.5f, hi_f(pp)));
  const u32 ep = pk_bf16(__expf(lo_f(tp)), __expf(hi_f(tp)));
  return pk_bf16(fmaf(lo_f(ep), 0.25f, 1e-6f), fmaf(hi_f(ep), 0.25f, 1e-6f));
}
__device__ __forceinline__ float swap1(float x) {
  return __builtin_bit_cast(float, __builtin_amdgcn_update_dpp(0, __builtin_bit_cast(int, x), 0xB1, 0xf, 0xf, true));
}
__device__ __forceinline__ void lds_add(LAS u32* p) {
  u64 sv;
  asm volatile("s_mov_b64 %0, exec\n\ts_mov_b64 exec, 1\n\tds_add_u32 %1, %2\n\ts_mov_b64 exec, %0"
               : "=&s"(sv) : "v"((u32)(size_t)p), "v"(1u) : "memory");
}
__device__ __forceinline__ void lds_signal(LAS u32* p) {
  asm volatile("s_waitcnt lgkmcnt(0)" ::: "memory");
  lds_add(p);
}
template <int SLEEP>
__device__ __forceinline__ void lds_wait(LAS u32* p, u32 want) {
  while (__builtin_amdgcn_readfirstlane(__hip_atomic_load(p, __ATOMIC_RELAXED, __HIP_MEMORY_SCOPE_WORKGROUP)) < want)
    if (SLEEP) __builtin_amdgcn_s_sleep(SLEEP);
  asm volatile("" ::: "memory");
}
__device__ __forceinline__ u32 xor3(u32 a, u32 b, u32 c) { return __builtin_amdgcn_bitop3_b32(a, b, c, 0x96); }
__device__ __forceinline__ u32 xor9(u32x4 a, u32x4 b, u32 c) {
  return xor3(xor3(a[0], a[1], a[2]), xor3(a[3], b[0], b[1]), xor3(b[2], b[3], c));
}

struct __attribute__((aligned(16))) Smem {
  u32x4 om[6][64];
  u32x4 phik[8][64];
  u32x2 kv[48 * 17];
  f32x4 z[4];
  u32 tag;
  u32 pad[3];
  u32 cnt[8];
};
enum { C_GATE, C_OMEGA, C_PHIK, C_QLATE, C_QMID, C_KV01, C_KV2 };
#define CNT(i) ((LAS u32*)&sm.cnt[i])

__global__ __launch_bounds__(704, 3) void rfa_fused(const u32* __restrict__ Q, const u32* __restrict__ K,
                                                    const u32* __restrict__ V, const u32* __restrict__ Om,
                                                    u32* __restrict__ O, u32* ws, float scale) {
  __shared__ Smem sm;
  const u64 t0 = __builtin_amdgcn_s_memrealtime();
  const int b = blockIdx.x;
  const int h = (b & 7) * 4 + (b >> 6), j = (b >> 3) & 7;
  const int w = __builtin_amdgcn_readfirstlane(threadIdx.x >> 6);
  const int l = threadIdx.x & 63, g = l >> 4, c = l & 15;
  const u32 rowb = h * S + j * CH;
  if (w < 3) {
    const int cg = w;
    auto rw = __builtin_amdgcn_make_buffer_rsrc((char*)ws + h * HEAD_BYTES, 0, HEAD_BYTES, 0x00020000);
    if (cg == 0) {
      auto rom = __builtin_amdgcn_make_buffer_rsrc((void*)Om, 0, M * D * 2, 0x00020000);
      u32x4 om[6];
#pragma unroll
      for (int i = 0; i < 6; ++i)
        om[i] = __builtin_amdgcn_raw_buffer_load_b128(rom, c * 384 + 2 * g * 16, (i >> 1) * 128 + (i & 1) * 16, 0);
      __builtin_amdgcn_sched_barrier(0);
      __builtin_amdgcn_s_barrier();
      asm volatile("" ::: "memory");
#pragma unroll
      for (int i = 0; i < 6; ++i) sm.om[i][l] = om[i];
      lds_signal(CNT(C_OMEGA));
    } else if (cg == 1) {
      const char* base = g == 0 ? (const char*)Q : g == 1 ? (const char*)O : (const char*)V;
      const char* p = base + rowb * 384 + (c < 2 ? c * 98300 : 49152);
      if (g == 3) p = (const char*)ws + h * HEAD_BYTES;
      const u32 pf = __builtin_nontemporal_load((const u32*)p);
      if (l < 8) sm.cnt[l] = 0;
      asm volatile("s_waitcnt lgkmcnt(0)" ::: "memory");
      __builtin_amdgcn_s_barrier();
      asm volatile("" ::"v"(pf));
    } else {
      u32 vz;
      asm volatile("v_mov_b32 %0, 0" : "=v"(vz));
      const u32 epoch = __builtin_amdgcn_raw_buffer_load_b32(rw, vz, EPOCH_OFF, AUX_DEV);
      __builtin_amdgcn_sched_barrier(0);
      __builtin_amdgcn_s_barrier();
      asm volatile("" ::: "memory");
      sm.tag = epoch + 1u;
      lds_signal(CNT(C_PHIK));
    }
    auto rv = __builtin_amdgcn_make_buffer_rsrc((void*)V, 0, TBYTES, 0x00020000);
    const u32 voff = (rowb + 2 * g) * 384 + cg * 128 + 8 * c;
    lds_wait<1>(CNT(C_GATE), 4);
    __builtin_amdgcn_sched_barrier(0);
    u32x2 vf[8][4][2];
#pragma unroll
    for (int s = 0; s < 8; ++s)
#pragma unroll
      for (int a = 0; a < 4; ++a)
#pragma unroll
        for (int bb = 0; bb < 2; ++bb)
          vf[s][a][bb] = __builtin_amdgcn_raw_buffer_load_b64(rv, voff + bb * 384, (s * 4 + a) * 3072, AUX_NT);
    __builtin_amdgcn_sched_barrier(0);
    lds_add(CNT(C_GATE));
    lds_wait<1>(CNT(C_PHIK), 9);
    const u32 tag = sm.tag;
    f32x4 acc[4], zacc = {0, 0, 0, 0};
#pragma unroll
    for (int t = 0; t < 4; ++t) acc[t] = (f32x4){0, 0, 0, 0};
    const u32x4 ones = {0x3f803f80u, 0x3f803f80u, 0x3f803f80u, 0x3f803f80u};
#pragma unroll
    for (int s = 0; s < 8; ++s) {
      const u32x4 phiT = sm.phik[s][l];
      zacc = mfma32(ones, phiT, zacc);
#pragma unroll
      for (int t = 0; t < 4; ++t) {
        u32x4 bf;
        const u32 sel = (t & 1) ? 0x07060302u : 0x05040100u;
#pragma unroll
        for (int a = 0; a < 4; ++a) bf[a] = __builtin_amdgcn_perm(vf[s][a][1][t >> 1], vf[s][a][0][t >> 1], sel);
        acc[t] = mfma32(phiT, bf, acc[t]);
      }
    }
    const u32x4 p0 = {pk_bf16(acc[0][0], acc[0][1]), pk_bf16(acc[0][2], acc[0][3]), pk_bf16(acc[1][0], acc[1][1]),
                      pk_bf16(acc[1][2], acc[1][3])};
    const u32x4 p1 = {pk_bf16(acc[2][0], acc[2][1]), pk_bf16(acc[2][2], acc[2][3]), pk_bf16(acc[3][0], acc[3][1]),
                      pk_bf16(acc[3][2], acc[3][3])};
    {
      const u32 chk = xor9(p0, p1, tag);
      const u32 so = j * PART_BYTES + cg * CG_BYTES;
      __builtin_amdgcn_raw_buffer_store_b128(p0, rw, l * 16, so, AUX_DEV);
      __builtin_amdgcn_raw_buffer_store_b128(p1, rw, l * 16 + 1024, so, AUX_DEV);
      u32x4 cw;
#pragma unroll
      for (int e = 0; e < 4; ++e) cw[e] = __builtin_amdgcn_ds_bpermute(((4 * l + e) & 63) * 4, chk);
      if (g == 1) cw = (u32x4){tag, __float_as_uint(zacc[0]), tag, __float_as_uint(zacc[0])};
      if (l < (cg == 2 ? 32 : 16)) __builtin_amdgcn_raw_buffer_store_b128(cw, rw, l * 16 + 2048, so, AUX_DEV);
    }
    u32x4 sel[2];
    {
      const u32 one = (c & 1) ? 0x3f800000u : 0x00003f80u;
#pragma unroll
      for (int hf = 0; hf < 2; ++hf)
#pragma unroll
        for (int e = 0; e < 4; ++e) sel[hf][e] = (g == 2 * hf + (c >> 3) && ((c & 7) >> 1) == e) ? one : 0u;
    }
    f32x4 own[2][2];
#pragma unroll
    for (int hf = 0; hf < 2; ++hf) {
      own[0][hf] = mfma32(sel[hf], p0, (f32x4){0, 0, 0, 0});
      own[1][hf] = mfma32(sel[hf], p1, (f32x4){0, 0, 0, 0});
    }
    lds_wait<1>(cg == 2 ? CNT(C_QMID) : CNT(C_QLATE), 8);
    f32x4 d[2][2];
    u32x2 za, zb;
    for (int spin = 0; spin < (1 << 18); ++spin) {
      u32x4 x[7][2];
      u32 xc[7];
#pragma unroll
      for (int i = 0; i < 7; ++i) {
        const u32 pb = ((j + 1 + i) & 7) * PART_BYTES + cg * CG_BYTES;
        x[i][0] = __builtin_amdgcn_raw_buffer_load_b128(rw, l * 16, pb, AUX_DEV);
        x[i][1] = __builtin_amdgcn_raw_buffer_load_b128(rw, l * 16 + 1024, pb, AUX_DEV);
        xc[i] = __builtin_amdgcn_raw_buffer_load_b32(rw, l * 4 + 2048, pb, AUX_DEV);
      }
      const u32 zo = cg == 2 ? g * PART_BYTES + c * 16 : 0x80000000u;
      za = __builtin_amdgcn_raw_buffer_load_b64(rw, zo, 3 * CG_BYTES, AUX_DEV);
      zb = __builtin_amdgcn_raw_buffer_load_b64(rw, zo, 3 * CG_BYTES + 4 * PART_BYTES, AUX_DEV);
      __builtin_amdgcn_sched_barrier(0);
      u32 bad = 0;
#pragma unroll
      for (int s2 = 0; s2 < 2; ++s2)
#pragma unroll
        for (int hf = 0; hf < 2; ++hf) d[s2][hf] = own[s2][hf];
#pragma unroll
      for (int i = 0; i < 7; ++i) {
#pragma unroll
        for (int s2 = 0; s2 < 2; ++s2)
#pragma unroll
          for (int hf = 0; hf < 2; ++hf) d[s2][hf] = mfma32(sel[hf], x[i][s2], d[s2][hf]);
        bad |= xor9(x[i][0], x[i][1], xc[i]) ^ tag;
      }
      if (cg == 2) bad |= (za[0] ^ tag) | (zb[0] ^ tag);
      if (__all(bad == 0)) break;
      __builtin_amdgcn_s_sleep(8);
      asm volatile("" ::: "memory");
    }
#pragma unroll
    for (int s2 = 0; s2 < 2; ++s2)
#pragma unroll
      for (int hf = 0; hf < 2; ++hf)
        sm.kv[4 * (64 * cg + 4 * c + 2 * s2 + (g & 1)) + 2 * hf + (g >> 1) + 16 * cg + c] =
            (u32x2){pk_bf16(d[s2][hf][0], d[s2][hf][1]), pk_bf16(d[s2][hf][2], d[s2][hf][3])};
    if (cg == 2) {
      const f32x4 zs = __builtin_amdgcn_mfma_f32_16x16x4f32(1.0f, __uint_as_float(za[1]) + __uint_as_float(zb[1]),
                                                            (f32x4){0, 0, 0, 0}, 0, 0, 0);
      if (l < 16) ((float*)sm.z)[l] = lo_f(pk_bf16(zs[0], 0.f));
    }
    lds_signal(cg == 2 ? CNT(C_KV2) : CNT(C_KV01));
    if (threadIdx.x == 0) ((u64*)((char*)ws + (64 << 20)))[b] = t0;
    if (cg == 2 && j == 0) {
      lds_wait<1>(CNT(C_KV01), 2);
      if (l == 0) __builtin_amdgcn_raw_buffer_store_b32(tag, rw, 0, EPOCH_OFF, AUX_DEV);
    }
  } else {
    const int wa = w - 3;
    const u32 row0 = rowb + wa * 32;
    const u32 off_frag = (row0 + (c >> 1)) * 384 + (2 * g + (c & 1)) * 16;
    auto rk = __builtin_amdgcn_make_buffer_rsrc((void*)K, 0, TBYTES, 0x00020000);
    auto rq = __builtin_amdgcn_make_buffer_rsrc((void*)Q, 0, TBYTES, 0x00020000);
    auto ro = __builtin_amdgcn_make_buffer_rsrc((void*)O, 0, TBYTES, 0x00020000);
    u32x4 om[3][2], xf[12];
    u32 cpf = 0;
    if (wa == 4) {
      u64 pc;
      asm volatile("s_getpc_b64 %0" : "=s"(pc));
      const u32 off = (((b >> 3) & 3) * 64 + l) * 64;
      cpf = *(const GAS u32*)(pc + (off < CODELEN ? off : 0));
    }
    if (wa >= 4) __builtin_amdgcn_s_barrier();
    __builtin_amdgcn_sched_barrier(0);
#pragma unroll
    for (int n = 0; n < 12; ++n)
      xf[n] = __builtin_amdgcn_raw_buffer_load_b128(rk, off_frag + (n % 3) * 128, (n / 3) * 3072, AUX_NT);
    __builtin_amdgcn_sched_barrier(0);
    if (wa < 4) __builtin_amdgcn_s_barrier();
    else lds_add(CNT(C_GATE));
    asm volatile("" ::"v"(cpf));
    lds_wait<1>(CNT(C_OMEGA), 1);
#pragma unroll
    for (int i = 0; i < 3; ++i)
#pragma unroll
      for (int p = 0; p < 2; ++p) om[i][p] = sm.om[2 * i + p][l];
    {
      u32x4 phiT;
#pragma unroll
      for (int a = 0; a < 4; ++a) {
        float ss = 0.f;
        f32x4 d0 = {0, 0, 0, 0}, d1 = {0, 0, 0, 0};
#pragma unroll
        for (int i = 0; i < 3; ++i) {
          u32x4 ks;
#pragma unroll
          for (int e = 0; e < 4; ++e) {
            ks[e] = pk_bf16(lo_f(xf[3 * a + i][e]) * scale, hi_f(xf[3 * a + i][e]) * scale);
            ss = sumsq2(ks[e], ss);
          }
          d0 = mfma32(ks, om[i][0], d0);
          d1 = mfma32(ks, om[i][1], d1);
        }
        const f32x4 sq = __builtin_amdgcn_mfma_f32_16x16x4f32(ss, 1.0f, (f32x4){0, 0, 0, 0}, 0, 0, 0);
        phiT[a] = phi2_bf16(d0[0] + d1[1], sq[0] + sq[1], d0[2] + d1[3], sq[2] + sq[3]);
      }
      sm.phik[wa][l] = phiT;
    }
    lds_signal(CNT(C_PHIK));
    lds_wait<1>(CNT(C_GATE), 7);
    if (!(__builtin_amdgcn_s_getreg(20) & 1)) __builtin_amdgcn_s_sleep(28);
    __builtin_amdgcn_sched_barrier(0);
#pragma unroll
    for (int n = 0; n < WQ; ++n) {
      xf[n] = __builtin_amdgcn_raw_buffer_load_b128(rq, off_frag + (n % 3) * 128, (n / 3) * 3072, AUX_NT);
      __builtin_amdgcn_sched_barrier(0);
    }
    const bool odd = c & 1;
    const u32 off_row = (row0 + 2 * g) * 384;
    u32x2 phq[2], kv2[4];
    f32x4 zf, ph[2];
    auto part2 = [&](int u) {
      const float dp = fmaf(ph[u][0], zf[0], ph[u][1] * zf[1]) + fmaf(ph[u][2], zf[2], ph[u][3] * zf[3]);
      const f32x4 den = __builtin_amdgcn_mfma_f32_16x16x4f32(1.0f, dp, (f32x4){1e-6f, 1e-6f, 1e-6f, 1e-6f}, 0, 0, 0);
      const float inv = __builtin_amdgcn_rcpf(den[0]);
      phq[u] = (u32x2){pk_bf16(ph[u][0] * inv, ph[u][1] * inv), pk_bf16(ph[u][2] * inv, ph[u][3] * inv)};
      f32x4 num[4];
#pragma unroll
      for (int t = 0; t < 4; ++t) num[t] = mfma16(phq[u], kv2[t], (f32x4){0, 0, 0, 0});
#pragma unroll
      for (int r = 0; r < 4; ++r)
        __builtin_amdgcn_raw_buffer_store_b64((u32x2){pk_bf16(num[0][r], num[1][r]), pk_bf16(num[2][r], num[3][r])}, ro,
                                              off_row + 8 * c + 256 + (r >> 1) * 384, (16 * u + 8 * (r & 1)) * 384,
                                              AUX_PLAIN);
    };
#pragma unroll
    for (int u = 0; u < 2; ++u) {
      float ss[2];
      f32x4 d[2][2];
#pragma unroll
      for (int a = 0; a < 2; ++a) {
        ss[a] = 0.f;
        d[a][0] = (f32x4){0, 0, 0, 0};
        d[a][1] = (f32x4){0, 0, 0, 0};
#pragma unroll
        for (int i = 0; i < 3; ++i) {
          const int n = 6 * u + 3 * a + i;
          if (n + WQ < 12) {
            asm volatile("" : "+v"(xf[n]));
            __builtin_amdgcn_sched_barrier(0);
            xf[n + WQ] = __builtin_amdgcn_raw_buffer_load_b128(rq, off_frag + ((n + WQ) % 3) * 128,
                                                               ((n + WQ) / 3) * 3072, AUX_NT);
            __builtin_amdgcn_sched_barrier(0);
          }
          if (n == QMID) lds_add(CNT(C_QMID));
          if (n == QLATE) lds_add(CNT(C_QLATE));
          if (n == QEARLY) {
            lds_wait<0>(CNT(C_KV2), 1);
            zf = sm.z[g];
#pragma unroll
            for (int t = 0; t < 4; ++t) kv2[t] = sm.kv[4 * (128 + 4 * c + t) + g + 32 + c];
            part2(0);
          }
#pragma unroll
          for (int e = 0; e < 4; ++e) ss[a] = sumsq2(xf[n][e], ss[a]);
          d[a][0] = mfma32(om[i][0], xf[n], d[a][0]);
          d[a][1] = mfma32(om[i][1], xf[n], d[a][1]);
        }
      }
      const float st = (odd ? ss[1] : ss[0]) + swap1(odd ? ss[0] : ss[1]);
      const f32x4 sq = __builtin_amdgcn_mfma_f32_16x16x4f32(1.0f, st, (f32x4){0, 0, 0, 0}, 0, 0, 0);
      const float sl = scale * 1.44269504f, hq = sq[0] * (-0.5f * scale * sl);
#pragma unroll
      for (int r = 0; r < 4; ++r) {
        const float pr = (odd ? d[1][1][r] : d[0][0][r]) + swap1(odd ? d[0][1][r] : d[1][0][r]);
        ph[u][r] = fmaf(__builtin_amdgcn_exp2f(fmaf(pr, sl, hq)), 0.25f, 1e-6f);
      }
    }
    part2(1);
    lds_wait<0>(CNT(C_KV01), 2);
    u32x2 kvf[8];
#pragma unroll
    for (int t = 0; t < 8; ++t) kvf[t] = sm.kv[4 * (8 * c + t) + g + 2 * c + (t >> 2)];
#pragma unroll
    for (int u = 0; u < 2; ++u) {
      f32x4 num[8];
#pragma unroll
      for (int t = 0; t < 8; ++t) num[t] = mfma16(phq[u], kvf[t], (f32x4){0, 0, 0, 0});
#pragma unroll
      for (int r = 0; r < 4; ++r)
        __builtin_amdgcn_raw_buffer_store_b128((u32x4){pk_bf16(num[0][r], num[1][r]), pk_bf16(num[2][r], num[3][r]),
                                                       pk_bf16(num[4][r], num[5][r]), pk_bf16(num[6][r], num[7][r])},
                                               ro, off_row + 16 * c + (r >> 1) * 384, (16 * u + 8 * (r & 1)) * 384, AUX_NT);
    }
  }
}

__device__ __forceinline__ u64 rt() { u64 t; __builtin_amdgcn_sched_barrier(0); asm volatile("s_memrealtime %0\n\ts_waitcnt lgkmcnt(0)" : "=s"(t) :: "memory"); __builtin_amdgcn_sched_barrier(0); return t; }
__global__ void next_k(u64* ts) { if (threadIdx.x == 0) ts[256] = rt(); }
__global__ void spin_k(u64* ts) {
  u64 tb = rt();
  u64 mn = ~0ull;
  for (int i = 0; i < 256; ++i) { u64 x = ts[i]; mn = x < mn ? x : mn; }
  u64 span = ts[256] - mn;
  u64 target = mn + 10000;
  while (rt() < target && rt() < tb + 30000) __builtin_amdgcn_s_sleep(1);
}
extern "C" void kernel_launch(void* const* d_in, const int* in_sizes, int n_in, void* d_out, int out_size,
                              void* d_ws, size_t ws_size, hipStream_t stream) {
  const float scale = 0.26864248f;
  u64* ts = (u64*)((char*)d_ws + (64 << 20));
  rfa_fused<<<H * NCH, 704, 0, stream>>>((const u32*)d_in[0], (const u32*)d_in[1], (const u32*)d_in[2], (const u32*)d_in[3], (u32*)d_out, (u32*)d_ws, scale);
  next_k<<<1, 64, 0, stream>>>(ts);
  spin_k<<<1, 64, 0, stream>>>(ts);
}
```

```cpp
#define VTH 18
#define WK 3
#define EVD 20
#include <hip/hip_runtime.h>
#include <stdint.h>

typedef unsigned u32;
typedef unsigned long long u64;
typedef u32 u32x4 __attribute__((ext_vector_type(4)));
typedef u32 u32x2 __attribute__((ext_vector_type(2)));
typedef float f32x4 __attribute__((ext_vector_type(4)));
typedef float f32x2 __attribute__((ext_vector_type(2)));
typedef __bf16 bf16x8 __attribute__((ext_vector_type(8)));
typedef __bf16 bf16x2 __attribute__((ext_vector_type(2)));
typedef short s16x4 __attribute__((ext_vector_type(4)));
#define LAS __attribute__((address_space(3)))
#define GAS __attribute__((address_space(1)))

constexpr int H = 32, S = 2048, D = 192, M = 16;
constexpr int CH = 256;
constexpr int NCH = S / CH;
constexpr u32 TBYTES = H * S * D * 2;
constexpr u32 CG_BYTES = 2304;
constexpr u32 PART_BYTES = 3 * CG_BYTES + 256;
constexpr u32 EPOCH_OFF = NCH * PART_BYTES;
constexpr u32 HEAD_BYTES = EPOCH_OFF + 256;
constexpr int AUX_NT = 2, AUX_DEV = 16, AUX_PLAIN = 0;
#define WQQ 4
constexpr int WQ = WQQ;
#define QL (11 - WQQ)
#define QM 2
constexpr int QMID = QM, QLATE = QL, QEARLY = 9;
#define KPRE (12 - WK)
#define KS 3
constexpr u32 CODELEN = 11200;

__device__ __forceinline__ float lo_f(u32 x) { return __uint_as_float(x << 16); }
__device__ __forceinline__ float hi_f(u32 x) { return __uint_as_float(x & 0xffff0000u); }
__device__ __forceinline__ u32 pk_bf16(float a, float b) {
  f32x2 v = {a, b};
  bf16x2 r = __builtin_convertvector(v, bf16x2);
  return __builtin_bit_cast(u32, r);
}
__device__ __forceinline__ f32x4 mfma32(u32x4 a, u32x4 b, f32x4 c) {
  return __builtin_amdgcn_mfma_f32_16x16x32_bf16(__builtin_bit_cast(bf16x8, a), __builtin_bit_cast(bf16x8, b), c, 0, 0, 0);
}
__device__ __forceinline__ f32x4 mfma16(u32x2 a, u32x2 b, f32x4 c) {
  return __builtin_amdgcn_mfma_f32_16x16x16bf16_1k(__builtin_bit_cast(s16x4, a), __builtin_bit_cast(s16x4, b), c, 0, 0, 0);
}
__device__ __forceinline__ float sumsq2(u32 p, float acc) {
  return __builtin_amdgcn_fdot2_f32_bf16(__builtin_bit_cast(bf16x2, p), __builtin_bit_cast(bf16x2, p), acc, false);
}
__device__ __forceinline__ u32 phi2_bf16(float p0, float s0, float p1, float s1) {
  const u32 pp = pk_bf16(p0, p1), sp = pk_bf16(s0, s1);
  const u32 tp = pk_bf16(fmaf(lo_f(sp), -0.5f, lo_f(pp)), fmaf(hi_f(sp), -0.5f, hi_f(pp)));
  const u32 ep = pk_bf16(__expf(lo_f(tp)), __expf(hi_f(tp)));
  return pk_bf16(fmaf(lo_f(ep), 0.25f, 1e-6f), fmaf(hi_f(ep), 0.25f, 1e-6f));
}
__device__ __forceinline__ float swap1(float x) {
  return __builtin_bit_cast(float, __builtin_amdgcn_update_dpp(0, __builtin_bit_cast(int, x), 0xB1, 0xf, 0xf, true));
}
__device__ __forceinline__ void lds_add(LAS u32* p) {
  u64 sv;
  asm volatile("s_mov_b64 %0, exec\n\ts_mov_b64 exec, 1\n\tds_add_u32 %1, %2\n\ts_mov_b64 exec, %0"
               : "=&s"(sv) : "v"((u32)(size_t)p), "v"(1u) : "memory");
}
__device__ __forceinline__ void lds_signal(LAS u32* p) {
  asm volatile("s_waitcnt lgkmcnt(0)" ::: "memory");
  lds_add(p);
}
template <int SLEEP>
__device__ __forceinline__ void lds_wait(LAS u32* p, u32 want) {
  while (__builtin_amdgcn_readfirstlane(__hip_atomic_load(p, __ATOMIC_RELAXED, __HIP_MEMORY_SCOPE_WORKGROUP)) < want)
    if (SLEEP) __builtin_amdgcn_s_sleep(SLEEP);
  asm volatile("" ::: "memory");
}
__device__ __forceinline__ u32 xor3(u32 a, u32 b, u32 c) { return __builtin_amdgcn_bitop3_b32(a, b, c, 0x96); }
__device__ __forceinline__ u32 xor9(u32x4 a, u32x4 b, u32 c) {
  return xor3(xor3(a[0], a[1], a[2]), xor3(a[3], b[0], b[1]), xor3(b[2], b[3], c));
}

struct __attribute__((aligned(16))) Smem {
  u32x4 om[6][64];
  u32x4 phik[8][64];
  u32x2 kv[48 * 17];
  f32x4 z[4];
  u32 tag;
  u32 pad[3];
  u32 cnt[8];
};
enum { C_GATE, C_OMEGA, C_PHIK, C_QLATE, C_QMID, C_KV01, C_KV2 };
#define CNT(i) ((LAS u32*)&sm.cnt[i])

__global__ __launch_bounds__(704, 3) void rfa_fused(const u32* __restrict__ Q, const u32* __restrict__ K,
                                                    const u32* __restrict__ V, const u32* __restrict__ Om,
                                                    u32* __restrict__ O, u32* ws, float scale) {
  __shared__ Smem sm;
  const int b = blockIdx.x;
  const int h = (b & 7) * 4 + (b >> 6), j = (b >> 3) & 7;
  const int w = __builtin_amdgcn_readfirstlane(threadIdx.x >> 6);
  const int l = threadIdx.x & 63, g = l >> 4, c = l & 15;
  const u32 rowb = h * S + j * CH;
  if (w < 3) {
    const int cg = w;
    auto rw = __builtin_amdgcn_make_buffer_rsrc((char*)ws + h * HEAD_BYTES, 0, HEAD_BYTES, 0x00020000);
    if (cg == 0) {
      auto rom = __builtin_amdgcn_make_buffer_rsrc((void*)Om, 0, M * D * 2, 0x00020000);
      u32x4 om[6];
#pragma unroll
      for (int i = 0; i < 6; ++i)
        om[i] = __builtin_amdgcn_raw_buffer_load_b128(rom, c * 384 + 2 * g * 16, (i >> 1) * 128 + (i & 1) * 16, 0);
#pragma unroll
      for (int i = 0; i < 6; ++i) sm.om[i][l] = om[i];
      asm volatile("s_waitcnt lgkmcnt(0)" ::: "memory");
      __builtin_amdgcn_s_barrier();
      asm volatile("" ::: "memory");
    } else if (cg == 1) {
      const char* base = g == 0 ? (const char*)Q : g == 1 ? (const char*)O : (const char*)V;
      const char* p = base + rowb * 384 + (c < 2 ? c * 98300 : 49152);
      const u32 pf = __builtin_nontemporal_load((const u32*)p);
      if (l < 8) sm.cnt[l] = 0;
      asm volatile("s_waitcnt lgkmcnt(0)" ::: "memory");
      __builtin_amdgcn_s_barrier();
      asm volatile("" ::"v"(pf) : "memory");
    } else {
      u32 vz;
      asm volatile("v_mov_b32 %0, 0" : "=v"(vz));
      const u32 epoch = __builtin_amdgcn_raw_buffer_load_b32(rw, vz, EPOCH_OFF, AUX_DEV);
      __builtin_amdgcn_sched_barrier(0);
      __builtin_amdgcn_s_barrier();
      asm volatile("" ::: "memory");
      sm.tag = epoch + 1u;
      lds_signal(CNT(C_PHIK));
    }
    auto rv = __builtin_amdgcn_make_buffer_rsrc((void*)V, 0, TBYTES, 0x00020000);
    const u32 voff = (rowb + 2 * g) * 384 + cg * 128 + 8 * c;

    __builtin_amdgcn_sched_barrier(0);
    u32x2 vf[8][4][2];
#pragma unroll
    for (int s = 0; s < 8; ++s)
#pragma unroll
      for (int a = 0; a < 4; ++a)
      {
        if (VTH && s * 8 + a * 2 >= VTH) {
          __builtin_amdgcn_sched_barrier(0);
          asm volatile("s_waitcnt vmcnt(%0)" :: "i"(VTH > 2 ? VTH - 2 : 0) : "memory");
          __builtin_amdgcn_sched_barrier(0);
        }
#pragma unroll
        for (int bb = 0; bb < 2; ++bb)
          vf[s][a][bb] = __builtin_amdgcn_raw_buffer_load_b64(rv, voff + bb * 384, (s * 4 + a) * 3072, AUX_NT);
      }
    __builtin_amdgcn_sched_barrier(0);
    lds_add(CNT(C_GATE));
    lds_wait<1>(CNT(C_PHIK), 9);
    const u32 tag = __builtin_amdgcn_readfirstlane(sm.tag);
    const u32x4 mk0 = {(tag + 0x3c6ef372u) * 0x9e3779b1u, 0u, (tag + 0x78dde6e4u) * 0x85ebca77u, 0u};
    const u32x4 mk1 = {(tag + 0xb54cda56u) * 0xc2b2ae3du, 0u, (tag + 0xf1bbcdc8u) * 0x27d4eb2fu, 0u};
    f32x4 acc[4], zacc = {0, 0, 0, 0};
#pragma unroll
    for (int t = 0; t < 4; ++t) acc[t] = (f32x4){0, 0, 0, 0};
    const u32x4 ones = {0x3f803f80u, 0x3f803f80u, 0x3f803f80u, 0x3f803f80u};
#pragma unroll
    for (int s = 0; s < 8; ++s) {
      const u32x4 phiT = sm.phik[s][l];
      zacc = mfma32(ones, phiT, zacc);
#pragma unroll
      for (int t = 0; t < 4; ++t) {
        u32x4 bf;
        const u32 sel = (t & 1) ? 0x07060302u : 0x05040100u;
#pragma unroll
        for (int a = 0; a < 4; ++a) bf[a] = __builtin_amdgcn_perm(vf[s][a][1][t >> 1], vf[s][a][0][t >> 1], sel);
        acc[t] = mfma32(phiT, bf, acc[t]);
      }
    }
    const u32x4 p0 = {pk_bf16(acc[0][0], acc[0][1]), pk_bf16(acc[0][2], acc[0][3]), pk_bf16(acc[1][0], acc[1][1]),
                      pk_bf16(acc[1][2], acc[1][3])};
    const u32x4 p1 = {pk_bf16(acc[2][0], acc[2][1]), pk_bf16(acc[2][2], acc[2][3]), pk_bf16(acc[3][0], acc[3][1]),
                      pk_bf16(acc[3][2], acc[3][3])};
    {
      const u32 chk = xor9(p0 ^ mk0, p1 ^ mk1, tag);
      const u32 so = j * PART_BYTES + cg * CG_BYTES;
      __builtin_amdgcn_raw_buffer_store_b128(p0 ^ mk0, rw, l * 16, so, AUX_DEV);
      __builtin_amdgcn_raw_buffer_store_b128(p1 ^ mk1, rw, l * 16 + 1024, so, AUX_DEV);
      u32x4 cw;
#pragma unroll
      for (int e = 0; e < 4; ++e) cw[e] = __builtin_amdgcn_ds_bpermute(((4 * l + e) & 63) * 4, chk);
      if (g == 1) cw = (u32x4){tag, __float_as_uint(zacc[0]), tag, __float_as_uint(zacc[0])};
      if (l < (cg == 2 ? 32 : 16)) __builtin_amdgcn_raw_buffer_store_b128(cw, rw, l * 16 + 2048, so, AUX_DEV);
    }
    u32x4 sel[2];
    {
      const u32 one = (c & 1) ? 0x3f800000u : 0x00003f80u;
#pragma unroll
      for (int hf = 0; hf < 2; ++hf)
#pragma unroll
        for (int e = 0; e < 4; ++e) sel[hf][e] = (g == 2 * hf + (c >> 3) && ((c & 7) >> 1) == e) ? one : 0u;
    }
    f32x4 own[2][2];
#pragma unroll
    for (int hf = 0; hf < 2; ++hf) {
      own[0][hf] = mfma32(sel[hf], p0, (f32x4){0, 0, 0, 0});
      own[1][hf] = mfma32(sel[hf], p1, (f32x4){0, 0, 0, 0});
    }
    lds_wait<1>(cg == 2 ? CNT(C_QMID) : CNT(C_QLATE), 8);
    const u32 want = tag ^ mk0[0] ^ mk0[2] ^ mk1[0] ^ mk1[2];
    f32x4 d[2][2];
    u32x2 za, zb;
    for (int spin = 0; spin < (1 << 18); ++spin) {
      u32x4 x[7][2];
      u32 xc[7];
#pragma unroll
      for (int i = 0; i < 7; ++i) {
        const u32 pb = ((j + 1 + i) & 7) * PART_BYTES + cg * CG_BYTES;
        x[i][0] = __builtin_amdgcn_raw_buffer_load_b128(rw, l * 16, pb, AUX_DEV);
        x[i][1] = __builtin_amdgcn_raw_buffer_load_b128(rw, l * 16 + 1024, pb, AUX_DEV);
        xc[i] = __builtin_amdgcn_raw_buffer_load_b32(rw, l * 4 + 2048, pb, AUX_DEV);
      }
      const u32 zo = cg == 2 ? g * PART_BYTES + c * 16 : 0x80000000u;
      za = __builtin_amdgcn_raw_buffer_load_b64(rw, zo, 3 * CG_BYTES, AUX_DEV);
      zb = __builtin_amdgcn_raw_buffer_load_b64(rw, zo, 3 * CG_BYTES + 4 * PART_BYTES, AUX_DEV);
      __builtin_amdgcn_sched_barrier(0);
      u32 bad = 0;
#pragma unroll
      for (int s2 = 0; s2 < 2; ++s2)
#pragma unroll
        for (int hf = 0; hf < 2; ++hf) d[s2][hf] = own[s2][hf];
#pragma unroll
      for (int i = 0; i < 7; ++i) {
        x[i][0] ^= mk0;
        x[i][1] ^= mk1;
#pragma unroll
        for (int s2 = 0; s2 < 2; ++s2)
#pragma unroll
          for (int hf = 0; hf < 2; ++hf) d[s2][hf] = mfma32(sel[hf], x[i][s2], d[s2][hf]);
        bad |= xor9(x[i][0], x[i][1], xc[i]) ^ want;
      }
      if (cg == 2) bad |= (za[0] ^ tag) | (zb[0] ^ tag);
      if (__all(bad == 0)) break;
      __builtin_amdgcn_s_sleep(8);
      asm volatile("" ::: "memory");
    }
#pragma unroll
    for (int s2 = 0; s2 < 2; ++s2)
#pragma unroll
      for (int hf = 0; hf < 2; ++hf)
        sm.kv[4 * (64 * cg + 4 * c + 2 * s2 + (g & 1)) + 2 * hf + (g >> 1) + 16 * cg + c] =
            (u32x2){pk_bf16(d[s2][hf][0], d[s2][hf][1]), pk_bf16(d[s2][hf][2], d[s2][hf][3])};
    if (cg == 2) {
      const f32x4 zs = __builtin_amdgcn_mfma_f32_16x16x4f32(1.0f, __uint_as_float(za[1]) + __uint_as_float(zb[1]),
                                                            (f32x4){0, 0, 0, 0}, 0, 0, 0);
      if (l < 16) ((float*)sm.z)[l] = lo_f(pk_bf16(zs[0], 0.f));
    }
    lds_signal(cg == 2 ? CNT(C_KV2) : CNT(C_KV01));
    if (cg == 2 && j == 0) {
      lds_wait<1>(CNT(C_KV01), 2);
      if (l == 0) __builtin_amdgcn_raw_buffer_store_b32(tag, rw, 0, EPOCH_OFF, AUX_DEV);
    }
  } else {
    const int wa = w - 3;
    const u32 row0 = rowb + wa * 32;
    const u32 off_frag = (row0 + (c >> 1)) * 384 + (2 * g + (c & 1)) * 16;
    auto rk = __builtin_amdgcn_make_buffer_rsrc((void*)K, 0, TBYTES, 0x00020000);
    auto rq = __builtin_amdgcn_make_buffer_rsrc((void*)Q, 0, TBYTES, 0x00020000);
    auto ro = __builtin_amdgcn_make_buffer_rsrc((void*)O, 0, TBYTES, 0x00020000);
    u32x4 om[3][2], xf[12];
    u32 cpf = 0;
    if (wa == 4) {
      u64 pc;
      asm volatile("s_getpc_b64 %0" : "=s"(pc));
      const u32 off = (((b >> 3) & 3) * 64 + l) * 64;
      cpf = *(const GAS u32*)(pc + (off < CODELEN ? off : 0));
    }
    __builtin_amdgcn_sched_barrier(0);
#pragma unroll
    for (int n = 0; n < WK; ++n) {
      xf[n] = __builtin_amdgcn_raw_buffer_load_b128(rk, off_frag + (n % 3) * 128, (n / 3) * 3072, AUX_NT);
      __builtin_amdgcn_sched_barrier(0);
    }
#pragma unroll
    for (int n = 0; n < KPRE; ++n) {
      asm volatile("" : "+v"(xf[n]));
      __builtin_amdgcn_sched_barrier(0);
      xf[n + WK] = __builtin_amdgcn_raw_buffer_load_b128(rk, off_frag + ((n + WK) % 3) * 128, ((n + WK) / 3) * 3072, AUX_NT);
      __builtin_amdgcn_sched_barrier(0);
    }
    __builtin_amdgcn_s_barrier();
    asm volatile("" ::"v"(cpf) : "memory");
#pragma unroll
    for (int i = 0; i < 3; ++i)
#pragma unroll
      for (int p = 0; p < 2; ++p) om[i][p] = sm.om[2 * i + p][l];
    {
      u32x4 phiT;
#pragma unroll
      for (int a = 0; a < 4; ++a) {
        float ss = 0.f;
        f32x4 d0 = {0, 0, 0, 0}, d1 = {0, 0, 0, 0};
#pragma unroll
        for (int i = 0; i < 3; ++i) {
          {
            const int n = 3 * a + i + KPRE;
            if (n + WK < 12) {
              asm volatile("" : "+v"(xf[n]));
              __builtin_amdgcn_sched_barrier(0);
              xf[n + WK] = __builtin_amdgcn_raw_buffer_load_b128(rk, off_frag + ((n + WK) % 3) * 128, ((n + WK) / 3) * 3072, AUX_NT);
              __builtin_amdgcn_sched_barrier(0);
              if (n + WK == 11) lds_add(CNT(C_GATE));
            }
          }
          u32x4 ks;
#pragma unroll
          for (int e = 0; e < 4; ++e) {
            ks[e] = pk_bf16(lo_f(xf[3 * a + i][e]) * scale, hi_f(xf[3 * a + i][e]) * scale);
            ss = sumsq2(ks[e], ss);
          }
          d0 = mfma32(ks, om[i][0], d0);
          d1 = mfma32(ks, om[i][1], d1);
          __builtin_amdgcn_sched_barrier(0);
          __builtin_amdgcn_s_sleep(KS);
          __builtin_amdgcn_sched_barrier(0);
        }
        const f32x4 sq = __builtin_amdgcn_mfma_f32_16x16x4f32(ss, 1.0f, (f32x4){0, 0, 0, 0}, 0, 0, 0);
        phiT[a] = phi2_bf16(d0[0] + d1[1], sq[0] + sq[1], d0[2] + d1[3], sq[2] + sq[3]);
      }
      sm.phik[wa][l] = phiT;
    }
    lds_signal(CNT(C_PHIK));
    lds_wait<1>(CNT(C_GATE), 3);
    if (!(__builtin_amdgcn_s_getreg(20) & 1)) __builtin_amdgcn_s_sleep(EVD);
    __builtin_amdgcn_sched_barrier(0);
#pragma unroll
    for (int n = 0; n < WQ; ++n) {
      xf[n] = __builtin_amdgcn_raw_buffer_load_b128(rq, off_frag + (n % 3) * 128, (n / 3) * 3072, AUX_NT);
      __builtin_amdgcn_sched_barrier(0);
    }
    const bool odd = c & 1;
    const u32 off_row = (row0 + 2 * g) * 384;
    u32x2 phq[2], kv2[4];
    f32x4 zf, ph[2];
    auto part2 = [&](int u) {
      const float dp = fmaf(ph[u][0], zf[0], ph[u][1] * zf[1]) + fmaf(ph[u][2], zf[2], ph[u][3] * zf[3]);
      const f32x4 den = __builtin_amdgcn_mfma_f32_16x16x4f32(1.0f, dp, (f32x4){1e-6f, 1e-6f, 1e-6f, 1e-6f}, 0, 0, 0);
      const float inv = __builtin_amdgcn_rcpf(den[0]);
      phq[u] = (u32x2){pk_bf16(ph[u][0] * inv, ph[u][1] * inv), pk_bf16(ph[u][2] * inv, ph[u][3] * inv)};
      f32x4 num[4];
#pragma unroll
      for (int t = 0; t < 4; ++t) num[t] = mfma16(phq[u], kv2[t], (f32x4){0, 0, 0, 0});
#pragma unroll
      for (int r = 0; r < 4; ++r)
        __builtin_amdgcn_raw_buffer_store_b64((u32x2){pk_bf16(num[0][r], num[1][r]), pk_bf16(num[2][r], num[3][r])}, ro,
                                              off_row + 8 * c + 256 + (r >> 1) * 384, (16 * u + 8 * (r & 1)) * 384,
                                              AUX_PLAIN);
    };
#pragma unroll
    for (int u = 0; u < 2; ++u) {
      float ss[2];
      f32x4 d[2][2];
#pragma unroll
      for (int a = 0; a < 2; ++a) {
        ss[a] = 0.f;
        d[a][0] = (f32x4){0, 0, 0, 0};
        d[a][1] = (f32x4){0, 0, 0, 0};
#pragma unroll
        for (int i = 0; i < 3; ++i) {
          const int n = 6 * u + 3 * a + i;
          if (n + WQ < 12) {
            asm volatile("" : "+v"(xf[n]));
            __builtin_amdgcn_sched_barrier(0);
            xf[n + WQ] = __builtin_amdgcn_raw_buffer_load_b128(rq, off_frag + ((n + WQ) % 3) * 128,
                                                               ((n + WQ) / 3) * 3072, AUX_NT);
            __builtin_amdgcn_sched_barrier(0);
          }
          if (n == QMID) lds_add(CNT(C_QMID));
          if (n == QLATE) lds_add(CNT(C_QLATE));
          if (n == QEARLY) {
            lds_wait<0>(CNT(C_KV2), 1);
            zf = sm.z[g];
#pragma unroll
            for (int t = 0; t < 4; ++t) kv2[t] = sm.kv[4 * (128 + 4 * c + t) + g + 32 + c];
            part2(0);
          }
#pragma unroll
          for (int e = 0; e < 4; ++e) ss[a] = sumsq2(xf[n][e], ss[a]);
          d[a][0] = mfma32(om[i][0], xf[n], d[a][0]);
          d[a][1] = mfma32(om[i][1], xf[n], d[a][1]);
        }
      }
      const float st = (odd ? ss[1] : ss[0]) + swap1(odd ? ss[0] : ss[1]);
      const f32x4 sq = __builtin_amdgcn_mfma_f32_16x16x4f32(1.0f, st, (f32x4){0, 0, 0, 0}, 0, 0, 0);
      const float sl = scale * 1.44269504f, hq = sq[0] * (-0.5f * scale * sl);
#pragma unroll
      for (int r = 0; r < 4; ++r) {
        const float pr = (odd ? d[1][1][r] : d[0][0][r]) + swap1(odd ? d[0][1][r] : d[1][0][r]);
        ph[u][r] = fmaf(__builtin_amdgcn_exp2f(fmaf(pr, sl, hq)), 0.25f, 1e-6f);
      }
    }
    part2(1);
    lds_wait<0>(CNT(C_KV01), 2);
    u32x2 kvf[8];
#pragma unroll
    for (int t = 0; t < 8; ++t) kvf[t] = sm.kv[4 * (8 * c + t) + g + 2 * c + (t >> 2)];
#pragma unroll
    for (int u = 0; u < 2; ++u) {
      f32x4 num[8];
#pragma unroll
      for (int t = 0; t < 8; ++t) num[t] = mfma16(phq[u], kvf[t], (f32x4){0, 0, 0, 0});
#pragma unroll
      for (int r = 0; r < 4; ++r)
        __builtin_amdgcn_raw_buffer_store_b128((u32x4){pk_bf16(num[0][r], num[1][r]), pk_bf16(num[2][r], num[3][r]),
                                                       pk_bf16(num[4][r], num[5][r]), pk_bf16(num[6][r], num[7][r])},
                                               ro, off_row + 16 * c + (r >> 1) * 384, (16 * u + 8 * (r & 1)) * 384, AUX_NT);
    }
  }
}

extern "C" void kernel_launch(void* const* d_in, const int* in_sizes, int n_in, void* d_out, int out_size,
                              void* d_ws, size_t ws_size, hipStream_t stream) {
  const float scale = 0.26864248f;
  rfa_fused<<<H * NCH, 704, 0, stream>>>((const u32*)d_in[0], (const u32*)d_in[1], (const u32*)d_in[2],
                                         (const u32*)d_in[3], (u32*)d_out, (u32*)d_ws, scale);
}
```

```cpp
#include <hip/hip_runtime.h>
#include <stdint.h>

typedef unsigned u32;
typedef unsigned long long u64;
typedef u32 u32x4 __attribute__((ext_vector_type(4)));
typedef u32 u32x2 __attribute__((ext_vector_type(2)));
typedef float f32x4 __attribute__((ext_vector_type(4)));
typedef float f32x2 __attribute__((ext_vector_type(2)));
typedef __bf16 bf16x8 __attribute__((ext_vector_type(8)));
typedef __bf16 bf16x2 __attribute__((ext_vector_type(2)));
typedef short s16x4 __attribute__((ext_vector_type(4)));
#define LAS __attribute__((address_space(3)))
#define GAS __attribute__((address_space(1)))

constexpr int H = 32, S = 2048, D = 192, M = 16;
constexpr int CH = 256;
constexpr int NCH = S / CH;
constexpr u32 TBYTES = H * S * D * 2;
constexpr u32 CG_BYTES = 2304;
constexpr u32 PART_BYTES = 3 * CG_BYTES + 256;
constexpr u32 EPOCH_OFF = NCH * PART_BYTES;
constexpr u32 HEAD_BYTES = EPOCH_OFF + 256;
constexpr int AUX_NT = 2, AUX_DEV = 16, AUX_PLAIN = 0;
constexpr int WK = 3, WV = 18, WQ = 4;
constexpr int QMID = 3, QLATE = 11 - WQ, QEARLY = 9;
constexpr u32 CODELEN = 11072;

__device__ __forceinline__ float lo_f(u32 x) { return __uint_as_float(x << 16); }
__device__ __forceinline__ float hi_f(u32 x) { return __uint_as_float(x & 0xffff0000u); }
__device__ __forceinline__ u32 pk_bf16(float a, float b) {
  f32x2 v = {a, b};
  bf16x2 r = __builtin_convertvector(v, bf16x2);
  return __builtin_bit_cast(u32, r);
}
__device__ __forceinline__ f32x4 mfma32(u32x4 a, u32x4 b, f32x4 c) {
  return __builtin_amdgcn_mfma_f32_16x16x32_bf16(__builtin_bit_cast(bf16x8, a), __builtin_bit_cast(bf16x8, b), c, 0, 0, 0);
}
__device__ __forceinline__ f32x4 mfma16(u32x2 a, u32x2 b, f32x4 c) {
  return __builtin_amdgcn_mfma_f32_16x16x16bf16_1k(__builtin_bit_cast(s16x4, a), __builtin_bit_cast(s16x4, b), c, 0, 0, 0);
}
__device__ __forceinline__ float sumsq2(u32 p, float acc) {
  return __builtin_amdgcn_fdot2_f32_bf16(__builtin_bit_cast(bf16x2, p), __builtin_bit_cast(bf16x2, p), acc, false);
}
__device__ __forceinline__ u32 phi2_bf16(float p0, float s0, float p1, float s1) {
  const u32 pp = pk_bf16(p0, p1), sp = pk_bf16(s0, s1);
  const u32 tp = pk_bf16(fmaf(lo_f(sp), -0.5f, lo_f(pp)), fmaf(hi_f(sp), -0.5f, hi_f(pp)));
  const u32 ep = pk_bf16(__expf(lo_f(tp)), __expf(hi_f(tp)));
  return pk_bf16(fmaf(lo_f(ep), 0.25f, 1e-6f), fmaf(hi_f(ep), 0.25f, 1e-6f));
}
__device__ __forceinline__ float swap1(float x) {
  return __builtin_bit_cast(float, __builtin_amdgcn_update_dpp(0, __builtin_bit_cast(int, x), 0xB1, 0xf, 0xf, true));
}
__device__ __forceinline__ void lds_add(LAS u32* p) {
  u64 sv;
  asm volatile("s_mov_b64 %0, exec\n\ts_mov_b64 exec, 1\n\tds_add_u32 %1, %2\n\ts_mov_b64 exec, %0"
               : "=&s"(sv) : "v"((u32)(size_t)p), "v"(1u) : "memory");
}
__device__ __forceinline__ void lds_signal(LAS u32* p) {
  asm volatile("s_waitcnt lgkmcnt(0)" ::: "memory");
  lds_add(p);
}
template <int SLEEP>
__device__ __forceinline__ void lds_wait(LAS u32* p, u32 want) {
  while (__builtin_amdgcn_readfirstlane(__hip_atomic_load(p, __ATOMIC_RELAXED, __HIP_MEMORY_SCOPE_WORKGROUP)) < want)
    if (SLEEP) __builtin_amdgcn_s_sleep(SLEEP);
  asm volatile("" ::: "memory");
}
__device__ __forceinline__ u32 xor3(u32 a, u32 b, u32 c) { return __builtin_amdgcn_bitop3_b32(a, b, c, 0x96); }
__device__ __forceinline__ u32 xor9(u32x4 a, u32x4 b, u32 c) {
  return xor3(xor3(a[0], a[1], a[2]), xor3(a[3], b[0], b[1]), xor3(b[2], b[3], c));
}

struct __attribute__((aligned(16))) Smem {
  u32x4 om[6][64];
  u32x4 phik[8][64];
  u32x2 kv[48 * 17];
  f32x4 z[4];
  u32 tag;
  u32 pad[3];
  u32 cnt[8];
};
enum { C_VOUT, C_PHIK, C_QLATE, C_QMID, C_KV01, C_KV2 };
#define CNT(i) ((LAS u32*)&sm.cnt[i])

__global__ __launch_bounds__(704, 3) void rfa_fused(const u32* __restrict__ Q, const u32* __restrict__ K,
                                                    const u32* __restrict__ V, const u32* __restrict__ Om,
                                                    u32* __restrict__ O, u32* ws, float scale) {
  __shared__ Smem sm;
  const int b = blockIdx.x;
  const int h = (b & 7) * 4 + (b >> 6), j = (b >> 3) & 7;
  const int w = __builtin_amdgcn_readfirstlane(threadIdx.x >> 6);
  const int l = threadIdx.x & 63, g = l >> 4, c = l & 15;
  const u32 rowb = h * S + j * CH;
  if (w < 3) {
    const int cg = w;
    auto rw = __builtin_amdgcn_make_buffer_rsrc((char*)ws + h * HEAD_BYTES, 0, HEAD_BYTES, 0x00020000);
    if (cg == 0) {
      auto rom = __builtin_amdgcn_make_buffer_rsrc((void*)Om, 0, M * D * 2, 0x00020000);
#pragma unroll
      for (int i = 0; i < 6; ++i)
        sm.om[i][l] = __builtin_amdgcn_raw_buffer_load_b128(rom, c * 384 + 2 * g * 16, (i >> 1) * 128 + (i & 1) * 16, 0);
    } else if (cg == 1) {
      const char* base = g == 0 ? (const char*)Q : g == 1 ? (const char*)O : (const char*)V;
      const u32 pf = __builtin_nontemporal_load((const u32*)(base + rowb * 384 + (c < 2 ? c * 98300 : 49152)));
      if (l < 8) sm.cnt[l] = 0;
      asm volatile("" ::"v"(pf));
    } else {
      sm.tag = __builtin_amdgcn_raw_buffer_load_b32(rw, 0, EPOCH_OFF, AUX_DEV) + 1u;
    }
    asm volatile("s_waitcnt lgkmcnt(0)" ::: "memory");
    __builtin_amdgcn_s_barrier();
    asm volatile("" ::: "memory");
    auto rv = __builtin_amdgcn_make_buffer_rsrc((void*)V, 0, TBYTES, 0x00020000);
    const u32 voff = (rowb + 2 * g) * 384 + cg * 128 + 8 * c;
    __builtin_amdgcn_sched_barrier(0);
    u32x2 vf[8][4][2];
#pragma unroll
    for (int s = 0; s < 8; ++s)
#pragma unroll
      for (int a = 0; a < 4; ++a) {
        if (s * 8 + a * 2 >= WV) {
          __builtin_amdgcn_sched_barrier(0);
          asm volatile("s_waitcnt vmcnt(%0)" ::"i"(WV - 2) : "memory");
          __builtin_amdgcn_sched_barrier(0);
        }
#pragma unroll
        for (int bb = 0; bb < 2; ++bb)
          vf[s][a][bb] = __builtin_amdgcn_raw_buffer_load_b64(rv, voff + bb * 384, (s * 4 + a) * 3072, AUX_NT);
      }
    __builtin_amdgcn_sched_barrier(0);
    lds_add(CNT(C_VOUT));
    lds_wait<1>(CNT(C_PHIK), 8);
    const u32 tag = __builtin_amdgcn_readfirstlane(sm.tag);
    const u32x4 mk0 = {(tag + 0x3c6ef372u) * 0x9e3779b1u, 0u, (tag + 0x78dde6e4u) * 0x85ebca77u, 0u};
    const u32x4 mk1 = {(tag + 0xb54cda56u) * 0xc2b2ae3du, 0u, (tag + 0xf1bbcdc8u) * 0x27d4eb2fu, 0u};
    f32x4 acc[4], zacc = {0, 0, 0, 0};
#pragma unroll
    for (int t = 0; t < 4; ++t) acc[t] = (f32x4){0, 0, 0, 0};
    const u32x4 ones = {0x3f803f80u, 0x3f803f80u, 0x3f803f80u, 0x3f803f80u};
#pragma unroll
    for (int s = 0; s < 8; ++s) {
      const u32x4 phiT = sm.phik[s][l];
      zacc = mfma32(ones, phiT, zacc);
#pragma unroll
      for (int t = 0; t < 4; ++t) {
        u32x4 bf;
        const u32 sel = (t & 1) ? 0x07060302u : 0x05040100u;
#pragma unroll
        for (int a = 0; a < 4; ++a) bf[a] = __builtin_amdgcn_perm(vf[s][a][1][t >> 1], vf[s][a][0][t >> 1], sel);
        acc[t] = mfma32(phiT, bf, acc[t]);
      }
    }
    const u32x4 p0 = {pk_bf16(acc[0][0], acc[0][1]), pk_bf16(acc[0][2], acc[0][3]), pk_bf16(acc[1][0], acc[1][1]),
                      pk_bf16(acc[1][2], acc[1][3])};
    const u32x4 p1 = {pk_bf16(acc[2][0], acc[2][1]), pk_bf16(acc[2][2], acc[2][3]), pk_bf16(acc[3][0], acc[3][1]),
                      pk_bf16(acc[3][2], acc[3][3])};
    {
      const u32 chk = xor9(p0 ^ mk0, p1 ^ mk1, tag);
      const u32 so = j * PART_BYTES + cg * CG_BYTES;
      __builtin_amdgcn_raw_buffer_store_b128(p0 ^ mk0, rw, l * 16, so, AUX_DEV);
      __builtin_amdgcn_raw_buffer_store_b128(p1 ^ mk1, rw, l * 16 + 1024, so, AUX_DEV);
      u32x4 cw;
#pragma unroll
      for (int e = 0; e < 4; ++e) cw[e] = __builtin_amdgcn_ds_bpermute(((4 * l + e) & 63) * 4, chk);
      if (g == 1) cw = (u32x4){tag, __float_as_uint(zacc[0]), tag, __float_as_uint(zacc[0])};
      if (l < (cg == 2 ? 32 : 16)) __builtin_amdgcn_raw_buffer_store_b128(cw, rw, l * 16 + 2048, so, AUX_DEV);
    }
    u32x4 sel[2];
    {
      const u32 one = (c & 1) ? 0x3f800000u : 0x00003f80u;
#pragma unroll
      for (int hf = 0; hf < 2; ++hf)
#pragma unroll
        for (int e = 0; e < 4; ++e) sel[hf][e] = (g == 2 * hf + (c >> 3) && ((c & 7) >> 1) == e) ? one : 0u;
    }
    f32x4 own[2][2];
#pragma unroll
    for (int hf = 0; hf < 2; ++hf) {
      own[0][hf] = mfma32(sel[hf], p0, (f32x4){0, 0, 0, 0});
      own[1][hf] = mfma32(sel[hf], p1, (f32x4){0, 0, 0, 0});
    }
    lds_wait<1>(cg == 2 ? CNT(C_QMID) : CNT(C_QLATE), 8);
    const u32 want = tag ^ mk0[0] ^ mk0[2] ^ mk1[0] ^ mk1[2];
    f32x4 d[2][2];
    u32x2 za, zb;
    for (int spin = 0; spin < (1 << 18); ++spin) {
      u32x4 x[7][2];
      u32 xc[7];
#pragma unroll
      for (int i = 0; i < 7; ++i) {
        const u32 pb = ((j + 1 + i) & 7) * PART_BYTES + cg * CG_BYTES;
        x[i][0] = __builtin_amdgcn_raw_buffer_load_b128(rw, l * 16, pb, AUX_DEV);
        x[i][1] = __builtin_amdgcn_raw_buffer_load_b128(rw, l * 16 + 1024, pb, AUX_DEV);
        xc[i] = __builtin_amdgcn_raw_buffer_load_b32(rw, l * 4 + 2048, pb, AUX_DEV);
      }
      const u32 zo = cg == 2 ? g * PART_BYTES + c * 16 : 0x80000000u;
      za = __builtin_amdgcn_raw_buffer_load_b64(rw, zo, 3 * CG_BYTES, AUX_DEV);
      zb = __builtin_amdgcn_raw_buffer_load_b64(rw, zo, 3 * CG_BYTES + 4 * PART_BYTES, AUX_DEV);
      __builtin_amdgcn_sched_barrier(0);
      u32 bad = 0;
#pragma unroll
      for (int s2 = 0; s2 < 2; ++s2)
#pragma unroll
        for (int hf = 0; hf < 2; ++hf) d[s2][hf] = own[s2][hf];
#pragma unroll
      for (int i = 0; i < 7; ++i) {
        x[i][0] ^= mk0;
        x[i][1] ^= mk1;
#pragma unroll
        for (int s2 = 0; s2 < 2; ++s2)
#pragma unroll
          for (int hf = 0; hf < 2; ++hf) d[s2][hf] = mfma32(sel[hf], x[i][s2], d[s2][hf]);
        bad |= xor9(x[i][0], x[i][1], xc[i]) ^ want;
      }
      if (cg == 2) bad |= (za[0] ^ tag) | (zb[0] ^ tag);
      if (__all(bad == 0)) break;
      __builtin_amdgcn_s_sleep(8);
      asm volatile("" ::: "memory");
    }
#pragma unroll
    for (int s2 = 0; s2 < 2; ++s2)
#pragma unroll
      for (int hf = 0; hf < 2; ++hf)
        sm.kv[4 * (64 * cg + 4 * c + 2 * s2 + (g & 1)) + 2 * hf + (g >> 1) + 16 * cg + c] =
            (u32x2){pk_bf16(d[s2][hf][0], d[s2][hf][1]), pk_bf16(d[s2][hf][2], d[s2][hf][3])};
    if (cg == 2) {
      const f32x4 zs = __builtin_amdgcn_mfma_f32_16x16x4f32(1.0f, __uint_as_float(za[1]) + __uint_as_float(zb[1]),
                                                            (f32x4){0, 0, 0, 0}, 0, 0, 0);
      if (l < 16) ((float*)sm.z)[l] = lo_f(pk_bf16(zs[0], 0.f));
    }
    lds_signal(cg == 2 ? CNT(C_KV2) : CNT(C_KV01));
    if (cg == 2 && j == 0) {
      lds_wait<1>(CNT(C_KV01), 2);
      if (l == 0) __builtin_amdgcn_raw_buffer_store_b32(tag, rw, 0, EPOCH_OFF, AUX_DEV);
    }
  } else {
    const int wa = w - 3;
    const u32 row0 = rowb + wa * 32;
    const u32 off_frag = (row0 + (c >> 1)) * 384 + (2 * g + (c & 1)) * 16;
    auto rk = __builtin_amdgcn_make_buffer_rsrc((void*)K, 0, TBYTES, 0x00020000);
    auto rq = __builtin_amdgcn_make_buffer_rsrc((void*)Q, 0, TBYTES, 0x00020000);
    auto ro = __builtin_amdgcn_make_buffer_rsrc((void*)O, 0, TBYTES, 0x00020000);
    u32x4 om[3][2], xf[12];
    u32 cpf = 0;
    if (wa == 4) {
      u64 pc;
      asm volatile("s_getpc_b64 %0" : "=s"(pc));
      const u32 off = (((b >> 3) & 3) * 64 + l) * 64;
      cpf = *(const GAS u32*)(pc + (off < CODELEN ? off : 0));
    }
    __builtin_amdgcn_sched_barrier(0);
#pragma unroll
    for (int n = 0; n < 12; ++n) {
      if (n >= WK) asm volatile("" : "+v"(xf[n - WK]));
      __builtin_amdgcn_sched_barrier(0);
      xf[n] = __builtin_amdgcn_raw_buffer_load_b128(rk, off_frag + (n % 3) * 128, (n / 3) * 3072, AUX_NT);
      __builtin_amdgcn_sched_barrier(0);
    }
    __builtin_amdgcn_s_barrier();
    asm volatile("" ::"v"(cpf) : "memory");
#pragma unroll
    for (int i = 0; i < 3; ++i)
#pragma unroll
      for (int p = 0; p < 2; ++p) om[i][p] = sm.om[2 * i + p][l];
    {
      u32x4 phiT;
#pragma unroll
      for (int a = 0; a < 4; ++a) {
        float ss = 0.f;
        f32x4 d0 = {0, 0, 0, 0}, d1 = {0, 0, 0, 0};
#pragma unroll
        for (int i = 0; i < 3; ++i) {
          u32x4 ks;
#pragma unroll
          for (int e = 0; e < 4; ++e) {
            ks[e] = pk_bf16(lo_f(xf[3 * a + i][e]) * scale, hi_f(xf[3 * a + i][e]) * scale);
            ss = sumsq2(ks[e], ss);
          }
          d0 = mfma32(ks, om[i][0], d0);
          d1 = mfma32(ks, om[i][1], d1);
          __builtin_amdgcn_sched_barrier(0);
          __builtin_amdgcn_s_sleep(3);
          __builtin_amdgcn_sched_barrier(0);
        }
        const f32x4 sq = __builtin_amdgcn_mfma_f32_16x16x4f32(ss, 1.0f, (f32x4){0, 0, 0, 0}, 0, 0, 0);
        phiT[a] = phi2_bf16(d0[0] + d1[1], sq[0] + sq[1], d0[2] + d1[3], sq[2] + sq[3]);
      }
      sm.phik[wa][l] = phiT;
    }
    lds_signal(CNT(C_PHIK));
    lds_wait<1>(CNT(C_VOUT), 3);
    if (!(__builtin_amdgcn_s_getreg(20) & 1)) __builtin_amdgcn_s_sleep(16);
    __builtin_amdgcn_sched_barrier(0);
#pragma unroll
    for (int n = 0; n < WQ; ++n) {
      xf[n] = __builtin_amdgcn_raw_buffer_load_b128(rq, off_frag + (n % 3) * 128, (n / 3) * 3072, AUX_NT);
      __builtin_amdgcn_sched_barrier(0);
    }
    const bool odd = c & 1;
    const u32 off_row = (row0 + 2 * g) * 384;
    u32x2 phq[2], kv2[4];
    f32x4 zf, ph[2];
    auto part2 = [&](int u) {
      const float dp = fmaf(ph[u][0], zf[0], ph[u][1] * zf[1]) + fmaf(ph[u][2], zf[2], ph[u][3] * zf[3]);
      const f32x4 den = __builtin_amdgcn_mfma_f32_16x16x4f32(1.0f, dp, (f32x4){1e-6f, 1e-6f, 1e-6f, 1e-6f}, 0, 0, 0);
      const float inv = __builtin_amdgcn_rcpf(den[0]);
      phq[u] = (u32x2){pk_bf16(ph[u][0] * inv, ph[u][1] * inv), pk_bf16(ph[u][2] * inv, ph[u][3] * inv)};
      f32x4 num[4];
#pragma unroll
      for (int t = 0; t < 4; ++t) num[t] = mfma16(phq[u], kv2[t], (f32x4){0, 0, 0, 0});
#pragma unroll
      for (int r = 0; r < 4; ++r)
        __builtin_amdgcn_raw_buffer_store_b64((u32x2){pk_bf16(num[0][r], num[1][r]), pk_bf16(num[2][r], num[3][r])}, ro,
                                              off_row + 8 * c + 256 + (r >> 1) * 384, (16 * u + 8 * (r & 1)) * 384,
                                              AUX_PLAIN);
    };
#pragma unroll
    for (int u = 0; u < 2; ++u) {
      float ss[2];
      f32x4 d[2][2];
#pragma unroll
      for (int a = 0; a < 2; ++a) {
        ss[a] = 0.f;
        d[a][0] = (f32x4){0, 0, 0, 0};
        d[a][1] = (f32x4){0, 0, 0, 0};
#pragma unroll
        for (int i = 0; i < 3; ++i) {
          const int n = 6 * u + 3 * a + i;
          if (n + WQ < 12) {
            asm volatile("" : "+v"(xf[n]));
            __builtin_amdgcn_sched_barrier(0);
            xf[n + WQ] = __builtin_amdgcn_raw_buffer_load_b128(rq, off_frag + ((n + WQ) % 3) * 128,
                                                               ((n + WQ) / 3) * 3072, AUX_NT);
            __builtin_amdgcn_sched_barrier(0);
          }
          if (n == QMID) lds_add(CNT(C_QMID));
          if (n == QLATE) lds_add(CNT(C_QLATE));
          if (n == QEARLY) {
            lds_wait<0>(CNT(C_KV2), 1);
            zf = sm.z[g];
#pragma unroll
            for (int t = 0; t < 4; ++t) kv2[t] = sm.kv[4 * (128 + 4 * c + t) + g + 32 + c];
            lds_wait<0>(CNT(C_QLATE), 8);
            part2(0);
            __builtin_amdgcn_sched_barrier(0);
          }
#pragma unroll
          for (int e = 0; e < 4; ++e) ss[a] = sumsq2(xf[n][e], ss[a]);
          d[a][0] = mfma32(om[i][0], xf[n], d[a][0]);
          d[a][1] = mfma32(om[i][1], xf[n], d[a][1]);
        }
      }
      const float st = (odd ? ss[1] : ss[0]) + swap1(odd ? ss[0] : ss[1]);
      const f32x4 sq = __builtin_amdgcn_mfma_f32_16x16x4f32(1.0f, st, (f32x4){0, 0, 0, 0}, 0, 0, 0);
      const float sl = scale * 1.44269504f, hq = sq[0] * (-0.5f * scale * sl);
#pragma unroll
      for (int r = 0; r < 4; ++r) {
        const float pr = (odd ? d[1][1][r] : d[0][0][r]) + swap1(odd ? d[0][1][r] : d[1][0][r]);
        ph[u][r] = fmaf(__builtin_amdgcn_exp2f(fmaf(pr, sl, hq)), 0.25f, 1e-6f);
      }
      if (u == 0) asm volatile("" : "+v"(ph[0]));
    }
    part2(1);
    lds_wait<0>(CNT(C_KV01), 2);
    u32x2 kvf[8];
#pragma unroll
    for (int t = 0; t < 8; ++t) kvf[t] = sm.kv[4 * (8 * c + t) + g + 2 * c + (t >> 2)];
#pragma unroll
    for (int u = 0; u < 2; ++u) {
      f32x4 num[8];
#pragma unroll
      for (int t = 0; t < 8; ++t) num[t] = mfma16(phq[u], kvf[t], (f32x4){0, 0, 0, 0});
#pragma unroll
      for (int r = 0; r < 4; ++r)
        __builtin_amdgcn_raw_buffer_store_b128((u32x4){pk_bf16(num[0][r], num[1][r]), pk_bf16(num[2][r], num[3][r]),
                                                       pk_bf16(num[4][r], num[5][r]), pk_bf16(num[6][r], num[7][r])},
                                               ro, off_row + 16 * c + (r >> 1) * 384, (16 * u + 8 * (r & 1)) * 384, AUX_NT);
    }
  }
}

extern "C" void kernel_launch(void* const* d_in, const int* in_sizes, int n_in, void* d_out, int out_size,
                              void* d_ws, size_t ws_size, hipStream_t stream) {
  const float scale = 0.26864248f;
  rfa_fused<<<H * NCH, 704, 0, stream>>>((const u32*)d_in[0], (const u32*)d_in[1], (const u32*)d_in[2],
                                         (const u32*)d_in[3], (u32*)d_out, (u32*)d_ws, scale);
}
```

```cpp
#include <hip/hip_runtime.h>
#include <stdint.h>

typedef unsigned u32;
typedef unsigned long long u64;
typedef u32 u32x4 __attribute__((ext_vector_type(4)));
typedef u32 u32x2 __attribute__((ext_vector_type(2)));
typedef float f32x4 __attribute__((ext_vector_type(4)));
typedef float f32x2 __attribute__((ext_vector_type(2)));
typedef __bf16 bf16x8 __attribute__((ext_vector_type(8)));
typedef __bf16 bf16x2 __attribute__((ext_vector_type(2)));
typedef short s16x4 __attribute__((ext_vector_type(4)));
#define LAS __attribute__((address_space(3)))
#define GAS __attribute__((address_space(1)))

constexpr int H = 32, S = 2048, D = 192, M = 16;
constexpr int CH = 256;
constexpr int NCH = S / CH;
constexpr u32 TBYTES = H * S * D * 2;
constexpr u32 CG_BYTES = 2304;
constexpr u32 PART_BYTES = 3 * CG_BYTES + 256;
constexpr u32 EPOCH_OFF = NCH * PART_BYTES;
constexpr u32 HEAD_BYTES = EPOCH_OFF + 256;
constexpr int AUX_NT = 2, AUX_DEV = 16, AUX_PLAIN = 0;
constexpr int WQ = 4;
constexpr int QMID = 2, QLATE = 11 - WQ, QEARLY = 9;
constexpr u32 CODELEN = 11200;

__device__ __forceinline__ float lo_f(u32 x) { return __uint_as_float(x << 16); }
__device__ __forceinline__ float hi_f(u32 x) { return __uint_as_float(x & 0xffff0000u); }
__device__ __forceinline__ u32 pk_bf16(float a, float b) {
  f32x2 v = {a, b};
  bf16x2 r = __builtin_convertvector(v, bf16x2);
  return __builtin_bit_cast(u32, r);
}
__device__ __forceinline__ f32x4 mfma32(u32x4 a, u32x4 b, f32x4 c) {
  return __builtin_amdgcn_mfma_f32_16x16x32_bf16(__builtin_bit_cast(bf16x8, a), __builtin_bit_cast(bf16x8, b), c, 0, 0, 0);
}
__device__ __forceinline__ f32x4 mfma16(u32x2 a, u32x2 b, f32x4 c) {
  return __builtin_amdgcn_mfma_f32_16x16x16bf16_1k(__builtin_bit_cast(s16x4, a), __builtin_bit_cast(s16x4, b), c, 0, 0, 0);
}
__device__ __forceinline__ float sumsq2(u32 p, float acc) {
  return __builtin_amdgcn_fdot2_f32_bf16(__builtin_bit_cast(bf16x2, p), __builtin_bit_cast(bf16x2, p), acc, false);
}
__device__ __forceinline__ u32 phi2_bf16(float p0, float s0, float p1, float s1) {
  const u32 pp = pk_bf16(p0, p1), sp = pk_bf16(s0, s1);
  const u32 tp = pk_bf16(fmaf(lo_f(sp), -0.5f, lo_f(pp)), fmaf(hi_f(sp), -0.5f, hi_f(pp)));
  const u32 ep = pk_bf16(__expf(lo_f(tp)), __expf(hi_f(tp)));
  return pk_bf16(fmaf(lo_f(ep), 0.25f, 1e-6f), fmaf(hi_f(ep), 0.25f, 1e-6f));
}
__device__ __forceinline__ float swap1(float x) {
  return __builtin_bit_cast(float, __builtin_amdgcn_update_dpp(0, __builtin_bit_cast(int, x), 0xB1, 0xf, 0xf, true));
}
__device__ __forceinline__ void lds_add(LAS u32* p) {
  u64 sv;
  asm volatile("s_mov_b64 %0, exec\n\ts_mov_b64 exec, 1\n\tds_add_u32 %1, %2\n\ts_mov_b64 exec, %0"
               : "=&s"(sv) : "v"((u32)(size_t)p), "v"(1u) : "memory");
}
__device__ __forceinline__ void lds_signal(LAS u32* p) {
  asm volatile("s_waitcnt lgkmcnt(0)" ::: "memory");
  lds_add(p);
}
template <int SLEEP>
__device__ __forceinline__ void lds_wait(LAS u32* p, u32 want) {
  while (__builtin_amdgcn_readfirstlane(__hip_atomic_load(p, __ATOMIC_RELAXED, __HIP_MEMORY_SCOPE_WORKGROUP)) < want)
    if (SLEEP) __builtin_amdgcn_s_sleep(SLEEP);
  asm volatile("" ::: "memory");
}
__device__ __forceinline__ u32 xor3(u32 a, u32 b, u32 c) { return __builtin_amdgcn_bitop3_b32(a, b, c, 0x96); }
__device__ __forceinline__ u32 xor9(u32x4 a, u32x4 b, u32 c) {
  return xor3(xor3(a[0], a[1], a[2]), xor3(a[3], b[0], b[1]), xor3(b[2], b[3], c));
}

struct __attribute__((aligned(16))) Smem {
  u32x4 om[6][64];
  u32x4 phik[8][64];
  u32x2 kv[48 * 17];
  f32x4 z[4];
  u32 tag;
  u32 pad[3];
  u32 cnt[8];
};
enum { C_GATE, C_OMEGA, C_PHIK, C_QLATE, C_QMID, C_KV01, C_KV2 };
#define CNT(i) ((LAS u32*)&sm.cnt[i])

__global__ __launch_bounds__(704, 3) void rfa_fused(const u32* __restrict__ Q, const u32* __restrict__ K,
                                                    const u32* __restrict__ V, const u32* __restrict__ Om,
                                                    u32* __restrict__ O, u32* ws, float scale) {
  __shared__ Smem sm;
  const int b = blockIdx.x;
  const int h = (b & 7) * 4 + (b >> 6), j = (b >> 3) & 7;
  const int w = __builtin_amdgcn_readfirstlane(threadIdx.x >> 6);
  const int l = threadIdx.x & 63, g = l >> 4, c = l & 15;
  const u32 rowb = h * S + j * CH;
  if (w < 3) {
    const int cg = w;
    auto rw = __builtin_amdgcn_make_buffer_rsrc((char*)ws + h * HEAD_BYTES, 0, HEAD_BYTES, 0x00020000);
    if (cg == 0) {
      __builtin_amdgcn_s_barrier();
      asm volatile("" ::: "memory");
    } else if (cg == 1) {
      const char* base = g == 0 ? (const char*)Q : g == 1 ? (const char*)O : (const char*)V;
      const char* p = base + rowb * 384 + (c < 2 ? c * 98300 : 49152);
      const u32 pf = __builtin_nontemporal_load((const u32*)p);
      if (l < 8) sm.cnt[l] = 0;
      asm volatile("s_waitcnt lgkmcnt(0)" ::: "memory");
      __builtin_amdgcn_s_barrier();
      asm volatile("" ::"v"(pf) : "memory");
    } else {
      u32 vz;
      asm volatile("v_mov_b32 %0, 0" : "=v"(vz));
      const u32 epoch = __builtin_amdgcn_raw_buffer_load_b32(rw, vz, EPOCH_OFF, AUX_DEV);
      __builtin_amdgcn_sched_barrier(0);
      __builtin_amdgcn_s_barrier();
      asm volatile("" ::: "memory");
      sm.tag = epoch + 1u;
      lds_signal(CNT(C_PHIK));
    }
    auto rv = __builtin_amdgcn_make_buffer_rsrc((void*)V, 0, TBYTES, 0x00020000);
    const u32 voff = (rowb + 2 * g) * 384 + cg * 128 + 8 * c;
    lds_wait<1>(CNT(C_GATE), 4);
    __builtin_amdgcn_sched_barrier(0);
    u32x2 vf[8][4][2];
#pragma unroll
    for (int s = 0; s < 8; ++s)
#pragma unroll
      for (int a = 0; a < 4; ++a)
#pragma unroll
        for (int bb = 0; bb < 2; ++bb)
          vf[s][a][bb] = __builtin_amdgcn_raw_buffer_load_b64(rv, voff + bb * 384, (s * 4 + a) * 3072, AUX_NT);
    __builtin_amdgcn_sched_barrier(0);
    lds_add(CNT(C_GATE));
    lds_wait<1>(CNT(C_PHIK), 9);
    const u32 tag = __builtin_amdgcn_readfirstlane(sm.tag);
    const u32x4 mk0 = {(tag + 0x3c6ef372u) * 0x9e3779b1u, 0u, (tag + 0x78dde6e4u) * 0x85ebca77u, 0u};
    const u32x4 mk1 = {(tag + 0xb54cda56u) * 0xc2b2ae3du, 0u, (tag + 0xf1bbcdc8u) * 0x27d4eb2fu, 0u};
    f32x4 acc[4], zacc = {0, 0, 0, 0};
#pragma unroll
    for (int t = 0; t < 4; ++t) acc[t] = (f32x4){0, 0, 0, 0};
    const u32x4 ones = {0x3f803f80u, 0x3f803f80u, 0x3f803f80u, 0x3f803f80u};
#pragma unroll
    for (int s = 0; s < 8; ++s) {
      const u32x4 phiT = sm.phik[s][l];
      zacc = mfma32(ones, phiT, zacc);
#pragma unroll
      for (int t = 0; t < 4; ++t) {
        u32x4 bf;
        const u32 sel = (t & 1) ? 0x07060302u : 0x05040100u;
#pragma unroll
        for (int a = 0; a < 4; ++a) bf[a] = __builtin_amdgcn_perm(vf[s][a][1][t >> 1], vf[s][a][0][t >> 1], sel);
        acc[t] = mfma32(phiT, bf, acc[t]);
      }
    }
    const u32x4 p0 = {pk_bf16(acc[0][0], acc[0][1]), pk_bf16(acc[0][2], acc[0][3]), pk_bf16(acc[1][0], acc[1][1]),
                      pk_bf16(acc[1][2], acc[1][3])};
    const u32x4 p1 = {pk_bf16(acc[2][0], acc[2][1]), pk_bf16(acc[2][2], acc[2][3]), pk_bf16(acc[3][0], acc[3][1]),
                      pk_bf16(acc[3][2], acc[3][3])};
    {
      const u32 chk = xor9(p0 ^ mk0, p1 ^ mk1, tag);
      const u32 so = j * PART_BYTES + cg * CG_BYTES;
      __builtin_amdgcn_raw_buffer_store_b128(p0 ^ mk0, rw, l * 16, so, AUX_DEV);
      __builtin_amdgcn_raw_buffer_store_b128(p1 ^ mk1, rw, l * 16 + 1024, so, AUX_DEV);
      u32x4 cw;
#pragma unroll
      for (int e = 0; e < 4; ++e) cw[e] = __builtin_amdgcn_ds_bpermute(((4 * l + e) & 63) * 4, chk);
      if (g == 1) cw = (u32x4){tag, __float_as_uint(zacc[0]), tag, __float_as_uint(zacc[0])};
      if (l < (cg == 2 ? 32 : 16)) __builtin_amdgcn_raw_buffer_store_b128(cw, rw, l * 16 + 2048, so, AUX_DEV);
    }
    u32x4 sel[2];
    {
      const u32 one = (c & 1) ? 0x3f800000u : 0x00003f80u;
#pragma unroll
      for (int hf = 0; hf < 2; ++hf)
#pragma unroll
        for (int e = 0; e < 4; ++e) sel[hf][e] = (g == 2 * hf + (c >> 3) && ((c & 7) >> 1) == e) ? one : 0u;
    }
    f32x4 own[2][2];
#pragma unroll
    for (int hf = 0; hf < 2; ++hf) {
      own[0][hf] = mfma32(sel[hf], p0, (f32x4){0, 0, 0, 0});
      own[1][hf] = mfma32(sel[hf], p1, (f32x4){0, 0, 0, 0});
    }
    lds_wait<1>(cg == 2 ? CNT(C_QMID) : CNT(C_QLATE), 8);
    const u32 want = tag ^ mk0[0] ^ mk0[2] ^ mk1[0] ^ mk1[2];
    f32x4 d[2][2];
    u32x2 za, zb;
    for (int spin = 0; spin < (1 << 18); ++spin) {
      u32x4 x[7][2];
      u32 xc[7];
#pragma unroll
      for (int i = 0; i < 7; ++i) {
        const u32 pb = ((j + 1 + i) & 7) * PART_BYTES + cg * CG_BYTES;
        x[i][0] = __builtin_amdgcn_raw_buffer_load_b128(rw, l * 16, pb, AUX_DEV);
        x[i][1] = __builtin_amdgcn_raw_buffer_load_b128(rw, l * 16 + 1024, pb, AUX_DEV);
        xc[i] = __builtin_amdgcn_raw_buffer_load_b32(rw, l * 4 + 2048, pb, AUX_DEV);
      }
      const u32 zo = cg == 2 ? g * PART_BYTES + c * 16 : 0x80000000u;
      za = __builtin_amdgcn_raw_buffer_load_b64(rw, zo, 3 * CG_BYTES, AUX_DEV);
      zb = __builtin_amdgcn_raw_buffer_load_b64(rw, zo, 3 * CG_BYTES + 4 * PART_BYTES, AUX_DEV);
      __builtin_amdgcn_sched_barrier(0);
      u32 bad = 0;
#pragma unroll
      for (int s2 = 0; s2 < 2; ++s2)
#pragma unroll
        for (int hf = 0; hf < 2; ++hf) d[s2][hf] = own[s2][hf];
#pragma unroll
      for (int i = 0; i < 7; ++i) {
        x[i][0] ^= mk0;
        x[i][1] ^= mk1;
#pragma unroll
        for (int s2 = 0; s2 < 2; ++s2)
#pragma unroll
          for (int hf = 0; hf < 2; ++hf) d[s2][hf] = mfma32(sel[hf], x[i][s2], d[s2][hf]);
        bad |= xor9(x[i][0], x[i][1], xc[i]) ^ want;
      }
      if (cg == 2) bad |= (za[0] ^ tag) | (zb[0] ^ tag);
      if (__all(bad == 0)) break;
      __builtin_amdgcn_s_sleep(8);
      asm volatile("" ::: "memory");
    }
#pragma unroll
    for (int s2 = 0; s2 < 2; ++s2)
#pragma unroll
      for (int hf = 0; hf < 2; ++hf)
        sm.kv[4 * (64 * cg + 4 * c + 2 * s2 + (g & 1)) + 2 * hf + (g >> 1) + 16 * cg + c] =
            (u32x2){pk_bf16(d[s2][hf][0], d[s2][hf][1]), pk_bf16(d[s2][hf][2], d[s2][hf][3])};
    if (cg == 2) {
      const f32x4 zs = __builtin_amdgcn_mfma_f32_16x16x4f32(1.0f, __uint_as_float(za[1]) + __uint_as_float(zb[1]),
                                                            (f32x4){0, 0, 0, 0}, 0, 0, 0);
      if (l < 16) ((float*)sm.z)[l] = lo_f(pk_bf16(zs[0], 0.f));
    }
    lds_signal(cg == 2 ? CNT(C_KV2) : CNT(C_KV01));
    if (cg == 2 && j == 0) {
      lds_wait<1>(CNT(C_KV01), 2);
      if (l == 0) __builtin_amdgcn_raw_buffer_store_b32(tag, rw, 0, EPOCH_OFF, AUX_DEV);
    }
  } else {
    const int wa = w - 3;
    const u32 row0 = rowb + wa * 32;
    const u32 off_frag = (row0 + (c >> 1)) * 384 + (2 * g + (c & 1)) * 16;
    auto rk = __builtin_amdgcn_make_buffer_rsrc((void*)K, 0, TBYTES, 0x00020000);
    auto rq = __builtin_amdgcn_make_buffer_rsrc((void*)Q, 0, TBYTES, 0x00020000);
    auto ro = __builtin_amdgcn_make_buffer_rsrc((void*)O, 0, TBYTES, 0x00020000);
    u32x4 om[3][2], xf[12];
    u32 cpf = 0;
    if (wa == 4) {
      u64 pc;
      asm volatile("s_getpc_b64 %0" : "=s"(pc));
      const u32 off = (((b >> 3) & 3) * 64 + l) * 64;
      cpf = *(const GAS u32*)(pc + (off < CODELEN ? off : 0));
    }
    if (wa >= 4) __builtin_amdgcn_s_barrier();
    __builtin_amdgcn_sched_barrier(0);
#pragma unroll
    for (int n = 0; n < 12; ++n)
      xf[n] = __builtin_amdgcn_raw_buffer_load_b128(rk, off_frag + (n % 3) * 128, (n / 3) * 3072, AUX_NT);
    __builtin_amdgcn_sched_barrier(0);
    {
      auto rom = __builtin_amdgcn_make_buffer_rsrc((void*)Om, 0, wa == 0 ? M * D * 2 : 0, 0x00020000);
#pragma unroll
      for (int i = 0; i < 6; ++i) om[i >> 1][i & 1] = __builtin_amdgcn_raw_buffer_load_b128(rom, l * 16, i * 1024, 0);
    }
    __builtin_amdgcn_sched_barrier(0);
    if (wa < 4) __builtin_amdgcn_s_barrier();
    else lds_add(CNT(C_GATE));
    asm volatile("" ::"v"(cpf) : "memory");
    if (wa == 0) {
#pragma unroll
      for (int i = 0; i < 6; ++i) {
        const u32 n = 64 * i + l, m = n / 24, cc = n - 24 * m;
        sm.om[(cc >> 3) * 2 + (cc & 1)][16 * ((cc & 7) >> 1) + m] = om[i >> 1][i & 1];
      }
      lds_signal(CNT(C_OMEGA));
    }
    lds_wait<1>(CNT(C_OMEGA), 1);
#pragma unroll
    for (int i = 0; i < 3; ++i)
#pragma unroll
      for (int p = 0; p < 2; ++p) om[i][p] = sm.om[2 * i + p][l];
    {
      u32x4 phiT;
#pragma unroll
      for (int a = 0; a < 4; ++a) {
        float ss = 0.f;
        f32x4 d0 = {0, 0, 0, 0}, d1 = {0, 0, 0, 0};
#pragma unroll
        for (int i = 0; i < 3; ++i) {
          u32x4 ks;
#pragma unroll
          for (int e = 0; e < 4; ++e) {
            ks[e] = pk_bf16(lo_f(xf[3 * a + i][e]) * scale, hi_f(xf[3 * a + i][e]) * scale);
            ss = sumsq2(ks[e], ss);
          }
          d0 = mfma32(ks, om[i][0], d0);
          d1 = mfma32(ks, om[i][1], d1);
          __builtin_amdgcn_sched_barrier(0);
          __builtin_amdgcn_s_sleep(3);
          __builtin_amdgcn_sched_barrier(0);
        }
        const f32x4 sq = __builtin_amdgcn_mfma_f32_16x16x4f32(ss, 1.0f, (f32x4){0, 0, 0, 0}, 0, 0, 0);
        phiT[a] = phi2_bf16(d0[0] + d1[1], sq[0] + sq[1], d0[2] + d1[3], sq[2] + sq[3]);
      }
      sm.phik[wa][l] = phiT;
    }
    lds_signal(CNT(C_PHIK));
    lds_wait<1>(CNT(C_GATE), 7);
    if (!(__builtin_amdgcn_s_getreg(20) & 1)) __builtin_amdgcn_s_sleep(28);
    __builtin_amdgcn_sched_barrier(0);
#pragma unroll
    for (int n = 0; n < WQ; ++n) {
      xf[n] = __builtin_amdgcn_raw_buffer_load_b128(rq, off_frag + (n % 3) * 128, (n / 3) * 3072, AUX_NT);
      __builtin_amdgcn_sched_barrier(0);
    }
    const bool odd = c & 1;
    const u32 off_row = (row0 + 2 * g) * 384;
    u32x2 phq[2], kv2[4];
    f32x4 zf, ph[2];
    auto part2 = [&](int u) {
      const float dp = fmaf(ph[u][0], zf[0], ph[u][1] * zf[1]) + fmaf(ph[u][2], zf[2], ph[u][3] * zf[3]);
      const f32x4 den = __builtin_amdgcn_mfma_f32_16x16x4f32(1.0f, dp, (f32x4){1e-6f, 1e-6f, 1e-6f, 1e-6f}, 0, 0, 0);
      const float inv = __builtin_amdgcn_rcpf(den[0]);
      phq[u] = (u32x2){pk_bf16(ph[u][0] * inv, ph[u][1] * inv), pk_bf16(ph[u][2] * inv, ph[u][3] * inv)};
      f32x4 num[4];
#pragma unroll
      for (int t = 0; t < 4; ++t) num[t] = mfma16(phq[u], kv2[t], (f32x4){0, 0, 0, 0});
#pragma unroll
      for (int r = 0; r < 4; ++r)
        __builtin_amdgcn_raw_buffer_store_b64((u32x2){pk_bf16(num[0][r], num[1][r]), pk_bf16(num[2][r], num[3][r])}, ro,
                                              off_row + 8 * c + 256 + (r >> 1) * 384, (16 * u + 8 * (r & 1)) * 384,
                                              AUX_PLAIN);
    };
#pragma unroll
    for (int u = 0; u < 2; ++u) {
      float ss[2];
      f32x4 d[2][2];
#pragma unroll
      for (int a = 0; a < 2; ++a) {
        ss[a] = 0.f;
        d[a][0] = (f32x4){0, 0, 0, 0};
        d[a][1] = (f32x4){0, 0, 0, 0};
#pragma unroll
        for (int i = 0; i < 3; ++i) {
          const int n = 6 * u + 3 * a + i;
          if (n + WQ < 12) {
            asm volatile("" : "+v"(xf[n]));
            __builtin_amdgcn_sched_barrier(0);
            xf[n + WQ] = __builtin_amdgcn_raw_buffer_load_b128(rq, off_frag + ((n + WQ) % 3) * 128,
                                                               ((n + WQ) / 3) * 3072, AUX_NT);
            __builtin_amdgcn_sched_barrier(0);
          }
          if (n == QMID) lds_add(CNT(C_QMID));
          if (n == QLATE) lds_add(CNT(C_QLATE));
          if (n == QEARLY) {
            lds_wait<0>(CNT(C_KV2), 1);
            zf = sm.z[g];
#pragma unroll
            for (int t = 0; t < 4; ++t) kv2[t] = sm.kv[4 * (128 + 4 * c + t) + g + 32 + c];
            part2(0);
          }
#pragma unroll
          for (int e = 0; e < 4; ++e) ss[a] = sumsq2(xf[n][e], ss[a]);
          d[a][0] = mfma32(om[i][0], xf[n], d[a][0]);
          d[a][1] = mfma32(om[i][1], xf[n], d[a][1]);
        }
      }
      const float st = (odd ? ss[1] : ss[0]) + swap1(odd ? ss[0] : ss[1]);
      const f32x4 sq = __builtin_amdgcn_mfma_f32_16x16x4f32(1.0f, st, (f32x4){0, 0, 0, 0}, 0, 0, 0);
      const float sl = scale * 1.44269504f, hq = sq[0] * (-0.5f * scale * sl);
#pragma unroll
      for (int r = 0; r < 4; ++r) {
        const float pr = (odd ? d[1][1][r] : d[0][0][r]) + swap1(odd ? d[0][1][r] : d[1][0][r]);
        ph[u][r] = fmaf(__builtin_amdgcn_exp2f(fmaf(pr, sl, hq)), 0.25f, 1e-6f);
      }
    }
    part2(1);
    lds_wait<0>(CNT(C_KV01), 2);
    u32x2 kvf[8];
#pragma unroll
    for (int t = 0; t < 8; ++t) kvf[t] = sm.kv[4 * (8 * c + t) + g + 2 * c + (t >> 2)];
#pragma unroll
    for (int u = 0; u < 2; ++u) {
      f32x4 num[8];
#pragma unroll
      for (int t = 0; t < 8; ++t) num[t] = mfma16(phq[u], kvf[t], (f32x4){0, 0, 0, 0});
#pragma unroll
      for (int r = 0; r < 4; ++r)
        __builtin_amdgcn_raw_buffer_store_b128((u32x4){pk_bf16(num[0][r], num[1][r]), pk_bf16(num[2][r], num[3][r]),
                                                       pk_bf16(num[4][r], num[5][r]), pk_bf16(num[6][r], num[7][r])},
                                               ro, off_row + 16 * c + (r >> 1) * 384, (16 * u + 8 * (r & 1)) * 384, AUX_NT);
    }
  }
}

extern "C" void kernel_launch(void* const* d_in, const int* in_sizes, int n_in, void* d_out, int out_size,
                              void* d_ws, size_t ws_size, hipStream_t stream) {
  const float scale = 0.26864248f;
  rfa_fused<<<H * NCH, 704, 0, stream>>>((const u32*)d_in[0], (const u32*)d_in[1], (const u32*)d_in[2],
                                         (const u32*)d_in[3], (u32*)d_out, (u32*)d_ws, scale);
}
```

```cpp
#include <hip/hip_runtime.h>
#include <stdint.h>

typedef unsigned u32;
typedef unsigned long long u64;
typedef u32 u32x4 __attribute__((ext_vector_type(4)));
typedef u32 u32x2 __attribute__((ext_vector_type(2)));
typedef float f32x4 __attribute__((ext_vector_type(4)));
typedef float f32x2 __attribute__((ext_vector_type(2)));
typedef __bf16 bf16x8 __attribute__((ext_vector_type(8)));
typedef __bf16 bf16x2 __attribute__((ext_vector_type(2)));
typedef short s16x4 __attribute__((ext_vector_type(4)));
#define LAS __attribute__((address_space(3)))
#define GAS __attribute__((address_space(1)))

constexpr int H = 32, S = 2048, D = 192, M = 16;
constexpr int CH = 256;
constexpr int NCH = S / CH;
constexpr u32 TBYTES = H * S * D * 2;
constexpr u32 CG_BYTES = 2304;
constexpr u32 PART_BYTES = 3 * CG_BYTES + 256;
constexpr u32 EPOCH_OFF = NCH * PART_BYTES;
constexpr u32 HEAD_BYTES = EPOCH_OFF + 256;
constexpr int AUX_NT = 2, AUX_DEV = 16, AUX_PLAIN = 0;
constexpr int WK = 3, WV = 18, WQ = 4;
constexpr int QMID = 3, QLATE = 11 - WQ, QEARLY = 9;
constexpr u32 CODELEN = 11072;

__device__ __forceinline__ float lo_f(u32 x) { return __uint_as_float(x << 16); }
__device__ __forceinline__ float hi_f(u32 x) { return __uint_as_float(x & 0xffff0000u); }
__device__ __forceinline__ u32 pk_bf16(float a, float b) {
  f32x2 v = {a, b};
  bf16x2 r = __builtin_convertvector(v, bf16x2);
  return __builtin_bit_cast(u32, r);
}
__device__ __forceinline__ f32x4 mfma32(u32x4 a, u32x4 b, f32x4 c) {
  return __builtin_amdgcn_mfma_f32_16x16x32_bf16(__builtin_bit_cast(bf16x8, a), __builtin_bit_cast(bf16x8, b), c, 0, 0, 0);
}
__device__ __forceinline__ f32x4 mfma16(u32x2 a, u32x2 b, f32x4 c) {
  return __builtin_amdgcn_mfma_f32_16x16x16bf16_1k(__builtin_bit_cast(s16x4, a), __builtin_bit_cast(s16x4, b), c, 0, 0, 0);
}
__device__ __forceinline__ float sumsq2(u32 p, float acc) {
  return __builtin_amdgcn_fdot2_f32_bf16(__builtin_bit_cast(bf16x2, p), __builtin_bit_cast(bf16x2, p), acc, false);
}
__device__ __forceinline__ u32 phi2_bf16(float p0, float s0, float p1, float s1) {
  const u32 pp = pk_bf16(p0, p1), sp = pk_bf16(s0, s1);
  const u32 tp = pk_bf16(fmaf(lo_f(sp), -0.5f, lo_f(pp)), fmaf(hi_f(sp), -0.5f, hi_f(pp)));
  const u32 ep = pk_bf16(__expf(lo_f(tp)), __expf(hi_f(tp)));
  return pk_bf16(fmaf(lo_f(ep), 0.25f, 1e-6f), fmaf(hi_f(ep), 0.25f, 1e-6f));
}
__device__ __forceinline__ float swap1(float x) {
  return __builtin_bit_cast(float, __builtin_amdgcn_update_dpp(0, __builtin_bit_cast(int, x), 0xB1, 0xf, 0xf, true));
}
__device__ __forceinline__ void lds_add(LAS u32* p) {
  u64 sv;
  asm volatile("s_mov_b64 %0, exec\n\ts_mov_b64 exec, 1\n\tds_add_u32 %1, %2\n\ts_mov_b64 exec, %0"
               : "=&s"(sv) : "v"((u32)(size_t)p), "v"(1u) : "memory");
}
__device__ __forceinline__ void lds_signal(LAS u32* p) {
  asm volatile("s_waitcnt lgkmcnt(0)" ::: "memory");
  lds_add(p);
}
template <int SLEEP>
__device__ __forceinline__ void lds_wait(LAS u32* p, u32 want) {
  while (__builtin_amdgcn_readfirstlane(__hip_atomic_load(p, __ATOMIC_RELAXED, __HIP_MEMORY_SCOPE_WORKGROUP)) < want)
    if (SLEEP) __builtin_amdgcn_s_sleep(SLEEP);
  asm volatile("" ::: "memory");
}
__device__ __forceinline__ u32 xor3(u32 a, u32 b, u32 c) { return __builtin_amdgcn_bitop3_b32(a, b, c, 0x96); }
__device__ __forceinline__ u32 xor9(u32x4 a, u32x4 b, u32 c) {
  return xor3(xor3(a[0], a[1], a[2]), xor3(a[3], b[0], b[1]), xor3(b[2], b[3], c));
}

struct __attribute__((aligned(16))) Smem {
  u32x4 om[6][64];
  u32x4 phik[8][64];
  u32x2 kv[48 * 17];
  f32x4 z[4];
  u32 tag;
  u32 pad[3];
  u32 cnt[8];
};
enum { C_VOUT, C_PHIK, C_QLATE, C_QMID, C_KV01, C_KV2 };
#define CNT(i) ((LAS u32*)&sm.cnt[i])

__global__ __launch_bounds__(704, 3) void rfa_fused(const u32* __restrict__ Q, const u32* __restrict__ K,
                                                    const u32* __restrict__ V, const u32* __restrict__ Om,
                                                    u32* __restrict__ O, u32* ws, float scale) {
  __shared__ Smem sm;
  const int b = blockIdx.x;
  const int h = (b & 7) * 4 + (b >> 6), j = (b >> 3) & 7;
  const int w = __builtin_amdgcn_readfirstlane(threadIdx.x >> 6);
  const int l = threadIdx.x & 63, g = l >> 4, c = l & 15;
  const u32 rowb = h * S + j * CH;
  if (w < 3) {
    const int cg = w;
    auto rw = __builtin_amdgcn_make_buffer_rsrc((char*)ws + h * HEAD_BYTES, 0, HEAD_BYTES, 0x00020000);
    if (cg == 0) {
      auto rom = __builtin_amdgcn_make_buffer_rsrc((void*)Om, 0, M * D * 2, 0x00020000);
#pragma unroll
      for (int i = 0; i < 6; ++i)
        sm.om[i][l] = __builtin_amdgcn_raw_buffer_load_b128(rom, c * 384 + 2 * g * 16, (i >> 1) * 128 + (i & 1) * 16, 0);
    } else if (cg == 1) {
      const char* base = g == 0 ? (const char*)Q : g == 1 ? (const char*)O : (const char*)V;
      const u32 pf = __builtin_nontemporal_load((const u32*)(base + rowb * 384 + (c < 2 ? c * 98300 : 49152)));
      if (l < 8) sm.cnt[l] = 0;
      asm volatile("" ::"v"(pf));
    } else {
      sm.tag = __builtin_amdgcn_raw_buffer_load_b32(rw, 0, EPOCH_OFF, AUX_DEV) + 1u;
    }
    asm volatile("s_waitcnt lgkmcnt(0)" ::: "memory");
    __builtin_amdgcn_s_barrier();
    asm volatile("" ::: "memory");
    auto rv = __builtin_amdgcn_make_buffer_rsrc((void*)V, 0, TBYTES, 0x00020000);
    const u32 voff = (rowb + 2 * g) * 384 + cg * 128 + 8 * c;
    __builtin_amdgcn_sched_barrier(0);
    u32x2 vf[8][4][2];
#pragma unroll
    for (int s = 0; s < 8; ++s)
#pragma unroll
      for (int a = 0; a < 4; ++a) {
        if (s * 8 + a * 2 >= WV) {
          __builtin_amdgcn_sched_barrier(0);
          asm volatile("s_waitcnt vmcnt(%0)" ::"i"(WV - 2) : "memory");
          __builtin_amdgcn_sched_barrier(0);
        }
#pragma unroll
        for (int bb = 0; bb < 2; ++bb)
          vf[s][a][bb] = __builtin_amdgcn_raw_buffer_load_b64(rv, voff + bb * 384, (s * 4 + a) * 3072, AUX_NT);
      }
    __builtin_amdgcn_sched_barrier(0);
    lds_add(CNT(C_VOUT));
    lds_wait<1>(CNT(C_PHIK), 8);
    const u32 tag = __builtin_amdgcn_readfirstlane(sm.tag);
    const u32x4 mk0 = {(tag + 0x3c6ef372u) * 0x9e3779b1u, 0u, (tag + 0x78dde6e4u) * 0x85ebca77u, 0u};
    const u32x4 mk1 = {(tag + 0xb54cda56u) * 0xc2b2ae3du, 0u, (tag + 0xf1bbcdc8u) * 0x27d4eb2fu, 0u};
    f32x4 acc[4], zacc = {0, 0, 0, 0};
#pragma unroll
    for (int t = 0; t < 4; ++t) acc[t] = (f32x4){0, 0, 0, 0};
    const u32x4 ones = {0x3f803f80u, 0x3f803f80u, 0x3f803f80u, 0x3f803f80u};
#pragma unroll
    for (int s = 0; s < 8; ++s) {
      const u32x4 phiT = sm.phik[s][l];
      zacc = mfma32(ones, phiT, zacc);
#pragma unroll
      for (int t = 0; t < 4; ++t) {
        u32x4 bf;
        const u32 sel = (t & 1) ? 0x07060302u : 0x05040100u;
#pragma unroll
        for (int a = 0; a < 4; ++a) bf[a] = __builtin_amdgcn_perm(vf[s][a][1][t >> 1], vf[s][a][0][t >> 1], sel);
        acc[t] = mfma32(phiT, bf, acc[t]);
      }
    }
    const u32x4 p0 = {pk_bf16(acc[0][0], acc[0][1]), pk_bf16(acc[0][2], acc[0][3]), pk_bf16(acc[1][0], acc[1][1]),
                      pk_bf16(acc[1][2], acc[1][3])};
    const u32x4 p1 = {pk_bf16(acc[2][0], acc[2][1]), pk_bf16(acc[2][2], acc[2][3]), pk_bf16(acc[3][0], acc[3][1]),
                      pk_bf16(acc[3][2], acc[3][3])};
    {
      const u32 chk = xor9(p0 ^ mk0, p1 ^ mk1, tag);
      const u32 so = j * PART_BYTES + cg * CG_BYTES;
      __builtin_amdgcn_raw_buffer_store_b128(p0 ^ mk0, rw, l * 16, so, AUX_DEV);
      __builtin_amdgcn_raw_buffer_store_b128(p1 ^ mk1, rw, l * 16 + 1024, so, AUX_DEV);
      u32x4 cw;
#pragma unroll
      for (int e = 0; e < 4; ++e) cw[e] = __builtin_amdgcn_ds_bpermute(((4 * l + e) & 63) * 4, chk);
      if (g == 1) cw = (u32x4){tag, __float_as_uint(zacc[0]), tag, __float_as_uint(zacc[0])};
      if (l < (cg == 2 ? 32 : 16)) __builtin_amdgcn_raw_buffer_store_b128(cw, rw, l * 16 + 2048, so, AUX_DEV);
    }
    u32x4 sel[2];
    {
      const u32 one = (c & 1) ? 0x3f800000u : 0x00003f80u;
#pragma unroll
      for (int hf = 0; hf < 2; ++hf)
#pragma unroll
        for (int e = 0; e < 4; ++e) sel[hf][e] = (g == 2 * hf + (c >> 3) && ((c & 7) >> 1) == e) ? one : 0u;
    }
    f32x4 own[2][2];
#pragma unroll
    for (int hf = 0; hf < 2; ++hf) {
      own[0][hf] = mfma32(sel[hf], p0, (f32x4){0, 0, 0, 0});
      own[1][hf] = mfma32(sel[hf], p1, (f32x4){0, 0, 0, 0});
    }
    lds_wait<1>(cg == 2 ? CNT(C_QMID) : CNT(C_QLATE), 8);
    const u32 want = tag ^ mk0[0] ^ mk0[2] ^ mk1[0] ^ mk1[2];
    f32x4 d[2][2];
    u32x2 za, zb;
    for (int spin = 0; spin < (1 << 18); ++spin) {
      u32x4 x[7][2];
      u32 xc[7];
#pragma unroll
      for (int i = 0; i < 7; ++i) {
        const u32 pb = ((j + 1 + i) & 7) * PART_BYTES + cg * CG_BYTES;
        x[i][0] = __builtin_amdgcn_raw_buffer_load_b128(rw, l * 16, pb, AUX_DEV);
        x[i][1] = __builtin_amdgcn_raw_buffer_load_b128(rw, l * 16 + 1024, pb, AUX_DEV);
        xc[i] = __builtin_amdgcn_raw_buffer_load_b32(rw, l * 4 + 2048, pb, AUX_DEV);
      }
      const u32 zo = cg == 2 ? g * PART_BYTES + c * 16 : 0x80000000u;
      za = __builtin_amdgcn_raw_buffer_load_b64(rw, zo, 3 * CG_BYTES, AUX_DEV);
      zb = __builtin_amdgcn_raw_buffer_load_b64(rw, zo, 3 * CG_BYTES + 4 * PART_BYTES, AUX_DEV);
      __builtin_amdgcn_sched_barrier(0);
      u32 bad = 0;
#pragma unroll
      for (int s2 = 0; s2 < 2; ++s2)
#pragma unroll
        for (int hf = 0; hf < 2; ++hf) d[s2][hf] = own[s2][hf];
#pragma unroll
      for (int i = 0; i < 7; ++i) {
        x[i][0] ^= mk0;
        x[i][1] ^= mk1;
#pragma unroll
        for (int s2 = 0; s2 < 2; ++s2)
#pragma unroll
          for (int hf = 0; hf < 2; ++hf) d[s2][hf] = mfma32(sel[hf], x[i][s2], d[s2][hf]);
        bad |= xor9(x[i][0], x[i][1], xc[i]) ^ want;
      }
      if (cg == 2) bad |= (za[0] ^ tag) | (zb[0] ^ tag);
      if (__all(bad == 0)) break;
      __builtin_amdgcn_s_sleep(8);
      asm volatile("" ::: "memory");
    }
#pragma unroll
    for (int s2 = 0; s2 < 2; ++s2)
#pragma unroll
      for (int hf = 0; hf < 2; ++hf)
        sm.kv[4 * (64 * cg + 4 * c + 2 * s2 + (g & 1)) + 2 * hf + (g >> 1) + 16 * cg + c] =
            (u32x2){pk_bf16(d[s2][hf][0], d[s2][hf][1]), pk_bf16(d[s2][hf][2], d[s2][hf][3])};
    if (cg == 2) {
      const f32x4 zs = __builtin_amdgcn_mfma_f32_16x16x4f32(1.0f, __uint_as_float(za[1]) + __uint_as_float(zb[1]),
                                                            (f32x4){0, 0, 0, 0}, 0, 0, 0);
      if (l < 16) ((float*)sm.z)[l] = lo_f(pk_bf16(zs[0], 0.f));
    }
    lds_signal(cg == 2 ? CNT(C_KV2) : CNT(C_KV01));
    if (cg == 2 && j == 0) {
      lds_wait<1>(CNT(C_KV01), 2);
      if (l == 0) __builtin_amdgcn_raw_buffer_store_b32(tag, rw, 0, EPOCH_OFF, AUX_DEV);
    }
  } else {
    const int wa = w - 3;
    const u32 row0 = rowb + wa * 32;
    const u32 off_frag = (row0 + (c >> 1)) * 384 + (2 * g + (c & 1)) * 16;
    auto rk = __builtin_amdgcn_make_buffer_rsrc((void*)K, 0, TBYTES, 0x00020000);
    auto rq = __builtin_amdgcn_make_buffer_rsrc((void*)Q, 0, TBYTES, 0x00020000);
    auto ro = __builtin_amdgcn_make_buffer_rsrc((void*)O, 0, TBYTES, 0x00020000);
    u32x4 om[3][2], xf[12];
    u32 cpf = 0;
    if (wa == 4) {
      u64 pc;
      asm volatile("s_getpc_b64 %0" : "=s"(pc));
      const u32 off = (((b >> 3) & 3) * 64 + l) * 64;
      cpf = *(const GAS u32*)(pc + (off < CODELEN ? off : 0));
    }
    __builtin_amdgcn_sched_barrier(0);
    float ssk[4] = {0.f, 0.f, 0.f, 0.f};
#pragma unroll
    for (int n = 0; n < 12 + WK; ++n) {
      if (n >= WK) asm volatile("" : "+v"(xf[n - WK]));
      __builtin_amdgcn_sched_barrier(0);
      if (n < 12) xf[n] = __builtin_amdgcn_raw_buffer_load_b128(rk, off_frag + (n % 3) * 128, (n / 3) * 3072, AUX_NT);
      if (n == 11) __builtin_amdgcn_s_barrier();
      __builtin_amdgcn_sched_barrier(0);
      if (n >= WK) {
#pragma unroll
        for (int e = 0; e < 4; ++e) {
          xf[n - WK][e] = pk_bf16(lo_f(xf[n - WK][e]) * scale, hi_f(xf[n - WK][e]) * scale);
          ssk[(n - WK) / 3] = sumsq2(xf[n - WK][e], ssk[(n - WK) / 3]);
        }
        asm volatile("" : "+v"(xf[n - WK]), "+v"(ssk[(n - WK) / 3]));
      }
    }
    asm volatile("" ::"v"(cpf) : "memory");
#pragma unroll
    for (int i = 0; i < 3; ++i)
#pragma unroll
      for (int p = 0; p < 2; ++p) om[i][p] = sm.om[2 * i + p][l];
    {
      u32x4 phiT;
#pragma unroll
      for (int a = 0; a < 4; ++a) {
        f32x4 d0 = {0, 0, 0, 0}, d1 = {0, 0, 0, 0};
#pragma unroll
        for (int i = 0; i < 3; ++i) {
          d0 = mfma32(xf[3 * a + i], om[i][0], d0);
          d1 = mfma32(xf[3 * a + i], om[i][1], d1);
        }
        const f32x4 sq = __builtin_amdgcn_mfma_f32_16x16x4f32(ssk[a], 1.0f, (f32x4){0, 0, 0, 0}, 0, 0, 0);
        phiT[a] = phi2_bf16(d0[0] + d1[1], sq[0] + sq[1], d0[2] + d1[3], sq[2] + sq[3]);
        __builtin_amdgcn_sched_barrier(0);
        __builtin_amdgcn_s_sleep(6);
        __builtin_amdgcn_sched_barrier(0);
      }
      sm.phik[wa][l] = phiT;
    }
    lds_signal(CNT(C_PHIK));
    lds_wait<1>(CNT(C_VOUT), 3);
    if (!(__builtin_amdgcn_s_getreg(20) & 1)) __builtin_amdgcn_s_sleep(16);
    __builtin_amdgcn_sched_barrier(0);
#pragma unroll
    for (int n = 0; n < WQ; ++n) {
      xf[n] = __builtin_amdgcn_raw_buffer_load_b128(rq, off_frag + (n % 3) * 128, (n / 3) * 3072, AUX_NT);
      __builtin_amdgcn_sched_barrier(0);
    }
    const bool odd = c & 1;
    const u32 off_row = (row0 + 2 * g) * 384;
    u32x2 phq[2], kv2[4];
    f32x4 zf, ph[2];
    auto part2 = [&](int u) {
      const float dp = fmaf(ph[u][0], zf[0], ph[u][1] * zf[1]) + fmaf(ph[u][2], zf[2], ph[u][3] * zf[3]);
      const f32x4 den = __builtin_amdgcn_mfma_f32_16x16x4f32(1.0f, dp, (f32x4){1e-6f, 1e-6f, 1e-6f, 1e-6f}, 0, 0, 0);
      const float inv = __builtin_amdgcn_rcpf(den[0]);
      phq[u] = (u32x2){pk_bf16(ph[u][0] * inv, ph[u][1] * inv), pk_bf16(ph[u][2] * inv, ph[u][3] * inv)};
      f32x4 num[4];
#pragma unroll
      for (int t = 0; t < 4; ++t) num[t] = mfma16(phq[u], kv2[t], (f32x4){0, 0, 0, 0});
#pragma unroll
      for (int r = 0; r < 4; ++r)
        __builtin_amdgcn_raw_buffer_store_b64((u32x2){pk_bf16(num[0][r], num[1][r]), pk_bf16(num[2][r], num[3][r])}, ro,
                                              off_row + 8 * c + 256 + (r >> 1) * 384, (16 * u + 8 * (r & 1)) * 384,
                                              AUX_PLAIN);
    };
#pragma unroll
    for (int u = 0; u < 2; ++u) {
      float ss[2];
      f32x4 d[2][2];
#pragma unroll
      for (int a = 0; a < 2; ++a) {
        ss[a] = 0.f;
        d[a][0] = (f32x4){0, 0, 0, 0};
        d[a][1] = (f32x4){0, 0, 0, 0};
#pragma unroll
        for (int i = 0; i < 3; ++i) {
          const int n = 6 * u + 3 * a + i;
          if (n + WQ < 12) {
            asm volatile("" : "+v"(xf[n]));
            __builtin_amdgcn_sched_barrier(0);
            xf[n + WQ] = __builtin_amdgcn_raw_buffer_load_b128(rq, off_frag + ((n + WQ) % 3) * 128,
                                                               ((n + WQ) / 3) * 3072, AUX_NT);
            __builtin_amdgcn_sched_barrier(0);
          }
          if (n == QMID) lds_add(CNT(C_QMID));
          if (n == QLATE) lds_add(CNT(C_QLATE));
          if (n == QEARLY) {
            lds_wait<0>(CNT(C_KV2), 1);
            zf = sm.z[g];
#pragma unroll
            for (int t = 0; t < 4; ++t) kv2[t] = sm.kv[4 * (128 + 4 * c + t) + g + 32 + c];
            lds_wait<0>(CNT(C_QLATE), 8);
            part2(0);
            __builtin_amdgcn_sched_barrier(0);
          }
#pragma unroll
          for (int e = 0; e < 4; ++e) ss[a] = sumsq2(xf[n][e], ss[a]);
          d[a][0] = mfma32(om[i][0], xf[n], d[a][0]);
          d[a][1] = mfma32(om[i][1], xf[n], d[a][1]);
        }
      }
      const float st = (odd ? ss[1] : ss[0]) + swap1(odd ? ss[0] : ss[1]);
      const f32x4 sq = __builtin_amdgcn_mfma_f32_16x16x4f32(1.0f, st, (f32x4){0, 0, 0, 0}, 0, 0, 0);
      const float sl = scale * 1.44269504f, hq = sq[0] * (-0.5f * scale * sl);
#pragma unroll
      for (int r = 0; r < 4; ++r) {
        const float pr = (odd ? d[1][1][r] : d[0][0][r]) + swap1(odd ? d[0][1][r] : d[1][0][r]);
        ph[u][r] = fmaf(__builtin_amdgcn_exp2f(fmaf(pr, sl, hq)), 0.25f, 1e-6f);
      }
      if (u == 0) asm volatile("" : "+v"(ph[0]));
    }
    part2(1);
    lds_wait<0>(CNT(C_KV01), 2);
    u32x2 kvf[8];
#pragma unroll
    for (int t = 0; t < 8; ++t) kvf[t] = sm.kv[4 * (8 * c + t) + g + 2 * c + (t >> 2)];
#pragma unroll
    for (int u = 0; u < 2; ++u) {
      f32x4 num[8];
#pragma unroll
      for (int t = 0; t < 8; ++t) num[t] = mfma16(phq[u], kvf[t], (f32x4){0, 0, 0, 0});
#pragma unroll
      for (int r = 0; r < 4; ++r)
        __builtin_amdgcn_raw_buffer_store_b128((u32x4){pk_bf16(num[0][r], num[1][r]), pk_bf16(num[2][r], num[3][r]),
                                                       pk_bf16(num[4][r], num[5][r]), pk_bf16(num[6][r], num[7][r])},
                                               ro, off_row + 16 * c + (r >> 1) * 384, (16 * u + 8 * (r & 1)) * 384, AUX_NT);
    }
  }
}

extern "C" void kernel_launch(void* const* d_in, const int* in_sizes, int n_in, void* d_out, int out_size,
                              void* d_ws, size_t ws_size, hipStream_t stream) {
  const float scale = 0.26864248f;
  rfa_fused<<<H * NCH, 704, 0, stream>>>((const u32*)d_in[0], (const u32*)d_in[1], (const u32*)d_in[2],
                                         (const u32*)d_in[3], (u32*)d_out, (u32*)d_ws, scale);
}
```

```cpp
#include <hip/hip_runtime.h>
#include <stdint.h>

typedef unsigned u32;
typedef unsigned long long u64;
typedef u32 u32x4 __attribute__((ext_vector_type(4)));
typedef u32 u32x2 __attribute__((ext_vector_type(2)));
typedef float f32x4 __attribute__((ext_vector_type(4)));
typedef float f32x2 __attribute__((ext_vector_type(2)));
typedef __bf16 bf16x8 __attribute__((ext_vector_type(8)));
typedef __bf16 bf16x2 __attribute__((ext_vector_type(2)));
typedef short s16x4 __attribute__((ext_vector_type(4)));
#define LAS __attribute__((address_space(3)))
#define GAS __attribute__((address_space(1)))

constexpr int H = 32, S = 2048, D = 192, M = 16;
constexpr int CH = 256;
constexpr int NCH = S / CH;
constexpr u32 TBYTES = H * S * D * 2;
constexpr u32 CG_BYTES = 2304;
constexpr u32 PART_BYTES = 3 * CG_BYTES + 256;
constexpr u32 EPOCH_OFF = NCH * PART_BYTES;
constexpr u32 HEAD_BYTES = EPOCH_OFF + 256;
constexpr int AUX_NT = 2, AUX_DEV = 16, AUX_PLAIN = 0;
constexpr int WK = 4, WV = 18, WQ = 3;
constexpr int QMID = 3, QLATE = 11 - WQ, QEARLY = 9;
constexpr u32 CODELEN = 10688;

__device__ __forceinline__ float lo_f(u32 x) { return __uint_as_float(x << 16); }
__device__ __forceinline__ float hi_f(u32 x) { return __uint_as_float(x & 0xffff0000u); }
__device__ __forceinline__ u32 pk_bf16(float a, float b) {
  f32x2 v = {a, b};
  bf16x2 r = __builtin_convertvector(v, bf16x2);
  return __builtin_bit_cast(u32, r);
}
__device__ __forceinline__ f32x4 mfma32(u32x4 a, u32x4 b, f32x4 c) {
  return __builtin_amdgcn_mfma_f32_16x16x32_bf16(__builtin_bit_cast(bf16x8, a), __builtin_bit_cast(bf16x8, b), c, 0, 0, 0);
}
__device__ __forceinline__ f32x4 mfma16(u32x2 a, u32x2 b, f32x4 c) {
  return __builtin_amdgcn_mfma_f32_16x16x16bf16_1k(__builtin_bit_cast(s16x4, a), __builtin_bit_cast(s16x4, b), c, 0, 0, 0);
}
__device__ __forceinline__ float sumsq2(u32 p, float acc) {
  return __builtin_amdgcn_fdot2_f32_bf16(__builtin_bit_cast(bf16x2, p), __builtin_bit_cast(bf16x2, p), acc, false);
}
__device__ __forceinline__ u32 phi2_bf16(float p0, float s0, float p1, float s1) {
  const u32 pp = pk_bf16(p0, p1), sp = pk_bf16(s0, s1);
  const u32 tp = pk_bf16(fmaf(lo_f(sp), -0.5f, lo_f(pp)), fmaf(hi_f(sp), -0.5f, hi_f(pp)));
  const u32 ep = pk_bf16(__expf(lo_f(tp)), __expf(hi_f(tp)));
  return pk_bf16(fmaf(lo_f(ep), 0.25f, 1e-6f), fmaf(hi_f(ep), 0.25f, 1e-6f));
}
__device__ __forceinline__ float swap1(float x) {
  return __builtin_bit_cast(float, __builtin_amdgcn_update_dpp(0, __builtin_bit_cast(int, x), 0xB1, 0xf, 0xf, true));
}
__device__ __forceinline__ void lds_add(LAS u32* p) {
  u64 sv;
  asm volatile("s_mov_b64 %0, exec\n\ts_mov_b64 exec, 1\n\tds_add_u32 %1, %2\n\ts_mov_b64 exec, %0"
               : "=&s"(sv) : "v"((u32)(size_t)p), "v"(1u) : "memory");
}
__device__ __forceinline__ void lds_signal(LAS u32* p) {
  asm volatile("s_waitcnt lgkmcnt(0)" ::: "memory");
  lds_add(p);
}
template <int SLEEP>
__device__ __forceinline__ void lds_wait(LAS u32* p, u32 want) {
  while (__builtin_amdgcn_readfirstlane(__hip_atomic_load(p, __ATOMIC_RELAXED, __HIP_MEMORY_SCOPE_WORKGROUP)) < want)
    if (SLEEP) __builtin_amdgcn_s_sleep(SLEEP);
  asm volatile("" ::: "memory");
}
__device__ __forceinline__ u32 xor3(u32 a, u32 b, u32 c) { return __builtin_amdgcn_bitop3_b32(a, b, c, 0x96); }
__device__ __forceinline__ u32 xor9(u32x4 a, u32x4 b, u32 c) {
  return xor3(xor3(a[0], a[1], a[2]), xor3(a[3], b[0], b[1]), xor3(b[2], b[3], c));
}

struct __attribute__((aligned(16))) Smem {
  u32x4 om[6][64];
  u32x4 phik[8][64];
  u32x2 kv[48 * 17];
  f32x4 z[4];
  u32 tag;
  u32 pad[3];
  u32 cnt[8];
};
enum { C_VOUT, C_PHIK, C_QLATE, C_QMID, C_KV01, C_KV2 };
#define CNT(i) ((LAS u32*)&sm.cnt[i])

__global__ __launch_bounds__(704, 3) void rfa_fused(const u32* __restrict__ Q, const u32* __restrict__ K,
                                                    const u32* __restrict__ V, const u32* __restrict__ Om,
                                                    u32* __restrict__ O, u32* ws, float scale) {
  __shared__ Smem sm;
  const int b = blockIdx.x;
  const int h = (b & 7) * 4 + (b >> 6), j = (b >> 3) & 7;
  const int w = __builtin_amdgcn_readfirstlane(threadIdx.x >> 6);
  const int l = threadIdx.x & 63, g = l >> 4, c = l & 15;
  const u32 rowb = h * S + j * CH;
  if (w < 3) {
    const int cg = w;
    auto rw = __builtin_amdgcn_make_buffer_rsrc((char*)ws + h * HEAD_BYTES, 0, HEAD_BYTES, 0x00020000);
    if (cg == 0) {
      auto rom = __builtin_amdgcn_make_buffer_rsrc((void*)Om, 0, M * D * 2, 0x00020000);
#pragma unroll
      for (int i = 0; i < 6; ++i)
        sm.om[i][l] = __builtin_amdgcn_raw_buffer_load_b128(rom, c * 384 + 2 * g * 16, (i >> 1) * 128 + (i & 1) * 16, 0);
    } else if (cg == 1) {
      const char* base = g == 0 ? (const char*)Q : g == 1 ? (const char*)O : (const char*)V;
      const u32 pf = __builtin_nontemporal_load((const u32*)(base + rowb * 384 + (c < 2 ? c * 98300 : 49152)));
      if (l < 8) sm.cnt[l] = 0;
      asm volatile("" ::"v"(pf));
    } else {
      sm.tag = __builtin_amdgcn_raw_buffer_load_b32(rw, 0, EPOCH_OFF, AUX_DEV) + 1u;
    }
    asm volatile("s_waitcnt lgkmcnt(0)" ::: "memory");
    __builtin_amdgcn_s_barrier();
    asm volatile("" ::: "memory");
    auto rv = __builtin_amdgcn_make_buffer_rsrc((void*)V, 0, TBYTES, 0x00020000);
    const u32 voff = (rowb + 2 * g) * 384 + cg * 128 + 8 * c;
    __builtin_amdgcn_sched_barrier(0);
    u32x2 vf[8][4][2];
#pragma unroll
    for (int s = 0; s < 8; ++s)
#pragma unroll
      for (int a = 0; a < 4; ++a) {
        if (s * 8 + a * 2 >= WV) {
          __builtin_amdgcn_sched_barrier(0);
          asm volatile("s_waitcnt vmcnt(%0)" ::"i"(WV - 2) : "memory");
          __builtin_amdgcn_sched_barrier(0);
        }
#pragma unroll
        for (int bb = 0; bb < 2; ++bb)
          vf[s][a][bb] = __builtin_amdgcn_raw_buffer_load_b64(rv, voff + bb * 384, (s * 4 + a) * 3072, AUX_NT);
      }
    __builtin_amdgcn_sched_barrier(0);
    lds_add(CNT(C_VOUT));
    lds_wait<1>(CNT(C_PHIK), 8);
    const u32 tag = __builtin_amdgcn_readfirstlane(sm.tag);
    const u32x4 mk0 = {(tag + 0x3c6ef372u) * 0x9e3779b1u, 0u, (tag + 0x78dde6e4u) * 0x85ebca77u, 0u};
    const u32x4 mk1 = {(tag + 0xb54cda56u) * 0xc2b2ae3du, 0u, (tag + 0xf1bbcdc8u) * 0x27d4eb2fu, 0u};
    f32x4 acc[4], zacc = {0, 0, 0, 0};
#pragma unroll
    for (int t = 0; t < 4; ++t) acc[t] = (f32x4){0, 0, 0, 0};
    const u32x4 ones = {0x3f803f80u, 0x3f803f80u, 0x3f803f80u, 0x3f803f80u};
#pragma unroll
    for (int s = 0; s < 8; ++s) {
      const u32x4 phiT = sm.phik[s][l];
      zacc = mfma32(ones, phiT, zacc);
#pragma unroll
      for (int t = 0; t < 4; ++t) {
        u32x4 bf;
        const u32 sel = (t & 1) ? 0x07060302u : 0x05040100u;
#pragma unroll
        for (int a = 0; a < 4; ++a) bf[a] = __builtin_amdgcn_perm(vf[s][a][1][t >> 1], vf[s][a][0][t >> 1], sel);
        acc[t] = mfma32(phiT, bf, acc[t]);
      }
    }
    const u32x4 p0 = {pk_bf16(acc[0][0], acc[0][1]), pk_bf16(acc[0][2], acc[0][3]), pk_bf16(acc[1][0], acc[1][1]),
                      pk_bf16(acc[1][2], acc[1][3])};
    const u32x4 p1 = {pk_bf16(acc[2][0], acc[2][1]), pk_bf16(acc[2][2], acc[2][3]), pk_bf16(acc[3][0], acc[3][1]),
                      pk_bf16(acc[3][2], acc[3][3])};
    {
      const u32 chk = xor9(p0 ^ mk0, p1 ^ mk1, tag);
      const u32 so = j * PART_BYTES + cg * CG_BYTES;
      __builtin_amdgcn_raw_buffer_store_b128(p0 ^ mk0, rw, l * 16, so, AUX_DEV);
      __builtin_amdgcn_raw_buffer_store_b128(p1 ^ mk1, rw, l * 16 + 1024, so, AUX_DEV);
      u32x4 cw;
#pragma unroll
      for (int e = 0; e < 4; ++e) cw[e] = __builtin_amdgcn_ds_bpermute(((4 * l + e) & 63) * 4, chk);
      if (g == 1) cw = (u32x4){tag, __float_as_uint(zacc[0]), tag, __float_as_uint(zacc[0])};
      if (l < (cg == 2 ? 32 : 16)) __builtin_amdgcn_raw_buffer_store_b128(cw, rw, l * 16 + 2048, so, AUX_DEV);
    }
    u32x4 sel[2];
    {
      const u32 one = (c & 1) ? 0x3f800000u : 0x00003f80u;
#pragma unroll
      for (int hf = 0; hf < 2; ++hf)
#pragma unroll
        for (int e = 0; e < 4; ++e) sel[hf][e] = (g == 2 * hf + (c >> 3) && ((c & 7) >> 1) == e) ? one : 0u;
    }
    f32x4 own[2][2];
#pragma unroll
    for (int hf = 0; hf < 2; ++hf) {
      own[0][hf] = mfma32(sel[hf], p0, (f32x4){0, 0, 0, 0});
      own[1][hf] = mfma32(sel[hf], p1, (f32x4){0, 0, 0, 0});
    }
    lds_wait<1>(cg == 2 ? CNT(C_QMID) : CNT(C_QLATE), 8);
    const u32 want = tag ^ mk0[0] ^ mk0[2] ^ mk1[0] ^ mk1[2];
    f32x4 d[2][2];
    u32x2 za, zb;
    for (int spin = 0; spin < (1 << 18); ++spin) {
      u32x4 x[7][2];
      u32 xc[7];
#pragma unroll
      for (int i = 0; i < 7; ++i) {
        const u32 pb = ((j + 1 + i) & 7) * PART_BYTES + cg * CG_BYTES;
        x[i][0] = __builtin_amdgcn_raw_buffer_load_b128(rw, l * 16, pb, AUX_DEV);
        x[i][1] = __builtin_amdgcn_raw_buffer_load_b128(rw, l * 16 + 1024, pb, AUX_DEV);
        xc[i] = __builtin_amdgcn_raw_buffer_load_b32(rw, l * 4 + 2048, pb, AUX_DEV);
      }
      const u32 zo = cg == 2 ? g * PART_BYTES + c * 16 : 0x80000000u;
      za = __builtin_amdgcn_raw_buffer_load_b64(rw, zo, 3 * CG_BYTES, AUX_DEV);
      zb = __builtin_amdgcn_raw_buffer_load_b64(rw, zo, 3 * CG_BYTES + 4 * PART_BYTES, AUX_DEV);
      __builtin_amdgcn_sched_barrier(0);
      u32 bad = 0;
#pragma unroll
      for (int s2 = 0; s2 < 2; ++s2)
#pragma unroll
        for (int hf = 0; hf < 2; ++hf) d[s2][hf] = own[s2][hf];
#pragma unroll
      for (int i = 0; i < 7; ++i) {
        x[i][0] ^= mk0;
        x[i][1] ^= mk1;
#pragma unroll
        for (int s2 = 0; s2 < 2; ++s2)
#pragma unroll
          for (int hf = 0; hf < 2; ++hf) d[s2][hf] = mfma32(sel[hf], x[i][s2], d[s2][hf]);
        bad |= xor9(x[i][0], x[i][1], xc[i]) ^ want;
      }
      if (cg == 2) bad |= (za[0] ^ tag) | (zb[0] ^ tag);
      if (__all(bad == 0)) break;
      __builtin_amdgcn_s_sleep(8);
      asm volatile("" ::: "memory");
    }
#pragma unroll
    for (int s2 = 0; s2 < 2; ++s2)
#pragma unroll
      for (int hf = 0; hf < 2; ++hf)
        sm.kv[4 * (64 * cg + 4 * c + 2 * s2 + (g & 1)) + 2 * hf + (g >> 1) + 16 * cg + c] =
            (u32x2){pk_bf16(d[s2][hf][0], d[s2][hf][1]), pk_bf16(d[s2][hf][2], d[s2][hf][3])};
    if (cg == 2) {
      const f32x4 zs = __builtin_amdgcn_mfma_f32_16x16x4f32(1.0f, __uint_as_float(za[1]) + __uint_as_float(zb[1]),
                                                            (f32x4){0, 0, 0, 0}, 0, 0, 0);
      if (l < 16) ((float*)sm.z)[l] = lo_f(pk_bf16(zs[0], 0.f));
    }
    lds_signal(cg == 2 ? CNT(C_KV2) : CNT(C_KV01));
    if (cg == 2 && j == 0) {
      lds_wait<1>(CNT(C_KV01), 2);
      if (l == 0) __builtin_amdgcn_raw_buffer_store_b32(tag, rw, 0, EPOCH_OFF, AUX_DEV);
    }
  } else {
    const int wa = w - 3;
    const u32 row0 = rowb + wa * 32;
    const u32 off_frag = (row0 + (c >> 1)) * 384 + (2 * g + (c & 1)) * 16;
    auto rk = __builtin_amdgcn_make_buffer_rsrc((void*)K, 0, TBYTES, 0x00020000);
    auto rq = __builtin_amdgcn_make_buffer_rsrc((void*)Q, 0, TBYTES, 0x00020000);
    auto ro = __builtin_amdgcn_make_buffer_rsrc((void*)O, 0, TBYTES, 0x00020000);
    u32x4 om[3][2], xf[12];
    u32 cpf = 0;
    if (wa == 4) {
      u64 pc;
      asm volatile("s_getpc_b64 %0" : "=s"(pc));
      const u32 off = (((b >> 3) & 3) * 64 + l) * 64;
      cpf = *(const GAS u32*)(pc + (off < CODELEN ? off : 0));
    }
    __builtin_amdgcn_sched_barrier(0);
    float ssk[4] = {0.f, 0.f, 0.f, 0.f};
#pragma unroll
    for (int n = 0; n < 12 + WK; ++n) {
      if (n >= WK) asm volatile("" : "+v"(xf[n - WK]));
      __builtin_amdgcn_sched_barrier(0);
      if (n >= WK - 1 && n < 12)
        asm volatile("s_cmp_gt_u32 %1, 3\n\ts_cbranch_scc1 1f\n\ts_waitcnt vmcnt(%0)\n1:" ::"i"(WK - 2), "s"(wa) : "scc", "memory");
      if (n < 12) xf[n] = __builtin_amdgcn_raw_buffer_load_b128(rk, off_frag + (n % 3) * 128, (n / 3) * 3072, AUX_NT);
      if (n == 11) __builtin_amdgcn_s_barrier();
      __builtin_amdgcn_sched_barrier(0);
      if (n >= WK) {
#pragma unroll
        for (int e = 0; e < 4; ++e) {
          xf[n - WK][e] = pk_bf16(lo_f(xf[n - WK][e]) * scale, hi_f(xf[n - WK][e]) * scale);
          ssk[(n - WK) / 3] = sumsq2(xf[n - WK][e], ssk[(n - WK) / 3]);
        }
        asm volatile("" : "+v"(xf[n - WK]), "+v"(ssk[(n - WK) / 3]));
      }
    }
    asm volatile("" ::"v"(cpf) : "memory");
#pragma unroll
    for (int i = 0; i < 3; ++i)
#pragma unroll
      for (int p = 0; p < 2; ++p) om[i][p] = sm.om[2 * i + p][l];
    {
      u32x4 phiT;
#pragma unroll
      for (int a = 0; a < 4; ++a) {
        f32x4 d0 = {0, 0, 0, 0}, d1 = {0, 0, 0, 0};
#pragma unroll
        for (int i = 0; i < 3; ++i) {
          d0 = mfma32(xf[3 * a + i], om[i][0], d0);
          d1 = mfma32(xf[3 * a + i], om[i][1], d1);
        }
        const f32x4 sq = __builtin_amdgcn_mfma_f32_16x16x4f32(ssk[a], 1.0f, (f32x4){0, 0, 0, 0}, 0, 0, 0);
        phiT[a] = phi2_bf16(d0[0] + d1[1], sq[0] + sq[1], d0[2] + d1[3], sq[2] + sq[3]);
        __builtin_amdgcn_sched_barrier(0);
        __builtin_amdgcn_s_sleep(6);
        __builtin_amdgcn_sched_barrier(0);
      }
      sm.phik[wa][l] = phiT;
    }
    lds_signal(CNT(C_PHIK));
    lds_wait<1>(CNT(C_VOUT), 3);
    if (!(__builtin_amdgcn_s_getreg(20) & 1)) __builtin_amdgcn_s_sleep(18);
    __builtin_amdgcn_sched_barrier(0);
#pragma unroll
    for (int n = 0; n < WQ; ++n) {
      xf[n] = __builtin_amdgcn_raw_buffer_load_b128(rq, off_frag + (n % 3) * 128, (n / 3) * 3072, AUX_NT);
      __builtin_amdgcn_sched_barrier(0);
    }
    const bool odd = c & 1;
    const u32 off_row = (row0 + 2 * g) * 384;
    u32x2 phq[2], kv2[4];
    f32x4 zf, ph[2];
    auto part2 = [&](int u) {
      const float dp = fmaf(ph[u][0], zf[0], ph[u][1] * zf[1]) + fmaf(ph[u][2], zf[2], ph[u][3] * zf[3]);
      const f32x4 den = __builtin_amdgcn_mfma_f32_16x16x4f32(1.0f, dp, (f32x4){1e-6f, 1e-6f, 1e-6f, 1e-6f}, 0, 0, 0);
      const float inv = __builtin_amdgcn_rcpf(den[0]);
      phq[u] = (u32x2){pk_bf16(ph[u][0] * inv, ph[u][1] * inv), pk_bf16(ph[u][2] * inv, ph[u][3] * inv)};
      f32x4 num[4];
#pragma unroll
      for (int t = 0; t < 4; ++t) num[t] = mfma16(phq[u], kv2[t], (f32x4){0, 0, 0, 0});
#pragma unroll
      for (int r = 0; r < 4; ++r)
        __builtin_amdgcn_raw_buffer_store_b64((u32x2){pk_bf16(num[0][r], num[1][r]), pk_bf16(num[2][r], num[3][r])}, ro,
                                              off_row + 8 * c + 256 + (r >> 1) * 384, (16 * u + 8 * (r & 1)) * 384,
                                              AUX_PLAIN);
    };
#pragma unroll
    for (int u = 0; u < 2; ++u) {
      float ss[2];
      f32x4 d[2][2];
#pragma unroll
      for (int a = 0; a < 2; ++a) {
        ss[a] = 0.f;
        d[a][0] = (f32x4){0, 0, 0, 0};
        d[a][1] = (f32x4){0, 0, 0, 0};
#pragma unroll
        for (int i = 0; i < 3; ++i) {
          const int n = 6 * u + 3 * a + i;
          if (n + WQ < 12) {
            asm volatile("" : "+v"(xf[n]));
            __builtin_amdgcn_sched_barrier(0);
            xf[n + WQ] = __builtin_amdgcn_raw_buffer_load_b128(rq, off_frag + ((n + WQ) % 3) * 128,
                                                               ((n + WQ) / 3) * 3072, AUX_NT);
            __builtin_amdgcn_sched_barrier(0);
          }
          if (n == QMID) lds_add(CNT(C_QMID));
          if (n == QLATE) lds_add(CNT(C_QLATE));
          if (n == QEARLY) {
            lds_wait<0>(CNT(C_KV2), 1);
            zf = sm.z[g];
#pragma unroll
            for (int t = 0; t < 4; ++t) kv2[t] = sm.kv[4 * (128 + 4 * c + t) + g + 32 + c];
            lds_wait<0>(CNT(C_QLATE), 8);
            part2(0);
            __builtin_amdgcn_sched_barrier(0);
          }
#pragma unroll
          for (int e = 0; e < 4; ++e) ss[a] = sumsq2(xf[n][e], ss[a]);
          d[a][0] = mfma32(om[i][0], xf[n], d[a][0]);
          d[a][1] = mfma32(om[i][1], xf[n], d[a][1]);
        }
      }
      const float st = (odd ? ss[1] : ss[0]) + swap1(odd ? ss[0] : ss[1]);
      const f32x4 sq = __builtin_amdgcn_mfma_f32_16x16x4f32(1.0f, st, (f32x4){0, 0, 0, 0}, 0, 0, 0);
      const float sl = scale * 1.44269504f, hq = sq[0] * (-0.5f * scale * sl);
#pragma unroll
      for (int r = 0; r < 4; ++r) {
        const float pr = (odd ? d[1][1][r] : d[0][0][r]) + swap1(odd ? d[0][1][r] : d[1][0][r]);
        ph[u][r] = fmaf(__builtin_amdgcn_exp2f(fmaf(pr, sl, hq)), 0.25f, 1e-6f);
      }
      if (u == 0) asm volatile("" : "+v"(ph[0]));
    }
    part2(1);
    lds_wait<0>(CNT(C_KV01), 2);
    u32x2 kvf[8];
#pragma unroll
    for (int t = 0; t < 8; ++t) kvf[t] = sm.kv[4 * (8 * c + t) + g + 2 * c + (t >> 2)];
#pragma unroll
    for (int u = 0; u < 2; ++u) {
      f32x4 num[8];
#pragma unroll
      for (int t = 0; t < 8; ++t) num[t] = mfma16(phq[u], kvf[t], (f32x4){0, 0, 0, 0});
#pragma unroll
      for (int r = 0; r < 4; ++r)
        __builtin_amdgcn_raw_buffer_store_b128((u32x4){pk_bf16(num[0][r], num[1][r]), pk_bf16(num[2][r], num[3][r]),
                                                       pk_bf16(num[4][r], num[5][r]), pk_bf16(num[6][r], num[7][r])},
                                               ro, off_row + 16 * c + (r >> 1) * 384, (16 * u + 8 * (r & 1)) * 384, AUX_NT);
    }
  }
}

extern "C" void kernel_launch(void* const* d_in, const int* in_sizes, int n_in, void* d_out, int out_size,
                              void* d_ws, size_t ws_size, hipStream_t stream) {
  const float scale = 0.26864248f;
  rfa_fused<<<H * NCH, 704, 0, stream>>>((const u32*)d_in[0], (const u32*)d_in[1], (const u32*)d_in[2],
                                         (const u32*)d_in[3], (u32*)d_out, (u32*)d_ws, scale);
}
```

```cpp
#include <hip/hip_runtime.h>
#include <stdint.h>

typedef unsigned u32;
typedef unsigned long long u64;
typedef u32 u32x4 __attribute__((ext_vector_type(4)));
typedef u32 u32x2 __attribute__((ext_vector_type(2)));
typedef float f32x4 __attribute__((ext_vector_type(4)));
typedef float f32x2 __attribute__((ext_vector_type(2)));
typedef __bf16 bf16x8 __attribute__((ext_vector_type(8)));
typedef __bf16 bf16x2 __attribute__((ext_vector_type(2)));
typedef short s16x4 __attribute__((ext_vector_type(4)));
#define LAS __attribute__((address_space(3)))
#define GAS __attribute__((address_space(1)))

constexpr int H = 32, S = 2048, D = 192, M = 16;
constexpr int CH = 256;
constexpr int NCH = S / CH;
constexpr u32 TBYTES = H * S * D * 2;
constexpr u32 CG_BYTES = 2304;
constexpr u32 PART_BYTES = 3 * CG_BYTES + 256;
constexpr u32 EPOCH_OFF = NCH * PART_BYTES;
constexpr u32 HEAD_BYTES = EPOCH_OFF + 256;
constexpr int AUX_NT = 2, AUX_DEV = 16, AUX_PLAIN = 0;
constexpr int WK = 4, WV = 18, WQ = 3;
constexpr int QMID = 3, QLATE = 11 - WQ, QEARLY = 9;
constexpr u32 CODELEN = 10688;

__device__ __forceinline__ float lo_f(u32 x) { return __uint_as_float(x << 16); }
__device__ __forceinline__ float hi_f(u32 x) { return __uint_as_float(x & 0xffff0000u); }
__device__ __forceinline__ u32 pk_bf16(float a, float b) {
  f32x2 v = {a, b};
  bf16x2 r = __builtin_convertvector(v, bf16x2);
  return __builtin_bit_cast(u32, r);
}
__device__ __forceinline__ f32x4 mfma32(u32x4 a, u32x4 b, f32x4 c) {
  return __builtin_amdgcn_mfma_f32_16x16x32_bf16(__builtin_bit_cast(bf16x8, a), __builtin_bit_cast(bf16x8, b), c, 0, 0, 0);
}
__device__ __forceinline__ f32x4 mfma16(u32x2 a, u32x2 b, f32x4 c) {
  return __builtin_amdgcn_mfma_f32_16x16x16bf16_1k(__builtin_bit_cast(s16x4, a), __builtin_bit_cast(s16x4, b), c, 0, 0, 0);
}
__device__ __forceinline__ float sumsq2(u32 p, float acc) {
  return __builtin_amdgcn_fdot2_f32_bf16(__builtin_bit_cast(bf16x2, p), __builtin_bit_cast(bf16x2, p), acc, false);
}
__device__ __forceinline__ u32 phi2_bf16(float p0, float s0, float p1, float s1) {
  const u32 pp = pk_bf16(p0, p1), sp = pk_bf16(s0, s1);
  const u32 tp = pk_bf16(fmaf(lo_f(sp), -0.5f, lo_f(pp)), fmaf(hi_f(sp), -0.5f, hi_f(pp)));
  const u32 ep = pk_bf16(__expf(lo_f(tp)), __expf(hi_f(tp)));
  return pk_bf16(fmaf(lo_f(ep), 0.25f, 1e-6f), fmaf(hi_f(ep), 0.25f, 1e-6f));
}
__device__ __forceinline__ float swap1(float x) {
  return __builtin_bit_cast(float, __builtin_amdgcn_update_dpp(0, __builtin_bit_cast(int, x), 0xB1, 0xf, 0xf, true));
}
__device__ __forceinline__ void lds_add(LAS u32* p) {
  u64 sv;
  asm volatile("s_mov_b64 %0, exec\n\ts_mov_b64 exec, 1\n\tds_add_u32 %1, %2\n\ts_mov_b64 exec, %0"
               : "=&s"(sv) : "v"((u32)(size_t)p), "v"(1u) : "memory");
}
__device__ __forceinline__ void lds_signal(LAS u32* p) {
  asm volatile("s_waitcnt lgkmcnt(0)" ::: "memory");
  lds_add(p);
}
template <int SLEEP>
__device__ __forceinline__ void lds_wait(LAS u32* p, u32 want) {
  while (__builtin_amdgcn_readfirstlane(__hip_atomic_load(p, __ATOMIC_RELAXED, __HIP_MEMORY_SCOPE_WORKGROUP)) < want)
    if (SLEEP) __builtin_amdgcn_s_sleep(SLEEP);
  asm volatile("" ::: "memory");
}
__device__ __forceinline__ u32 xor3(u32 a, u32 b, u32 c) { return __builtin_amdgcn_bitop3_b32(a, b, c, 0x96); }
__device__ __forceinline__ u32 xor9(u32x4 a, u32x4 b, u32 c) {
  return xor3(xor3(a[0], a[1], a[2]), xor3(a[3], b[0], b[1]), xor3(b[2], b[3], c));
}

struct __attribute__((aligned(16))) Smem {
  u32x4 om[6][64];
  u32x4 phik[8][64];
  u32x2 kv[48 * 17];
  f32x4 z[4];
  u32 tag;
  u32 pad[3];
  u32 cnt[8];
};
enum { C_VOUT, C_PHIK, C_QLATE, C_QMID, C_KV01, C_KV2 };
#define CNT(i) ((LAS u32*)&sm.cnt[i])

__global__ __launch_bounds__(704, 3) void rfa_fused(const u32* __restrict__ Q, const u32* __restrict__ K,
                                                    const u32* __restrict__ V, const u32* __restrict__ Om,
                                                    u32* __restrict__ O, u32* ws, float scale) {
  __shared__ Smem sm;
  const int b = blockIdx.x;
  const int h = (b & 7) * 4 + (b >> 6), j = (b >> 3) & 7;
  const int w = __builtin_amdgcn_readfirstlane(threadIdx.x >> 6);
  const int l = threadIdx.x & 63, g = l >> 4, c = l & 15;
  const u32 rowb = h * S + j * CH;
  if (w < 3) {
    const int cg = w;
    auto rw = __builtin_amdgcn_make_buffer_rsrc((char*)ws + h * HEAD_BYTES, 0, HEAD_BYTES, 0x00020000);
    if (cg == 0) {
      auto rom = __builtin_amdgcn_make_buffer_rsrc((void*)Om, 0, M * D * 2, 0x00020000);
#pragma unroll
      for (int i = 0; i < 6; ++i)
        sm.om[i][l] = __builtin_amdgcn_raw_buffer_load_b128(rom, c * 384 + 2 * g * 16, (i >> 1) * 128 + (i & 1) * 16, 0);
    } else if (cg == 1) {
      const char* base = g == 0 ? (const char*)Q : g == 1 ? (const char*)O : (const char*)V;
      const u32 pf = __builtin_nontemporal_load((const u32*)(base + rowb * 384 + (c < 2 ? c * 98300 : 49152)));
      if (l < 8) sm.cnt[l] = 0;
      asm volatile("" ::"v"(pf));
    } else {
      sm.tag = __builtin_amdgcn_raw_buffer_load_b32(rw, 0, EPOCH_OFF, AUX_DEV) + 1u;
    }
    asm volatile("s_waitcnt lgkmcnt(0)" ::: "memory");
    __builtin_amdgcn_s_barrier();
    asm volatile("" ::: "memory");
    auto rv = __builtin_amdgcn_make_buffer_rsrc((void*)V, 0, TBYTES, 0x00020000);
    const u32 voff = (rowb + 2 * g) * 384 + cg * 128 + 8 * c;
    __builtin_amdgcn_sched_barrier(0);
    u32x2 vf[8][4][2];
#pragma unroll
    for (int s = 0; s < 8; ++s)
#pragma unroll
      for (int a = 0; a < 4; ++a) {
        if (s * 8 + a * 2 >= WV) {
          __builtin_amdgcn_sched_barrier(0);
          asm volatile("s_waitcnt vmcnt(%0)" ::"i"(WV - 2) : "memory");
          __builtin_amdgcn_sched_barrier(0);
        }
#pragma unroll
        for (int bb = 0; bb < 2; ++bb)
          vf[s][a][bb] = __builtin_amdgcn_raw_buffer_load_b64(rv, voff + bb * 384, (s * 4 + a) * 3072, AUX_NT);
      }
    __builtin_amdgcn_sched_barrier(0);
    lds_add(CNT(C_VOUT));
    lds_wait<1>(CNT(C_PHIK), 8);
    const u32 tag = __builtin_amdgcn_readfirstlane(sm.tag);
    const u32x4 mk0 = {(tag + 0x3c6ef372u) * 0x9e3779b1u, 0u, (tag + 0x78dde6e4u) * 0x85ebca77u, 0u};
    const u32x4 mk1 = {(tag + 0xb54cda56u) * 0xc2b2ae3du, 0u, (tag + 0xf1bbcdc8u) * 0x27d4eb2fu, 0u};
    f32x4 acc[4], zacc = {0, 0, 0, 0};
#pragma unroll
    for (int t = 0; t < 4; ++t) acc[t] = (f32x4){0, 0, 0, 0};
    const u32x4 ones = {0x3f803f80u, 0x3f803f80u, 0x3f803f80u, 0x3f803f80u};
#pragma unroll
    for (int s = 0; s < 8; ++s) {
      const u32x4 phiT = sm.phik[s][l];
      zacc = mfma32(ones, phiT, zacc);
#pragma unroll
      for (int t = 0; t < 4; ++t) {
        u32x4 bf;
        const u32 sel = (t & 1) ? 0x07060302u : 0x05040100u;
#pragma unroll
        for (int a = 0; a < 4; ++a) bf[a] = __builtin_amdgcn_perm(vf[s][a][1][t >> 1], vf[s][a][0][t >> 1], sel);
        acc[t] = mfma32(phiT, bf, acc[t]);
      }
    }
    const u32x4 p0 = {pk_bf16(acc[0][0], acc[0][1]), pk_bf16(acc[0][2], acc[0][3]), pk_bf16(acc[1][0], acc[1][1]),
                      pk_bf16(acc[1][2], acc[1][3])};
    const u32x4 p1 = {pk_bf16(acc[2][0], acc[2][1]), pk_bf16(acc[2][2], acc[2][3]), pk_bf16(acc[3][0], acc[3][1]),
                      pk_bf16(acc[3][2], acc[3][3])};
    {
      const u32 chk = xor9(p0 ^ mk0, p1 ^ mk1, tag);
      const u32 so = j * PART_BYTES + cg * CG_BYTES;
      __builtin_amdgcn_raw_buffer_store_b128(p0 ^ mk0, rw, l * 16, so, AUX_DEV);
      __builtin_amdgcn_raw_buffer_store_b128(p1 ^ mk1, rw, l * 16 + 1024, so, AUX_DEV);
      u32x4 cw;
#pragma unroll
      for (int e = 0; e < 4; ++e) cw[e] = __builtin_amdgcn_ds_bpermute(((4 * l + e) & 63) * 4, chk);
      if (g == 1) cw = (u32x4){tag, __float_as_uint(zacc[0]), tag, __float_as_uint(zacc[0])};
      if (l < (cg == 2 ? 32 : 16)) __builtin_amdgcn_raw_buffer_store_b128(cw, rw, l * 16 + 2048, so, AUX_DEV);
    }
    u32x4 sel[2];
    {
      const u32 one = (c & 1) ? 0x3f800000u : 0x00003f80u;
#pragma unroll
      for (int hf = 0; hf < 2; ++hf)
#pragma unroll
        for (int e = 0; e < 4; ++e) sel[hf][e] = (g == 2 * hf + (c >> 3) && ((c & 7) >> 1) == e) ? one : 0u;
    }
    f32x4 own[2][2];
#pragma unroll
    for (int hf = 0; hf < 2; ++hf) {
      own[0][hf] = mfma32(sel[hf], p0, (f32x4){0, 0, 0, 0});
      own[1][hf] = mfma32(sel[hf], p1, (f32x4){0, 0, 0, 0});
    }
    lds_wait<1>(cg == 2 ? CNT(C_QMID) : CNT(C_QLATE), 8);
    const u32 want = tag ^ mk0[0] ^ mk0[2] ^ mk1[0] ^ mk1[2];
    f32x4 d[2][2];
    u32x2 za, zb;
    for (int spin = 0; spin < (1 << 18); ++spin) {
      u32x4 x[7][2];
      u32 xc[7];
#pragma unroll
      for (int i = 0; i < 7; ++i) {
        const u32 pb = ((j + 1 + i) & 7) * PART_BYTES + cg * CG_BYTES;
        x[i][0] = __builtin_amdgcn_raw_buffer_load_b128(rw, l * 16, pb, AUX_DEV);
        x[i][1] = __builtin_amdgcn_raw_buffer_load_b128(rw, l * 16 + 1024, pb, AUX_DEV);
        xc[i] = __builtin_amdgcn_raw_buffer_load_b32(rw, l * 4 + 2048, pb, AUX_DEV);
        __builtin_amdgcn_sched_barrier(0);
      }
      const u32 zo = cg == 2 ? g * PART_BYTES + c * 16 : 0x80000000u;
      za = __builtin_amdgcn_raw_buffer_load_b64(rw, zo, 3 * CG_BYTES, AUX_DEV);
      zb = __builtin_amdgcn_raw_buffer_load_b64(rw, zo, 3 * CG_BYTES + 4 * PART_BYTES, AUX_DEV);
      __builtin_amdgcn_sched_barrier(0);
      u32 bad = 0;
#pragma unroll
      for (int s2 = 0; s2 < 2; ++s2)
#pragma unroll
        for (int hf = 0; hf < 2; ++hf) d[s2][hf] = own[s2][hf];
#pragma unroll
      for (int i = 0; i < 7; ++i) {
        x[i][0] ^= mk0;
        x[i][1] ^= mk1;
#pragma unroll
        for (int s2 = 0; s2 < 2; ++s2)
#pragma unroll
          for (int hf = 0; hf < 2; ++hf) d[s2][hf] = mfma32(sel[hf], x[i][s2], d[s2][hf]);
        bad |= xor9(x[i][0], x[i][1], xc[i]) ^ want;
        __builtin_amdgcn_sched_barrier(0);
      }
      if (cg == 2) bad |= (za[0] ^ tag) | (zb[0] ^ tag);
      if (__all(bad == 0)) break;
      __builtin_amdgcn_s_sleep(8);
      asm volatile("" ::: "memory");
    }
#pragma unroll
    for (int s2 = 0; s2 < 2; ++s2)
#pragma unroll
      for (int hf = 0; hf < 2; ++hf)
        sm.kv[4 * (64 * cg + 4 * c + 2 * s2 + (g & 1)) + 2 * hf + (g >> 1) + 16 * cg + c] =
            (u32x2){pk_bf16(d[s2][hf][0], d[s2][hf][1]), pk_bf16(d[s2][hf][2], d[s2][hf][3])};
    if (cg == 2) {
      const f32x4 zs = __builtin_amdgcn_mfma_f32_16x16x4f32(1.0f, __uint_as_float(za[1]) + __uint_as_float(zb[1]),
                                                            (f32x4){0, 0, 0, 0}, 0, 0, 0);
      if (l < 16) ((float*)sm.z)[l] = lo_f(pk_bf16(zs[0], 0.f));
    }
    lds_signal(cg == 2 ? CNT(C_KV2) : CNT(C_KV01));
    if (cg == 2 && j == 0) {
      lds_wait<1>(CNT(C_KV01), 2);
      if (l == 0) __builtin_amdgcn_raw_buffer_store_b32(tag, rw, 0, EPOCH_OFF, AUX_DEV);
    }
  } else {
    const int wa = w - 3;
    const u32 row0 = rowb + wa * 32;
    const u32 off_frag = (row0 + (c >> 1)) * 384 + (2 * g + (c & 1)) * 16;
    auto rk = __builtin_amdgcn_make_buffer_rsrc((void*)K, 0, TBYTES, 0x00020000);
    auto rq = __builtin_amdgcn_make_buffer_rsrc((void*)Q, 0, TBYTES, 0x00020000);
    auto ro = __builtin_amdgcn_make_buffer_rsrc((void*)O, 0, TBYTES, 0x00020000);
    u32x4 om[3][2], xf[12];
    u32 cpf = 0;
    if (wa == 4) {
      u64 pc;
      asm volatile("s_getpc_b64 %0" : "=s"(pc));
      const u32 off = (((b >> 3) & 3) * 64 + l) * 64;
      cpf = *(const GAS u32*)(pc + (off < CODELEN ? off : 0));
    }
    __builtin_amdgcn_sched_barrier(0);
    float ssk[4] = {0.f, 0.f, 0.f, 0.f};
#pragma unroll
    for (int n = 0; n < 12 + WK; ++n) {
      if (n >= WK) asm volatile("" : "+v"(xf[n - WK]));
      __builtin_amdgcn_sched_barrier(0);
      if (n >= WK - 1 && n < 12)
        asm volatile("s_cmp_gt_u32 %1, 3\n\ts_cbranch_scc1 1f\n\ts_waitcnt vmcnt(%0)\n1:" ::"i"(WK - 2), "s"(wa) : "scc", "memory");
      if (n < 12) xf[n] = __builtin_amdgcn_raw_buffer_load_b128(rk, off_frag + (n % 3) * 128, (n / 3) * 3072, AUX_NT);
      if (n == 11) __builtin_amdgcn_s_barrier();
      __builtin_amdgcn_sched_barrier(0);
      if (n >= WK) {
#pragma unroll
        for (int e = 0; e < 4; ++e) {
          xf[n - WK][e] = pk_bf16(lo_f(xf[n - WK][e]) * scale, hi_f(xf[n - WK][e]) * scale);
          ssk[(n - WK) / 3] = sumsq2(xf[n - WK][e], ssk[(n - WK) / 3]);
        }
        asm volatile("" : "+v"(xf[n - WK]), "+v"(ssk[(n - WK) / 3]));
      }
    }
    asm volatile("" ::"v"(cpf) : "memory");
#pragma unroll
    for (int i = 0; i < 3; ++i)
#pragma unroll
      for (int p = 0; p < 2; ++p) om[i][p] = sm.om[2 * i + p][l];
    {
      u32x4 phiT;
#pragma unroll
      for (int a = 0; a < 4; ++a) {
        f32x4 d0 = {0, 0, 0, 0}, d1 = {0, 0, 0, 0};
#pragma unroll
        for (int i = 0; i < 3; ++i) {
          d0 = mfma32(xf[3 * a + i], om[i][0], d0);
          d1 = mfma32(xf[3 * a + i], om[i][1], d1);
        }
        const f32x4 sq = __builtin_amdgcn_mfma_f32_16x16x4f32(ssk[a], 1.0f, (f32x4){0, 0, 0, 0}, 0, 0, 0);
        phiT[a] = phi2_bf16(d0[0] + d1[1], sq[0] + sq[1], d0[2] + d1[3], sq[2] + sq[3]);
        __builtin_amdgcn_sched_barrier(0);
        __builtin_amdgcn_s_sleep(6);
        __builtin_amdgcn_sched_barrier(0);
      }
      sm.phik[wa][l] = phiT;
    }
    lds_signal(CNT(C_PHIK));
    lds_wait<1>(CNT(C_VOUT), 3);
    if (!(__builtin_amdgcn_s_getreg(20) & 1)) __builtin_amdgcn_s_sleep(18);
    __builtin_amdgcn_sched_barrier(0);
#pragma unroll
    for (int n = 0; n < WQ; ++n) {
      xf[n] = __builtin_amdgcn_raw_buffer_load_b128(rq, off_frag + (n % 3) * 128, (n / 3) * 3072, AUX_NT);
      __builtin_amdgcn_sched_barrier(0);
    }
    const bool odd = c & 1;
    const u32 off_row = (row0 + 2 * g) * 384;
    u32x2 phq[2], kv2[4];
    f32x4 zf, ph[2];
    auto part2 = [&](int u) {
      const float dp = fmaf(ph[u][0], zf[0], ph[u][1] * zf[1]) + fmaf(ph[u][2], zf[2], ph[u][3] * zf[3]);
      const f32x4 den = __builtin_amdgcn_mfma_f32_16x16x4f32(1.0f, dp, (f32x4){1e-6f, 1e-6f, 1e-6f, 1e-6f}, 0, 0, 0);
      const float inv = __builtin_amdgcn_rcpf(den[0]);
      phq[u] = (u32x2){pk_bf16(ph[u][0] * inv, ph[u][1] * inv), pk_bf16(ph[u][2] * inv, ph[u][3] * inv)};
      f32x4 num[4];
#pragma unroll
      for (int t = 0; t < 4; ++t) num[t] = mfma16(phq[u], kv2[t], (f32x4){0, 0, 0, 0});
#pragma unroll
      for (int r = 0; r < 4; ++r) {
        const u32x2 o = {pk_bf16(num[0][r], num[1][r]), pk_bf16(num[2][r], num[3][r])};
        const u32 vo = off_row + 8 * c + 256 + (r >> 1) * 384, so = (16 * u + 8 * (r & 1)) * 384;
        if (u == 0) __builtin_amdgcn_raw_buffer_store_b64(o, ro, vo, so, AUX_PLAIN);
        else __builtin_amdgcn_raw_buffer_store_b64(o, ro, vo, so, AUX_NT);
      }
    };
#pragma unroll
    for (int u = 0; u < 2; ++u) {
      float ss[2];
      f32x4 d[2][2];
#pragma unroll
      for (int a = 0; a < 2; ++a) {
        ss[a] = 0.f;
        d[a][0] = (f32x4){0, 0, 0, 0};
        d[a][1] = (f32x4){0, 0, 0, 0};
#pragma unroll
        for (int i = 0; i < 3; ++i) {
          const int n = 6 * u + 3 * a + i;
          if (n + WQ < 12) {
            asm volatile("" : "+v"(xf[n]));
            __builtin_amdgcn_sched_barrier(0);
            xf[n + WQ] = __builtin_amdgcn_raw_buffer_load_b128(rq, off_frag + ((n + WQ) % 3) * 128,
                                                               ((n + WQ) / 3) * 3072, AUX_NT);
            __builtin_amdgcn_sched_barrier(0);
          }
          if (n == QMID) lds_add(CNT(C_QMID));
          if (n == QLATE) lds_add(CNT(C_QLATE));
          if (n == QEARLY) {
            lds_wait<0>(CNT(C_KV2), 1);
            zf = sm.z[g];
#pragma unroll
            for (int t = 0; t < 4; ++t) kv2[t] = sm.kv[4 * (128 + 4 * c + t) + g + 32 + c];
            lds_wait<0>(CNT(C_QLATE), 8);
            part2(0);
            __builtin_amdgcn_sched_barrier(0);
          }
#pragma unroll
          for (int e = 0; e < 4; ++e) ss[a] = sumsq2(xf[n][e], ss[a]);
          d[a][0] = mfma32(om[i][0], xf[n], d[a][0]);
          d[a][1] = mfma32(om[i][1], xf[n], d[a][1]);
        }
      }
      const float st = (odd ? ss[1] : ss[0]) + swap1(odd ? ss[0] : ss[1]);
      const f32x4 sq = __builtin_amdgcn_mfma_f32_16x16x4f32(1.0f, st, (f32x4){0, 0, 0, 0}, 0, 0, 0);
      const float sl = scale * 1.44269504f, hq = sq[0] * (-0.5f * scale * sl);
#pragma unroll
      for (int r = 0; r < 4; ++r) {
        const float pr = (odd ? d[1][1][r] : d[0][0][r]) + swap1(odd ? d[0][1][r] : d[1][0][r]);
        ph[u][r] = fmaf(__builtin_amdgcn_exp2f(fmaf(pr, sl, hq)), 0.25f, 1e-6f);
      }
      if (u == 0) asm volatile("" : "+v"(ph[0]));
    }
    part2(1);
    lds_wait<0>(CNT(C_KV01), 2);
    u32x2 kvf[8];
#pragma unroll
    for (int t = 0; t < 8; ++t) kvf[t] = sm.kv[4 * (8 * c + t) + g + 2 * c + (t >> 2)];
#pragma unroll
    for (int u = 0; u < 2; ++u) {
      f32x4 num[8];
#pragma unroll
      for (int t = 0; t < 8; ++t) num[t] = mfma16(phq[u], kvf[t], (f32x4){0, 0, 0, 0});
#pragma unroll
      for (int r = 0; r < 4; ++r)
        __builtin_amdgcn_raw_buffer_store_b128((u32x4){pk_bf16(num[0][r], num[1][r]), pk_bf16(num[2][r], num[3][r]),
                                                       pk_bf16(num[4][r], num[5][r]), pk_bf16(num[6][r], num[7][r])},
                                               ro, off_row + 16 * c + (r >> 1) * 384, (16 * u + 8 * (r & 1)) * 384, AUX_NT);
    }
  }
}

extern "C" void kernel_launch(void* const* d_in, const int* in_sizes, int n_in, void* d_out, int out_size,
                              void* d_ws, size_t ws_size, hipStream_t stream) {
  const float scale = 0.26864248f;
  rfa_fused<<<H * NCH, 704, 0, stream>>>((const u32*)d_in[0], (const u32*)d_in[1], (const u32*)d_in[2],
                                         (const u32*)d_in[3], (u32*)d_out, (u32*)d_ws, scale);
}
```
